```python
import torch

def _softmax(x, axis=-1):
    return torch.softmax(x, dim=axis)
def _gelu(x):
    return torch.nn.functional.gelu(x, approximate="tanh")
def _silu(x):
    return torch.nn.functional.silu(x)
def _sigmoid(x):
    return torch.sigmoid(x)
def _layernorm(x, axis=-1, eps=1e-5):
    m = x.mean(dim=axis, keepdim=True)
    v = x.var(dim=axis, keepdim=True, unbiased=False)
    return (x - m) / torch.sqrt(v + eps)
def _scatter_add(tgt, idx, src, axis=0):
    return tgt.clone().index_add_(axis, idx, src)
def _segment_sum(src, seg, n):
    out = torch.zeros((n,) + src.shape[1:], dtype=src.dtype)
    return out.index_add_(0, seg, src)
def _pad(x, pw):
    flat = tuple(v for pair in reversed(pw) for v in pair)
    return torch.nn.functional.pad(x, flat)

kernel_name = 'sequential_preLN__linear_attn_block__mixer_s_mlp__prompt'

def setup_inputs():

    _w = (lambda a: (lambda t: t.to(torch.bfloat16) if t.is_floating_point() else t)(torch.as_tensor(a)))
    def _A_setup():
        B, S, D = 1, 2048, 640
        return {
            "x": _w((lambda *s: torch.randn(*s, dtype=torch.bfloat16) * 0.05)(B, S, D)),
            "wqkv": _w((lambda *s: torch.randn(*s, dtype=torch.bfloat16) * 0.05)(D, 3 * D)),
            "wo": _w((lambda *s: torch.randn(*s, dtype=torch.bfloat16) * 0.05)(D, D)),
        }
    _A_in = _A_setup()

    def _B_setup():
        B, S, D, F = 1, 2048, 640, 1728
        return {
            "x":  _w((lambda *s: torch.randn(*s, dtype=torch.bfloat16) * 0.05)(B, S, D)),
            "w1": _w((lambda *s: torch.randn(*s, dtype=torch.bfloat16) * 3.2)(D, D) * (D ** -0.5)),
            "w2": _w((lambda *s: torch.randn(*s, dtype=torch.bfloat16) * 3.2)(D, D) * (D ** -0.5)),
            "w3": _w((lambda *s: torch.randn(*s, dtype=torch.bfloat16) * 3.2)(D, F) * (D ** -0.5)),
            "w4": _w((lambda *s: torch.randn(*s, dtype=torch.bfloat16) * 3.2)(F, D) * (F ** -0.5)),
        }
    _B_in = _B_setup()

    return {
        "A_x": _A_in["x"],
        "A_wqkv": _A_in["wqkv"],
        "A_wo": _A_in["wo"],
        "B_x": _B_in["x"],
        "B_w1": _B_in["w1"],
        "B_w2": _B_in["w2"],
        "B_w3": _B_in["w3"],
        "B_w4": _B_in["w4"],
    }

def reference(A_x, A_wqkv, A_wo, B_x, B_w1, B_w2, B_w3, B_w4):
    def _A_ref(x, wqkv, wo):
        B, S, D = x.shape
        H = 32; Dh = D // H
        qkv = (lambda x, *d: x.permute(*d))((x @ wqkv).reshape(B, S, 3, H, Dh), 2, 0, 3, 1, 4)
        q, k, v = qkv[0], qkv[1], qkv[2]
        phi = lambda t: torch.where(t > 0, t + 1, torch.exp(t)) + 1e-6
        pq, pk = phi(q), phi(k)
        kv = torch.einsum("bhsd,bhse->bhde", pk, v)
        z = torch.einsum("bhsd->bhd", pk)
        num = torch.einsum("bhqd,bhde->bhqe", pq, kv)
        den = torch.einsum("bhqd,bhd->bhq", pq, z)[..., None] + 1e-6
        o = num / den
        return (lambda x, *d: x.permute(*d))(o, 0, 2, 1, 3).reshape(B, S, D) @ wo

    def _B_ref(x, w1, w2, w3, w4):
        B, S, D = x.shape
        h = _gelu(_layernorm(x, axis=-1) @ w1)
        c = D // 4
        g0 = _pad(h[:, :, 0*c:1*c], ((0, 0), (1, 0), (0, 0)))[:, :S, :]
        g1 = _pad(h[:, :, 1*c:2*c], ((0, 0), (0, 1), (0, 0)))[:, 1:, :]
        g2 = _pad(h[:, :, 2*c:3*c], ((0, 0), (2, 0), (0, 0)))[:, :S, :]
        g3 = _pad(h[:, :, 3*c:   ], ((0, 0), (0, 2), (0, 0)))[:, 2:, :]
        hs = torch.concatenate([g0, g1, g2, g3], axis=-1) @ w2
        x1 = x + hs
        h2 = _gelu(_layernorm(x1, axis=-1) @ w3) @ w4
        return x1 + h2

    x = A_x
    h = x + _A_ref(_layernorm(x, axis=-1), A_wqkv, A_wo)
    h2 = h + _B_ref(_layernorm(h, axis=-1), B_w1, B_w2, B_w3, B_w4)
    return h2
```

```cpp
#include <hip/hip_runtime.h>
#include <hip/hip_bf16.h>
#include <stdint.h>

typedef unsigned short u16;
using bf16x8 = __attribute__((ext_vector_type(8))) short;
using f32x4  = __attribute__((ext_vector_type(4))) float;

static constexpr int S = 2048, D = 640, H = 32, DH = 20, F = 1728;

__device__ __forceinline__ float bf2f(u16 v) { return __uint_as_float(((unsigned)v) << 16); }
__device__ __forceinline__ u16 f2bf(float f) { __hip_bfloat16 b = __float2bfloat16(f); return *reinterpret_cast<u16*>(&b); }
__device__ __forceinline__ float rbf(float f) { return bf2f(f2bf(f)); }

__global__ void ln_kernel(const u16* x, u16* y) {
  int row = blockIdx.x * 4 + (threadIdx.x >> 6);
  int lane = threadIdx.x & 63;
  const u16* xr = x + (size_t)row * D;
  float v[10]; float s = 0.f;
  for (int i = 0; i < 10; i++) { v[i] = bf2f(xr[lane + 64 * i]); s += v[i]; }
  for (int o = 32; o > 0; o >>= 1) s += __shfl_xor(s, o);
  float m = s / D;
  float q = 0.f;
  for (int i = 0; i < 10; i++) { float d = v[i] - m; q += d * d; }
  for (int o = 32; o > 0; o >>= 1) q += __shfl_xor(q, o);
  float r = rsqrtf(q / D + 1e-5f);
  for (int i = 0; i < 10; i++) y[(size_t)row * D + lane + 64 * i] = f2bf((v[i] - m) * r);
}

__device__ __forceinline__ float gelu_tanh(float x) {
  float u = 0.7978845608028654f * (x + 0.044715f * x * x * x);
  return 0.5f * x * (1.f + tanhf(u));
}
template<int MODE, bool SHIFT>
__global__ __launch_bounds__(64) void gemm_naive(const u16* A, const u16* B, u16* C, int K, int N, const u16* res, const u16* res2) {
  __shared__ u16 sA[16][32];
  __shared__ u16 sB[32][16];
  const int bx = blockIdx.x * 16, by = blockIdx.y * 16;
  const int lane = threadIdx.x, row = lane & 15, quad = lane >> 4;
  f32x4 acc = {0.f, 0.f, 0.f, 0.f};
  for (int k0 = 0; k0 < K; k0 += 32) {
    for (int e = lane; e < 512; e += 64) {
      int r = e / 32, c = e % 32;
      int gr = by + r;
      if (SHIFT) {
        int g = (k0 + c) / 160;
        int sh = (g == 0) ? -1 : (g == 1) ? 1 : (g == 2) ? -2 : 2;
        gr += sh;
      }
      sA[r][c] = (gr >= 0 && gr < S) ? A[(size_t)gr * K + k0 + c] : (u16)0;
      sB[c][r] = B[(size_t)(k0 + c) * N + bx + r];
    }
    __syncthreads();
    bf16x8 a, b;
    for (int j = 0; j < 8; ++j) { a[j] = (short)sA[row][quad * 8 + j]; b[j] = (short)sB[quad * 8 + j][row]; }
    acc = __builtin_amdgcn_mfma_f32_16x16x32_bf16(a, b, acc, 0, 0, 0);
    __syncthreads();
  }
  for (int j = 0; j < 4; ++j) {
    int cr = by + quad * 4 + j, cc = bx + row;
    float v = rbf(acc[j]);
    if (MODE == 1) v = gelu_tanh(v);
    if (MODE == 2) v = v + bf2f(res[(size_t)cr * N + cc]);
    if (MODE == 3) { v = rbf(v + bf2f(res[(size_t)cr * N + cc])); v = v + bf2f(res2[(size_t)cr * N + cc]); }
    C[(size_t)cr * N + cc] = f2bf(v);
  }
}

__device__ __forceinline__ float phi(float t) { return rbf(rbf(t > 0.f ? t + 1.f : __expf(t)) + 1e-6f); }

__global__ void kv_kernel(const u16* qkv, float* kv) {
  int h = blockIdx.x;
  int t = threadIdx.x;
  if (t >= 420) return;
  int d = t / 21, e = t % 21;
  float acc = 0.f;
  for (int s = 0; s < S; s++) {
    float pk = phi(bf2f(qkv[(size_t)s * 1920 + 640 + h * 20 + d]));
    float v = (e < 20) ? bf2f(qkv[(size_t)s * 1920 + 1280 + h * 20 + e]) : 1.f;
    acc += pk * v;
  }
  kv[h * 420 + t] = rbf(acc);
}

__global__ void o_kernel(const u16* qkv, const float* kv, u16* o) {
  int idx = blockIdx.x * blockDim.x + threadIdx.x;
  if (idx >= S * D) return;
  int s = idx / D, c = idx % D, h = c / 20, e = c % 20;
  float num = 0.f, den = 0.f;
  for (int d = 0; d < 20; d++) {
    float pq = phi(bf2f(qkv[(size_t)s * 1920 + h * 20 + d]));
    num += pq * kv[h * 420 + d * 21 + e];
    den += pq * kv[h * 420 + d * 21 + 20];
  }
  o[idx] = f2bf(rbf(num) / rbf(rbf(den) + 1e-6f));
}

__global__ void add_kernel(const u16* a, const u16* b, u16* c, int n) {
  int idx = blockIdx.x * blockDim.x + threadIdx.x;
  if (idx < n) c[idx] = f2bf(bf2f(a[idx]) + bf2f(b[idx]));
}

extern "C" void kernel_launch(void* const* d_in, const int* in_sizes, int n_in,
                              void* d_out, int out_size, void* d_ws, size_t ws_size,
                              hipStream_t stream) {
  const u16* x = (const u16*)d_in[0];
  const u16* wqkv = (const u16*)d_in[1];
  const u16* wo = (const u16*)d_in[2];
  const u16* w1 = (const u16*)d_in[4];
  const u16* w2 = (const u16*)d_in[5];
  const u16* w3 = (const u16*)d_in[6];
  const u16* w4 = (const u16*)d_in[7];
  u16* out = (u16*)d_out;
  char* ws = (char*)d_ws;
  size_t off = 0;
  auto alloc = [&](size_t bytes) { void* p = ws + off; off += (bytes + 255) & ~(size_t)255; return p; };
  u16* xn = (u16*)alloc((size_t)S * D * 2);
  u16* qkv = (u16*)alloc((size_t)S * 1920 * 2);
  float* kv = (float*)alloc(32 * 420 * 4);
  u16* o = (u16*)alloc((size_t)S * D * 2);
  u16* h = (u16*)alloc((size_t)S * D * 2);
  u16* xp = (u16*)alloc((size_t)S * D * 2);
  u16* xpp = (u16*)alloc((size_t)S * D * 2);
  u16* hp = (u16*)alloc((size_t)S * D * 2);
  u16* x1 = (u16*)alloc((size_t)S * D * 2);
  u16* x1n = (u16*)alloc((size_t)S * D * 2);
  u16* Hh = (u16*)alloc((size_t)S * F * 2);
  u16* bres = (u16*)alloc((size_t)S * D * 2);

  ln_kernel<<<S / 4, 256, 0, stream>>>(x, xn);
  gemm_naive<0, false><<<dim3(1920 / 16, S / 16), 64, 0, stream>>>(xn, wqkv, qkv, D, 1920, nullptr, nullptr);
  kv_kernel<<<32, 448, 0, stream>>>(qkv, kv);
  o_kernel<<<(S * D + 255) / 256, 256, 0, stream>>>(qkv, kv, o);
  gemm_naive<2, false><<<dim3(D / 16, S / 16), 64, 0, stream>>>(o, wo, h, D, D, x, nullptr);
  ln_kernel<<<S / 4, 256, 0, stream>>>(h, xp);
  ln_kernel<<<S / 4, 256, 0, stream>>>(xp, xpp);
  gemm_naive<1, false><<<dim3(D / 16, S / 16), 64, 0, stream>>>(xpp, w1, hp, D, D, nullptr, nullptr);
  gemm_naive<2, true><<<dim3(D / 16, S / 16), 64, 0, stream>>>(hp, w2, x1, D, D, xp, nullptr);
  ln_kernel<<<S / 4, 256, 0, stream>>>(x1, x1n);
  gemm_naive<1, false><<<dim3(F / 16, S / 16), 64, 0, stream>>>(x1n, w3, Hh, D, F, nullptr, nullptr);
  gemm_naive<3, false><<<dim3(D / 16, S / 16), 64, 0, stream>>>(Hh, w4, out, F, D, x1, h);
}
```

```cpp
#include <hip/hip_runtime.h>
#include <stdint.h>
#include <type_traits>

typedef unsigned short u16;
typedef __attribute__((ext_vector_type(8))) short s16x8;
typedef __attribute__((ext_vector_type(4))) short s16x4;
typedef __attribute__((ext_vector_type(4))) float f32x4;
typedef __attribute__((ext_vector_type(2))) float f32x2;
typedef __attribute__((ext_vector_type(4))) unsigned u32x4;
typedef __attribute__((ext_vector_type(2))) unsigned u32x2;
typedef __attribute__((ext_vector_type(2))) __bf16 bf16x2;
#define GAS __attribute__((address_space(1)))
#define LAS __attribute__((address_space(3)))

static constexpr unsigned OFF_FLAGS = 0;
static constexpr unsigned OFF_TMO   = 65536;
static constexpr unsigned OFF_KVP   = 131072;
static constexpr unsigned OFF_PQF   = OFF_KVP + 16 * 32 * 420 * 4;
static constexpr unsigned OFF_OB    = OFF_PQF + 32 * 128 * 768;
static constexpr unsigned OFF_HB    = OFF_OB + 128 * 20 * 1024;
static constexpr unsigned OFF_X4    = OFF_HB + 128 * 20 * 1024;
static constexpr unsigned OFF_X1    = OFF_X4 + 128 * 20 * 1024;
static constexpr unsigned OFF_HH    = OFF_X1 + 128 * 20 * 1024;
static constexpr unsigned OFF_ST1   = OFF_HH + 128 * 56 * 1024;
static constexpr unsigned OFF_ST2   = OFF_ST1 + 2048 * 128;
static constexpr unsigned OFF_CS    = OFF_ST2 + 2048 * 128;
static constexpr unsigned OFF_WP    = OFF_CS + 20 * 2432 * 4;
static constexpr unsigned WS_BYTES  = OFF_WP + 16 * 88 * 5120;

template <int I, int N, class F>
__device__ __forceinline__ void sfor(F&& f) {
  if constexpr (I < N) { f(std::integral_constant<int, I>{}); sfor<I + 1, N>(f); }
}
template <int N> __device__ __forceinline__ void wait_vm() { asm volatile("s_waitcnt vmcnt(%0)" ::"n"(N) : "memory"); }
__device__ __forceinline__ void bar() { asm volatile("s_waitcnt lgkmcnt(0)\n\ts_barrier" ::: "memory"); }
__device__ __forceinline__ void bar_raw() { asm volatile("s_barrier" ::: "memory"); }
__device__ __forceinline__ float bflo(unsigned d) { return __uint_as_float(d << 16); }
__device__ __forceinline__ float bfhi(unsigned d) { return __uint_as_float(d & 0xffff0000u); }
__device__ __forceinline__ unsigned pk2(float lo, float hi) {
  bf16x2 v; v[0] = (__bf16)lo; v[1] = (__bf16)hi;
  return __builtin_bit_cast(unsigned, v);
}
__device__ __forceinline__ f32x4 mfma16(s16x8 a, s16x8 b, f32x4 c) { return __builtin_amdgcn_mfma_f32_16x16x32_bf16(a, b, c, 0, 0, 0); }
__device__ __forceinline__ s16x8 as_frag(u32x4 v) { return __builtin_bit_cast(s16x8, v); }
__device__ __forceinline__ void dma16(__amdgpu_buffer_rsrc_t rs, unsigned voff, unsigned soff, unsigned ldsaddr) {
  asm volatile("s_mov_b32 m0, %0\n\ts_nop 0\n\tbuffer_load_dwordx4 %1, %2, %3 offen lds"
               :: "s"(ldsaddr), "v"(voff), "s"(rs), "s"(soff) : "m0", "memory");
}
__device__ __forceinline__ unsigned lds_base(char* p) { return (unsigned)(uintptr_t)(LAS char*)p; }
__device__ __forceinline__ float clamp01(float v) { return __builtin_amdgcn_fmed3f(v, 0.f, 1.f); }
__device__ __forceinline__ float phi1(float t1) {
  const float L = 1.4426950408889634f;
  return fmaxf(t1, 1e-6f) + clamp01(__builtin_amdgcn_exp2f(__builtin_fmaf(t1, L, -1e-6f * L)));
}
__device__ __forceinline__ float xsum16(float v) {
  auto r = __builtin_amdgcn_permlane16_swap(__float_as_uint(v), __float_as_uint(v), false, false);
  return __uint_as_float(r[0]) + __uint_as_float(r[1]);
}
__device__ __forceinline__ float xsum32(float v) {
  auto r = __builtin_amdgcn_permlane32_swap(__float_as_uint(v), __float_as_uint(v), false, false);
  return __uint_as_float(r[0]) + __uint_as_float(r[1]);
}
__device__ __forceinline__ s16x8 lds_rd128(unsigned a) { return *(const LAS s16x8*)(uintptr_t)a; }
__device__ __forceinline__ s16x8 lds_rdtr(unsigned a0, unsigned a1, int off) {
  s16x4 lo = __builtin_amdgcn_ds_read_tr16_b64_v4i16((LAS s16x4*)(uintptr_t)(a0 + off));
  s16x4 hi = __builtin_amdgcn_ds_read_tr16_b64_v4i16((LAS s16x4*)(uintptr_t)(a1 + off));
  return __builtin_shufflevector(lo, hi, 0, 1, 2, 3, 4, 5, 6, 7);
}
#define AI __attribute__((always_inline))
#define SB0 __builtin_amdgcn_sched_barrier(0)
#define SGB(m, n) __builtin_amdgcn_sched_group_barrier(m, n, 0)

#define STAMP(i) do {} while (0)
static constexpr unsigned OFF_PROF = WS_BYTES;

static constexpr int K1_LDS = 5 * 32768;
__global__ __launch_bounds__(768) void k1_qkv(const u16* __restrict__ x, const u16* __restrict__ wqkv, const u16* __restrict__ wo, const u16* __restrict__ w1,
                                               const u16* __restrict__ w2, const u16* __restrict__ w3, const u16* __restrict__ w4, char* __restrict__ ws) {
  extern __shared__ __attribute__((aligned(16))) char lds[];
  const int tid = threadIdx.x, lane = tid & 63;
  const int w = __builtin_amdgcn_readfirstlane(tid >> 6);
  const int b = blockIdx.x, xcd = b & 7, idx = b >> 3;
  const int l15 = lane & 15, g = lane >> 4;
  const int rg = (xcd >> 1) * 4 + (idx >> 3), hp = (xcd & 1) * 8 + (idx & 7), hq = hp ^ 1;
  const unsigned l0 = lds_base(lds);
  const int wq = w & 3;
  float* cst = (float*)lds;
  float* sm = (float*)(lds + 1024);
  float* sq = (float*)(lds + 1536);
  const unsigned frg = l0 + 4096;
  const s16x8 ones = {0x3F80, 0x3F80, 0x3F80, 0x3F80, 0x3F80, 0x3F80, 0x3F80, 0x3F80};
  unsigned xa[2];
  { const int row = 32 * wq + l15;
#pragma unroll
    for (int t = 0; t < 2; ++t) xa[t] = l0 + (unsigned)(row * 128 + (((4 * t + g) ^ ((row >> 1) & 7)) * 16)); }

  if (w >= 8) {
    unsigned xoff[4], woff[4];
#pragma unroll
    for (int q = 0; q < 4; ++q) {
      const int p = 4 * wq + q;
      { const int row = 8 * p + (lane >> 3), lc = (lane & 7) ^ ((row >> 1) & 7);
        xoff[q] = (unsigned)(((rg * 128 + row) * 640 + lc * 8) * 2); }
      { const int row = 4 * p + (lane >> 4), lc = (lane & 15) ^ (((row & 3) << 2) | ((row >> 2) & 3));
        const int col = lc < 5 ? 640 + 40 * hp + 8 * lc : lc < 10 ? 1280 + 40 * hp + 8 * (lc - 5) : 40 * hq + 8 * (lc < 15 ? lc - 10 : 4);
        woff[q] = (unsigned)((row * 1920 + col) * 2); }
    }
    const __amdgpu_buffer_rsrc_t rsx = __builtin_amdgcn_make_buffer_rsrc((void*)x, 0, 2048u * 640u * 2u, 0x00020000);
    const __amdgpu_buffer_rsrc_t rsw = __builtin_amdgcn_make_buffer_rsrc((void*)wqkv, 0, 640u * 1920u * 2u, 0x00020000);
    const unsigned ldsb = l0 + wq * 4096;
    auto issue = [&](int c) {
      const unsigned sb = ldsb + (c % 5) * 32768;
#pragma unroll
      for (int q = 0; q < 4; ++q) {
        dma16(rsx, xoff[q], c * 128, sb + q * 1024);
        dma16(rsw, woff[q], (unsigned)c * (64 * 3840), sb + 16384 + q * 1024);
      }
    };
#pragma unroll
    for (int c = 0; c < 5; ++c) issue(c);
    f32x4 gram[2], sum[2];
    gram[0] = gram[1] = sum[0] = sum[1] = (f32x4){0, 0, 0, 0};
    wait_vm<32>();
    bar_raw();
    sfor<0, 10>([&](auto C) {
      constexpr int c = C;
      s16x8 xf[4];
#pragma unroll
      for (int t = 0; t < 2; ++t) { xf[2 * t] = lds_rd128(xa[t] + (c % 5) * 32768); xf[2 * t + 1] = lds_rd128(xa[t] + (c % 5) * 32768 + 2048); }
#pragma unroll
      for (int t = 0; t < 2; ++t)
#pragma unroll
        for (int mt = 0; mt < 2; ++mt) {
          gram[mt] = mfma16(xf[2 * t + mt], xf[2 * t + mt], gram[mt]);
          sum[mt] = mfma16(ones, xf[2 * t + mt], sum[mt]);
        }
      if constexpr (c < 9) {
        wait_vm<(c == 0 ? 24 : c <= 6 ? 16 : c == 7 ? 8 : 0)>();
        bar();
        if constexpr (c >= 1 && c + 4 <= 9) issue(c + 4);
      }
    });
#pragma unroll
    for (int mt = 0; mt < 2; ++mt) {
      const int r = l15 & 3;
      const float dg = r == 0 ? gram[mt][0] : r == 1 ? gram[mt][1] : r == 2 ? gram[mt][2] : gram[mt][3];
      const float m = sum[mt][0] * (1.f / 640.f);
      const float var = fmaxf(dg * (1.f / 640.f) - m * m, 0.f);
      const float rs = rsqrtf(var + 1e-5f);
      if (g == (l15 >> 2)) { sm[32 * wq + 16 * mt + l15] = -m * rs; sq[32 * wq + 16 * mt + l15] = rs; }
    }
    bar();
    bar_raw();
    return;
  }
  if (w == 0) ((unsigned*)(ws + OFF_FLAGS))[b * 64 + lane] = 0u;
  if (w == 1 && b == 0) ((unsigned*)(ws + OFF_TMO))[lane] = 0u;
  const int tq = (lane >> 2) & 3, tp = lane & 3;
  auto wtr_addr = [&](int cg, int u) {
    const int row = 8 * g + 4 * u + tq;
    const int f = (tq << 2) | ((2 * g + u) & 3);
    return l0 + (unsigned)(16384 + row * 256 + (((cg >> 1) ^ f) * 16) + (cg & 1) * 8);
  };
  const int cgA = 20 + (tp == 0 ? 0 : tp == 1 ? 2 : tp == 2 ? 5 : 7);
  const int cgC = 20 + ((tp & 1) ? 9 : 4);
  unsigned wc[2];
  { const int cg = w < 5 ? 4 * w + tp : w == 5 ? cgA : w == 6 ? cgA + 1 : cgC;
    wc[0] = wtr_addr(cg, 0); wc[1] = wtr_addr(cg, 1); }
  f32x4 cs = {0, 0, 0, 0};
  const int ta = w < 2 ? 0 : w < 5 ? 1 : 2, tb = w < 2 ? w : w < 5 ? w - 2 : w - 5;
  unsigned kvdst;
  { const int dcol = 16 * ta + 4 * g;
    const int hl = dcol >= 20 ? 1 : 0, d = dcol - 20 * hl;
    int e, hle;
    if (tb == 0) { e = l15 < 8 ? 20 : l15 - 8; hle = l15 < 8 ? hl : 0; }
    else { const int vc = 16 * tb - 8 + l15; hle = vc >= 20 ? 1 : 0; e = vc - 20 * hle; }
    const bool ok = dcol < 40 && hle == hl && !(tb == 0 && l15 >= 1 && l15 < 8);
    kvdst = ok ? OFF_KVP + (unsigned)((((rg * 32 + 2 * hp + hl) * 21 + e) * 20 + d) * 4) : 0x80000000u; }
  const __amdgpu_buffer_rsrc_t rsws = __builtin_amdgcn_make_buffer_rsrc((void*)ws, 0, WS_BYTES, 0x00020000);

  {
    const unsigned OOB = 0x80000000u;
    const int U = b * 2 + w;
    const bool docs = w < 2 && U < 380;
    u32x4 cv[8];
    unsigned csdst = OOB;
    if (docs) {
      const int blk = U % 19, part = U / 19;
      const bool is1 = blk < 5;
      const int c0 = 128 * (is1 ? blk : blk - 5) + 8 * l15;
      const unsigned ld = is1 ? 1280u : 3456u;
      const __amdgpu_buffer_rsrc_t rs = __builtin_amdgcn_make_buffer_rsrc((void*)(is1 ? +w1 : +w3), 0, 640u * ld, 0x00020000);
      const unsigned vo = (is1 || c0 < 1728) ? (unsigned)(32 * part + g) * ld + (unsigned)c0 * 2u : OOB;
#pragma unroll
      for (int i = 0; i < 8; ++i) cv[i] = __builtin_amdgcn_raw_buffer_load_b128(rs, vo, (unsigned)(4 * i) * ld, 0);
      csdst = OFF_CS + (unsigned)((part * 2432 + 128 * blk + 8 * l15) * 4);
    }
    const bool light = ((hp ^ (hp >> 2)) & 1) == 0;
    const int G = rg * 8 + (hp >> 3) * 4 + ((hp & 7) >> 1);
    const int jp = G >> 4, Lp = (G >> 3) & 1;
    s16x8 sel[2];
#pragma unroll
    for (int hf = 0; hf < 2; ++hf)
#pragma unroll
      for (int i = 0; i < 8; ++i) sel[hf][i] = (8 * g + i == 16 * hf + l15) ? (short)0x3F80 : (short)0;
    for (int rep = 0; rep < 2; ++rep) {
      const int t = w + 8 * rep;
      if (!light || t >= 11) break;
      const int n = 11 * (G & 7) + t;
      const int ph = n < 80 ? n / 10 : 8, s = n - 10 * ph;
      const u16* wp = ph == 0 ? +wo : ph == 1 ? +w1 : ph == 2 ? +w2 : ph < 6 ? +w3 : +w4;
      const bool is3 = ph >= 3 && ph < 6;
      const unsigned ld = is3 ? 3456u : 1280u;
      const __amdgpu_buffer_rsrc_t rs = __builtin_amdgcn_make_buffer_rsrc((void*)wp, 0, ph < 3 ? 640u * 1280u : 640u * 3456u, 0x00020000);
      const int colb = is3 ? 432 * jp + 160 * (ph - 3) : 160 * jp;
      int rowb, a1 = 8, a0 = 4, au = 16;
      if (ph == 0) {
        const int ks = 10 * Lp + s;
        au = 20;
        if (ks < 16) rowb = 40 * ks; else { rowb = 160 * (ks - 16) + 16; a1 = 80; a0 = 40; }
      } else if (ph == 8) {
        const int ks = 8 * Lp + s;
        rowb = 216 * (ks >> 1) + 160 + 32 * (ks & 1);
      } else {
        const int m = 2 * Lp + (s >= 5 ? 1 : 0), tt = s >= 5 ? s - 5 : s, A = ph >= 6 ? 216 : 80;
        rowb = (ph < 6 ? 160 * m : 432 * m + 80 * (ph - 6)) + (tt == 0 ? 0 : tt == 1 ? 32 : tt == 2 ? 64 : tt == 3 ? A : A + 32);
        if (tt == 2) { a1 = A; a0 = 8; au = 4; }
      }
      const int row = rowb + a1 * (l15 >> 3) + a0 * ((l15 >> 2) & 1) + (l15 & 3);
      const unsigned vo = (unsigned)row * ld + (unsigned)(colb + 16 * g);
      s16x8 a[2][3];
#pragma unroll
      for (int u = 0; u < 2; ++u)
#pragma unroll
        for (int cgp = 0; cgp < 3; ++cgp)
          a[u][cgp] = as_frag(__builtin_amdgcn_raw_buffer_load_b128(rs, (cgp == 2 && g >= 2) ? OOB : vo + (unsigned)(cgp * 64), (unsigned)(u * au) * ld, 0));
      const unsigned dst = OFF_WP + (unsigned)(((jp * 2 + Lp) * 88 + n) * 5120 + lane * 16);
      const f32x4 z = {0, 0, 0, 0};
#pragma unroll
      for (int T = 0; T < 5; ++T) {
        const f32x4 d0 = mfma16(a[0][T >> 1], sel[T & 1], z), d1 = mfma16(a[1][T >> 1], sel[T & 1], z);
        u32x4 o = {pk2(d0[0], d0[1]), pk2(d0[2], d0[3]), pk2(d1[0], d1[1]), pk2(d1[2], d1[3])};
        __builtin_amdgcn_raw_buffer_store_b128(o, rsws, dst + (unsigned)(T * 1024), 0, 0);
      }
    }
    if (docs) {
      float sc[8];
#pragma unroll
      for (int q = 0; q < 4; ++q) {
        float lo = 0.f, hi = 0.f;
#pragma unroll
        for (int i = 0; i < 8; ++i) { lo += bflo(cv[i][q]); hi += bfhi(cv[i][q]); }
        sc[2 * q] = xsum32(xsum16(lo)); sc[2 * q + 1] = xsum32(xsum16(hi));
      }
      if (g < 2) {
        const f32x4 o = g == 0 ? (f32x4){sc[0], sc[1], sc[2], sc[3]} : (f32x4){sc[4], sc[5], sc[6], sc[7]};
        __builtin_amdgcn_raw_buffer_store_b128(__builtin_bit_cast(u32x4, o), rsws, csdst + (unsigned)(g * 16), 0, 0);
      }
    }
  }

  if (w < 4) {
    unsigned wa[5][2];
#pragma unroll
    for (int T = 0; T < 5; ++T)
#pragma unroll
      for (int u = 0; u < 2; ++u) wa[T][u] = wtr_addr(4 * T + tp, u);
    f32x4 acc[2][5];
#pragma unroll
    for (int T = 0; T < 5; ++T) { acc[0][T] = (f32x4){0, 0, 0, 0}; acc[1][T] = (f32x4){0, 0, 0, 0}; }
    s16x8 xA[2], wA[6], xB[2], wB[6];
    auto rd = [&](int t, s16x8 (&xf)[2], s16x8 (&wf)[6]) {
      xf[0] = lds_rd128(xa[t]);
      wf[0] = lds_rdtr(wa[0][0], wa[0][1], t * 8192);
      xf[1] = lds_rd128(xa[t] + 2048);
#pragma unroll
      for (int T = 1; T < 5; ++T) wf[T] = lds_rdtr(wa[T][0], wa[T][1], t * 8192);
      wf[5] = lds_rdtr(wc[0], wc[1], t * 8192);
    };
    auto mm = [&](const s16x8 (&xf)[2], const s16x8 (&wf)[6]) {
#pragma unroll
      for (int T = 0; T < 5; ++T) {
        acc[0][T] = mfma16(xf[0], wf[T], acc[0][T]);
        acc[1][T] = mfma16(xf[1], wf[T], acc[1][T]);
      }
      cs = mfma16(ones, wf[5], cs);
    };
    auto adv = [&](unsigned d, int half) {
      xa[half] += d; wc[half] += d;
#pragma unroll
      for (int T = 0; T < 5; ++T) wa[T][half] += d;
    };
    auto half = [&](const s16x8 (&xc)[2], const s16x8 (&wcur)[6], s16x8 (&xn)[2], s16x8 (&wn)[6], int t, unsigned d, bool doadv) {
      s16x4 lo[6], hi[6];
      auto tr = [&](unsigned a) { return __builtin_amdgcn_ds_read_tr16_b64_v4i16((LAS s16x4*)(uintptr_t)(a + t * 8192)); };
      SB0; acc[0][0] = mfma16(xc[0], wcur[0], acc[0][0]); SB0; xn[0] = lds_rd128(xa[t]); lo[0] = tr(wa[0][0]);
      SB0; acc[1][0] = mfma16(xc[1], wcur[0], acc[1][0]); SB0; hi[0] = tr(wa[0][1]); xn[1] = lds_rd128(xa[t] + 2048);
      sfor<1, 5>([&](auto TT) {
        constexpr int T = TT;
        SB0; acc[0][T] = mfma16(xc[0], wcur[T], acc[0][T]); SB0; lo[T] = tr(wa[T][0]); hi[T] = tr(wa[T][1]);
        if (T == 4) { lo[5] = tr(wc[0]); hi[5] = tr(wc[1]); }
        SB0; acc[1][T] = mfma16(xc[1], wcur[T], acc[1][T]);
        if (doadv) { wa[T][0] += d; wa[T][1] += d;
                     if (T == 1) { wa[0][0] += d; wa[0][1] += d; }
                     if (T == 2) { xa[0] += d; xa[1] += d; } }
      });
      SB0; cs = mfma16(ones, wcur[5], cs);
      if (doadv) { wc[0] += d; wc[1] += d; }
      SB0;
#pragma unroll
      for (int T = 0; T < 6; ++T) wn[T] = __builtin_shufflevector(lo[T], hi[T], 0, 1, 2, 3, 4, 5, 6, 7);
    };
    bar_raw();
    rd(0, xA, wA);
    int slot = 0;
#pragma unroll 1
    for (int c = 0; c < 9; ++c) {
      const unsigned d = slot == 4 ? (unsigned)(-4 * 32768) : 32768u;
      slot = slot == 4 ? 0 : slot + 1;
      half(xA, wA, xB, wB, 1, d, true);
      bar_raw();
      half(xB, wB, xA, wA, 0, 0u, false);
    }
    half(xA, wA, xB, wB, 1, 0u, false);
    mm(xB, wB);
    if (g == 0) cst[w * 16 + l15] = cs[0];
    bar();
    f32x4 mr[2], rr[2];
#pragma unroll
    for (int mt = 0; mt < 2; ++mt) {
      mr[mt] = *(const f32x4*)(sm + 32 * wq + 16 * mt + 4 * g);
      rr[mt] = *(const f32x4*)(sq + 32 * wq + 16 * mt + 4 * g);
    }
    s16x8 pk[5];
#pragma unroll
    for (int T = 0; T < 5; ++T) {
      const float c = cst[T * 16 + l15];
      const float eps = T < 2 ? 1e-6f : T == 2 ? (l15 < 8 ? 1e-6f : 0.f) : 0.f;
      float v[8];
#pragma unroll
      for (int mt = 0; mt < 2; ++mt)
#pragma unroll
        for (int r = 0; r < 4; ++r) {
          float val = rr[mt][r] * acc[mt][T][r] + (mr[mt][r] * c + eps);
          if (T < 2) val = phi1(val);
          if (T == 2) val = l15 < 8 ? phi1(val) : val;
          v[4 * mt + r] = val;
        }
      u32x4 pp = {pk2(v[0], v[1]), pk2(v[2], v[3]), pk2(v[4], v[5]), pk2(v[6], v[7])};
      pk[T] = as_frag(pp);
      *(LAS s16x8*)(uintptr_t)(frg + (wq * 6 + (T < 3 ? T : T + 1)) * 1024 + lane * 16) = pk[T];
      if (T == 2) *(LAS s16x8*)(uintptr_t)(frg + (wq * 6 + 3) * 1024 + lane * 16) = l15 < 8 ? ones : pk[2];
    }
  } else {
    unsigned wa[3][2];
#pragma unroll
    for (int u = 0; u < 2; ++u) { wa[0][u] = wtr_addr(cgA, u); wa[1][u] = wtr_addr(cgA + 1, u); wa[2][u] = wtr_addr(cgC, u); }
    f32x4 acc[2][3];
#pragma unroll
    for (int T = 0; T < 3; ++T) { acc[0][T] = (f32x4){0, 0, 0, 0}; acc[1][T] = (f32x4){0, 0, 0, 0}; }
    s16x8 xA[2], wA[4], xB[2], wB[4];
    auto rd = [&](int t, s16x8 (&xf)[2], s16x8 (&wf)[4]) {
      xf[0] = lds_rd128(xa[t]);
      wf[0] = lds_rdtr(wa[0][0], wa[0][1], t * 8192);
      xf[1] = lds_rd128(xa[t] + 2048);
      wf[1] = lds_rdtr(wa[1][0], wa[1][1], t * 8192);
      wf[2] = lds_rdtr(wa[2][0], wa[2][1], t * 8192);
      wf[3] = lds_rdtr(wc[0], wc[1], t * 8192);
    };
    auto mm = [&](const s16x8 (&xf)[2], const s16x8 (&wf)[4]) {
#pragma unroll
      for (int T = 0; T < 3; ++T) {
        acc[0][T] = mfma16(wf[T], xf[0], acc[0][T]);
        acc[1][T] = mfma16(wf[T], xf[1], acc[1][T]);
      }
      cs = mfma16(ones, wf[3], cs);
    };
    auto adv = [&](unsigned d) {
      xa[0] += d; xa[1] += d; wc[0] += d; wc[1] += d;
#pragma unroll
      for (int T = 0; T < 3; ++T) { wa[T][0] += d; wa[T][1] += d; }
    };
    auto half = [&](const s16x8 (&xc)[2], const s16x8 (&wcur)[4], s16x8 (&xn)[2], s16x8 (&wn)[4], int t, unsigned d, bool doadv) {
      s16x4 lo[4], hi[4];
      auto tr = [&](unsigned a) { return __builtin_amdgcn_ds_read_tr16_b64_v4i16((LAS s16x4*)(uintptr_t)(a + t * 8192)); };
      SB0; acc[0][0] = mfma16(wcur[0], xc[0], acc[0][0]); SB0; xn[0] = lds_rd128(xa[t]); lo[0] = tr(wa[0][0]);
      SB0; acc[1][0] = mfma16(wcur[0], xc[1], acc[1][0]); SB0; hi[0] = tr(wa[0][1]); xn[1] = lds_rd128(xa[t] + 2048);
      SB0; acc[0][1] = mfma16(wcur[1], xc[0], acc[0][1]); SB0; lo[1] = tr(wa[1][0]); hi[1] = tr(wa[1][1]);
      SB0; acc[1][1] = mfma16(wcur[1], xc[1], acc[1][1]); SB0; lo[2] = tr(wa[2][0]); hi[2] = tr(wa[2][1]);
      SB0; acc[0][2] = mfma16(wcur[2], xc[0], acc[0][2]); SB0; lo[3] = tr(wc[0]); hi[3] = tr(wc[1]);
      SB0; acc[1][2] = mfma16(wcur[2], xc[1], acc[1][2]);
      if (doadv) { wa[0][0] += d; wa[0][1] += d; wa[1][0] += d; wa[1][1] += d; xa[0] += d; xa[1] += d; }
      SB0; cs = mfma16(ones, wcur[3], cs);
      if (doadv) { wa[2][0] += d; wa[2][1] += d; wc[0] += d; wc[1] += d; }
      SB0;
#pragma unroll
      for (int T = 0; T < 4; ++T) wn[T] = __builtin_shufflevector(lo[T], hi[T], 0, 1, 2, 3, 4, 5, 6, 7);
    };
    bar_raw();
    rd(0, xA, wA);
    int slot = 0;
#pragma unroll 1
    for (int c = 0; c < 9; ++c) {
      const unsigned d = slot == 4 ? (unsigned)(-4 * 32768) : 32768u;
      slot = slot == 4 ? 0 : slot + 1;
      half(xA, wA, xB, wB, 1, d, true);
      bar_raw();
      half(xB, wB, xA, wA, 0, 0u, false);
    }
    half(xA, wA, xB, wB, 1, 0u, false);
    mm(xB, wB);
    if (g == 0) cst[w * 16 + l15] = cs[0];
    bar();
    f32x4 c4[3];
#pragma unroll
    for (int T = 0; T < 3; ++T) c4[T] = *(const f32x4*)(cst + (5 + T) * 16 + 4 * g);
#pragma unroll
    for (int mt = 0; mt < 2; ++mt) {
      const float rs = sq[32 * wq + 16 * mt + l15], mrs = sm[32 * wq + 16 * mt + l15];
      float v[3][4];
#pragma unroll
      for (int T = 0; T < 3; ++T)
#pragma unroll
        for (int r = 0; r < 4; ++r) v[T][r] = phi1(rs * acc[mt][T][r] + (mrs * c4[T][r] + 1e-6f));
      const int MT = rg * 8 + 2 * wq + mt;
      const int h = 2 * hq + (g >> 1), ch = g & 1;
      u32x4 o = {pk2(v[0][0], v[0][1]), pk2(v[0][2], v[0][3]), pk2(v[1][0], v[1][1]), pk2(v[1][2], v[1][3])};
      *(u32x4*)(ws + OFF_PQF + (unsigned)(((h * 128 + MT) * 3 + ch) * 256 + l15 * 16)) = o;
      if (g < 2) {
        u32x4 o2 = {pk2(v[2][0], v[2][1]), pk2(v[2][2], v[2][3]), 0u, 0u};
        *(u32x4*)(ws + OFF_PQF + (unsigned)((((2 * hq + g) * 128 + MT) * 3 + 2) * 256 + l15 * 16)) = o2;
      }
    }
  }
  bar();
  {
    f32x4 kvd = {0, 0, 0, 0};
    s16x8 af[4], bf[4];
#pragma unroll
    for (int r = 0; r < 4; ++r) {
      af[r] = lds_rd128(frg + (r * 6 + ta) * 1024 + lane * 16);
      bf[r] = lds_rd128(frg + (r * 6 + 3 + tb) * 1024 + lane * 16);
    }
#pragma unroll
    for (int r = 0; r < 4; ++r) kvd = mfma16(af[r], bf[r], kvd);
    __builtin_amdgcn_raw_buffer_store_b128(__builtin_bit_cast(u32x4, kvd), rsws, kvdst, 0, 0);
  }
}

static constexpr int K2_LDS = 163840;
static constexpr unsigned L_HALF = 13 * 5120;
static constexpr unsigned L_RED = 2 * L_HALF;
static constexpr unsigned L_CTRL = L_RED + 20480;
static constexpr unsigned L_CS = L_CTRL + 256;
static constexpr unsigned L_KV = L_CS + 1280;
static constexpr unsigned C_READY = L_CTRL, C_DONE = L_CTRL + 32, C_PF = L_CTRL + 64, C_CNT = L_CTRL + 96, C_GO = L_CTRL + 100, C_CS1 = L_CTRL + 104, C_CS3 = L_CTRL + 108;
static constexpr int SPIN_LDS = 1 << 21, SPIN_GLB = 1 << 17;
static constexpr int NTILE = 88;

__device__ __forceinline__ unsigned lds_ld(unsigned a) { return *(volatile LAS unsigned*)(uintptr_t)a; }
__device__ __forceinline__ void lds_st(unsigned a, unsigned v) { *(volatile LAS unsigned*)(uintptr_t)a = v; }
__device__ __forceinline__ int lds_wait_ge(unsigned a, int v) {
  int r = 0;
  for (int it = 0; it < SPIN_LDS; ++it) {
    r = __builtin_amdgcn_readfirstlane((int)lds_ld(a));
    if (r >= v) break;
    __builtin_amdgcn_s_sleep(1);
  }
  asm volatile("" ::: "memory");
  return r;
}
__device__ __forceinline__ float gelu(float x) {
  const float c1 = -2.302208198f, c2 = -0.1029432397f;
  const float e = __builtin_amdgcn_exp2f(x * __builtin_fmaf(x * x, c2, c1));
  return x * __builtin_amdgcn_rcpf(1.f + e);
}

__global__ __launch_bounds__(768) void k2_main(const u16* __restrict__ x, const u16* __restrict__ wo, const u16* __restrict__ w1,
                                               const u16* __restrict__ w2, const u16* __restrict__ w3, const u16* __restrict__ w4,
                                               u16* __restrict__ out, char* __restrict__ ws) {
  extern __shared__ __attribute__((aligned(16))) char lds[];
  const int tid = threadIdx.x, lane = tid & 63;
  const int w = __builtin_amdgcn_readfirstlane(tid >> 6);
  const int b = blockIdx.x, xcd = b & 7, idx = b >> 3;
  const int cl = xcd * 4 + (idx >> 3), j = idx & 7;
  const int l15 = lane & 15, g = lane >> 4;
  const unsigned l0 = lds_base(lds);
  const __amdgpu_buffer_rsrc_t rsws = __builtin_amdgcn_make_buffer_rsrc((void*)ws, 0, WS_BYTES, 0x00020000);
  if (w >= 8) {
    __builtin_amdgcn_s_setprio(3);
    if (w < 10) {
      const int L = w - 8;
      const unsigned ringb = l0 + L * L_HALF, vo = (unsigned)(lane * 16);
      unsigned src = OFF_WP + (unsigned)((j * 2 + L) * NTILE * 5120), dstl = ringb;
      auto issue = [&]() AI {
        asm volatile("s_mov_b32 m0, %0\n\ts_nop 0\n\t"
                     "buffer_load_dwordx4 %1, %2, %3 offen lds\n\t"
                     "buffer_load_dwordx4 %1, %2, %3 offen offset:1024 lds\n\t"
                     "buffer_load_dwordx4 %1, %2, %3 offen offset:2048 lds\n\t"
                     "buffer_load_dwordx4 %1, %2, %3 offen offset:3072 lds"
                     :: "s"(dstl), "v"(vo), "s"(rsws), "s"(src) : "m0", "memory");
        dma16(rsws, vo, src + 4096u, dstl + 4096u);
        src += 5120u;
        dstl = dstl == ringb + 12u * 5120u ? ringb : dstl + 5120u;
      };
#pragma unroll 1
      for (int i = 0; i < 12; ++i) issue();
      bar_raw();
      int n_iss = 12, n_rdy = 0;
      const unsigned dna = l0 + C_DONE + 16 * L, rda = l0 + C_READY + 16 * L;
      u32x4 dv = *(volatile LAS u32x4*)(uintptr_t)dna;
#pragma unroll 1
      for (int guard = 0; guard < SPIN_LDS; ++guard) {
        unsigned ib;
        asm volatile("s_getreg_b32 %0, hwreg(HW_REG_IB_STS)" : "=s"(ib) :: "memory");
        const int vm = (int)((ib & 15u) | ((ib >> 18) & 48u));
        const int landed = n_iss - (((vm + 4) * 205) >> 10);
        if (landed != n_rdy) {
          n_rdy = landed; lds_st(rda, (unsigned)landed);
          if (landed == NTILE) break;
        }
        const int mind = __builtin_amdgcn_readfirstlane((int)min(min(dv[0], dv[1]), min(dv[2], dv[3])));
        dv = *(volatile LAS u32x4*)(uintptr_t)dna;
        int lim = min(min(mind + 13, NTILE), n_rdy + 12);
        if (n_iss < lim) {
          do {
            issue(); ++n_iss;
          } while (n_iss < lim);
        } else __builtin_amdgcn_s_sleep(1);
      }
      wait_vm<0>();
    } else if (w == 10) {
      u32x4 cv[20];
      { const unsigned oa = lane < 20 ? OFF_CS + (unsigned)((80 * j + 4 * lane) * 4) : 0x80000000u;
#pragma unroll
        for (int i = 0; i < 20; ++i) cv[i] = __builtin_amdgcn_raw_buffer_load_b128(rsws, oa, i * 2432 * 4, 0); }
      if (lane < 32) lds_st(l0 + L_CTRL + lane * 4, 0u);
      bar();
      { f32x4 a = {0, 0, 0, 0};
#pragma unroll
        for (int i = 0; i < 20; ++i) a += __builtin_bit_cast(f32x4, cv[i]);
        if (lane < 20) *(LAS f32x4*)(uintptr_t)(l0 + L_CS + lane * 16) = a;
        asm volatile("" ::: "memory");
        lds_st(l0 + C_CS1, 1u); }
      GAS unsigned* myflag = (GAS unsigned*)(ws + OFF_FLAGS + b * 256);
      for (int e = 1; e <= 7; ++e) {
        lds_wait_ge(l0 + C_CNT, 8 * e);
        if (lane == 0) __hip_atomic_store(myflag, (unsigned)e, __ATOMIC_RELAXED, __HIP_MEMORY_SCOPE_AGENT);
      }
    } else {
      { u32x4 cv[20];
        const unsigned oc = lane < 54 ? OFF_CS + (unsigned)((640 + 216 * j + 4 * lane) * 4) : 0x80000000u;
#pragma unroll
        for (int i = 0; i < 20; ++i) cv[i] = __builtin_amdgcn_raw_buffer_load_b128(rsws, oc, i * 2432 * 4, 0);
        bar();
        f32x4 c = {0, 0, 0, 0};
#pragma unroll
        for (int i = 0; i < 20; ++i) c += __builtin_bit_cast(f32x4, cv[i]);
        if (lane < 60) *(LAS f32x4*)(uintptr_t)(l0 + L_CS + 320 + lane * 16) = c;
        asm volatile("" ::: "memory");
        lds_st(l0 + C_CS3, 1u); }
      int bp;
      if (lane < 8) bp = xcd + 8 * (((cl & 3) << 3) + lane);
      else if (lane < 12) { const int c2 = cl - 1, jj = (lane & 1) + ((lane & 2) << 1); bp = (c2 >> 2) + 8 * (((c2 & 3) << 3) + jj); }
      else if (lane < 16) { const int c2 = cl + 1, jj = 2 + (lane & 1) + ((lane & 2) << 1); bp = (c2 >> 2) + 8 * (((c2 & 3) << 3) + jj); }
      else bp = xcd + 8 * ((lane & 15) + 16 * ((cl & 3) >> 1));
      GAS unsigned* fp = (GAS unsigned*)(ws + OFF_FLAGS) + (bp & 255) * 64;
      bool dead = false;
      for (int e = 1; e <= 7 && !dead; ++e) {
        bool act;
        if (e == 1) act = lane >= 16 && lane < 32;
        else if (e == 3) act = lane < 8 || (lane < 12 && cl > 0) || (lane >= 12 && lane < 16 && cl < 31);
        else act = lane < 8;
        lds_wait_ge(l0 + C_CNT, 8 * e);
        int it = 0;
        for (;; ++it) {
          unsigned f = 0xffffu;
          if (act) f = __hip_atomic_load(fp, __ATOMIC_RELAXED, __HIP_MEMORY_SCOPE_AGENT);
          if (__all(f >= (unsigned)e)) break;
          if (it >= SPIN_GLB) { dead = true; break; }
          __builtin_amdgcn_s_sleep(1);
        }
        if (dead) { if (lane == 0) __hip_atomic_store((GAS unsigned*)(ws + OFF_TMO), (unsigned)(e + 100 * b), __ATOMIC_RELAXED, __HIP_MEMORY_SCOPE_AGENT); lds_st(l0 + C_GO, 1000u); }
        else lds_st(l0 + C_GO, (unsigned)e);
      }
    }
    return;
  }

  const int wm = w & 3, wk = w >> 2;
  const int tq = (lane >> 2) & 3, tp = lane & 3;
  const int MT = 4 * cl + wm, row = 16 * MT + l15;
  const unsigned OOB = 0x80000000u;
  u32x2 xr[3];
  { const __amdgpu_buffer_rsrc_t rsx = __builtin_amdgcn_make_buffer_rsrc((void*)x, 0, 2048u * 640u * 2u, 0x00020000);
    const unsigned o = (unsigned)((row * 640 + 80 * j + 4 * g) * 2);
    xr[0] = __builtin_amdgcn_raw_buffer_load_b64(rsx, o + 64 * wk, 0, 0);
    xr[1] = __builtin_amdgcn_raw_buffer_load_b64(rsx, o + 64 * wk + 32, 0, 0);
    xr[2] = __builtin_amdgcn_raw_buffer_load_b64(rsx, o + 128, 0, 0); }
  {
    const int hp = idx & 15, rh = idx >> 4, MT1 = 16 * xcd + 8 * rh + w;
    s16x8 pq[2];
#pragma unroll
    for (int a = 0; a < 2; ++a)
      pq[a] = as_frag(__builtin_amdgcn_raw_buffer_load_b128(rsws, g < 3 ? OFF_PQF + (unsigned)(((((2 * hp + a) * 128 + MT1) * 3 + g) * 256 + l15 * 16)) : OOB, 0, 0));
    if (w < 4) {
      const unsigned o = tid < 210 ? OFF_KVP + (unsigned)(2 * hp * 1680 + tid * 16) : OOB;
      f32x4 s4 = {0, 0, 0, 0};
      f32x4 t[16];
#pragma unroll
      for (int rg = 0; rg < 16; ++rg) t[rg] = __builtin_bit_cast(f32x4, __builtin_amdgcn_raw_buffer_load_b128(rsws, o, rg * 32 * 1680, 0));
#pragma unroll
      for (int rg = 0; rg < 16; ++rg) s4 += t[rg];
      const int a = tid >= 105 ? 1 : 0, r = tid - 105 * a, e = r / 5, d4 = r - 5 * e;
      u32x2 pk = {pk2(s4[0], s4[1]), pk2(s4[2], s4[3])};
      if (tid < 210) *(LAS u32x2*)(uintptr_t)(l0 + L_KV + (a * 21 + e) * 80 + d4 * 8) = pk;
    } else if (tid < 256 + 126) {
      const int i = tid - 256, r = i / 3, part = i - 3 * r;
      *(LAS u32x2*)(uintptr_t)(l0 + L_KV + r * 80 + 40 + part * 8) = (u32x2){0u, 0u};
    }
    bar();
    float o0[2][4], o1[2][4];
#pragma unroll
    for (int a = 0; a < 2; ++a) {
      const s16x8 f0 = lds_rd128(l0 + L_KV + (a * 21 + l15) * 80 + g * 16);
      const s16x8 f1 = lds_rd128(l0 + L_KV + (a * 21 + 16 + (l15 & 3)) * 80 + g * 16);
      const s16x8 f2 = lds_rd128(l0 + L_KV + (a * 21 + 20) * 80 + g * 16);
      const f32x4 z = {0, 0, 0, 0};
      const f32x4 n0 = mfma16(f0, pq[a], z), n1 = mfma16(f1, pq[a], z), dn = mfma16(f2, pq[a], z);
      const float inv = __builtin_amdgcn_rcpf(dn[0] + 1e-6f);
#pragma unroll
      for (int r = 0; r < 4; ++r) { o0[a][r] = n0[r] * inv; o1[a][r] = n1[r] * inv; }
    }
    u32x4 vx = {pk2(o0[0][0], o0[0][1]), pk2(o0[0][2], o0[0][3]), pk2(o0[1][0], o0[1][1]), pk2(o0[1][2], o0[1][3])};
    u32x4 vy = {pk2(o1[0][0], o1[0][1]), pk2(o1[0][2], o1[0][3]), pk2(o1[1][0], o1[1][1]), pk2(o1[1][2], o1[1][3])};
    __builtin_amdgcn_raw_buffer_store_b128(vx, rsws, OFF_OB + (unsigned)((MT1 * 20 + hp) * 1024 + lane * 16), 0, 16);
    __builtin_amdgcn_raw_buffer_store_b128(vy, rsws, g == 0 ? OFF_OB + (unsigned)((MT1 * 20 + 16 + (hp >> 2)) * 1024 + ((hp & 3) * 16 + l15) * 16) : OOB, 0, 16);
  }
  auto publish = [&]() AI {
    wait_vm<0>();
    if (lane == 0) __hip_atomic_fetch_add((LAS unsigned*)(uintptr_t)(l0 + C_CNT), 1u, __ATOMIC_RELAXED, __HIP_MEMORY_SCOPE_WORKGROUP);
  };
  publish();
  const unsigned wa0 = l0 + wk * L_HALF + (unsigned)(lane * 16);
  int slot = 0, ntile = 0, rdy = 0, redep = 0;
  f32x4 acc[5];
  s16x8 xf[10];
  auto wait_tile = [&](int n) AI { if (n >= rdy) rdy = lds_wait_ge(l0 + C_READY + 16 * wk, n + 1); };
  const unsigned dn_addr = l0 + C_DONE + 16 * wk + 4 * wm;
  auto gemm = [&](int nst) AI {
    s16x8 wf[2][5];
    wait_tile(ntile);
    { const unsigned a = wa0 + (unsigned)slot * 5120u;
#pragma unroll
      for (int T = 0; T < 5; ++T) wf[0][T] = lds_rd128(a + 1024 * T);
      lds_st(dn_addr, (unsigned)(ntile + 1));
      slot = slot == 12 ? 0 : slot + 1; }
    sfor<0, 10>([&](auto S) AI {
      constexpr int s = S, c = s & 1, nx = c ^ 1;
      if (s < 8 || nst == 10) {
        const bool more = s < 7 || (s < 9 && nst == 10);
        unsigned a = 0;
        if (more) { wait_tile(ntile + s + 1); a = wa0 + (unsigned)slot * 5120u; }
#pragma unroll
        for (int T = 0; T < 5; ++T) {
          SB0;
          acc[T] = mfma16(wf[c][T], xf[s], acc[T]);
          SB0;
          if (more) wf[nx][T] = lds_rd128(a + 1024 * T);
        }
        SB0;
        if (more) { lds_st(dn_addr, (unsigned)(ntile + s + 2)); slot = slot == 12 ? 0 : slot + 1; }
      }
    });
    ntile += nst;
  };
  auto zero_acc = [&]() AI {
#pragma unroll
    for (int T = 0; T < 5; ++T) acc[T] = (f32x4){0, 0, 0, 0};
  };
  auto load_x = [&](unsigned base, int n) AI {
    const unsigned vo = base + (unsigned)(lane * 16);
#pragma unroll
    for (int s = 0; s < 10; ++s)
      if (s < 8 || n == 10) xf[s] = as_frag(__builtin_amdgcn_raw_buffer_load_b128(rsws, vo, s * 1024, 16));
  };
  f32x4 vA, vB, vC;
  const unsigned redb = l0 + L_RED + (unsigned)(wm * 5120 + lane * 16);
  const unsigned pf_me = l0 + C_PF + 4 * w, pf_pa = l0 + C_PF + 4 * (w ^ 4);
  auto reduce = [&]() AI {
    lds_wait_ge(pf_pa, 2 * redep);
    ++redep;
    if (wk == 0) {
      *(LAS f32x4*)(uintptr_t)(redb + 2 * 1024) = acc[2];
      *(LAS f32x4*)(uintptr_t)(redb + 3 * 1024) = acc[3];
    } else {
      *(LAS f32x4*)(uintptr_t)(redb + 0 * 1024) = acc[0];
      *(LAS f32x4*)(uintptr_t)(redb + 1 * 1024) = acc[1];
      *(LAS f32x4*)(uintptr_t)(redb + 4 * 1024) = acc[4];
    }
    asm volatile("" ::: "memory");
    lds_st(pf_me, (unsigned)(2 * redep - 1));
    lds_wait_ge(pf_pa, 2 * redep - 1);
    if (wk == 0) {
      vA = acc[0] + *(const LAS f32x4*)(uintptr_t)(redb + 0 * 1024);
      vB = acc[1] + *(const LAS f32x4*)(uintptr_t)(redb + 1 * 1024);
      vC = acc[4] + *(const LAS f32x4*)(uintptr_t)(redb + 4 * 1024);
    } else {
      vA = acc[2] + *(const LAS f32x4*)(uintptr_t)(redb + 2 * 1024);
      vB = acc[3] + *(const LAS f32x4*)(uintptr_t)(redb + 3 * 1024);
      vC = (f32x4){0, 0, 0, 0};
    }
    asm volatile("" ::: "memory");
    lds_st(pf_me, (unsigned)(2 * redep));
  };
  const int ksP = 5 * (j >> 1) + 3 * (j & 1) + wk, ks4 = 5 * (j >> 1) + 2, g4 = 2 * (j & 1) + (g >> 1);
  auto st_frag = [&](unsigned bufoff, int nks, int ksoff, int r, u32x2 a, u32x2 bb, u32x2 c, bool with4) AI {
    const bool ok = r >= 0 && r < 2048;
    const unsigned mt = (unsigned)(r >> 4), rl = (unsigned)(r & 15);
    const unsigned base = bufoff + (mt * nks + ksoff) * 1024u + rl * 16u;
    u32x4 p = {a[0], a[1], bb[0], bb[1]};
    __builtin_amdgcn_raw_buffer_store_b128(p, rsws, ok ? base + (unsigned)(ksP * 1024 + g * 256) : OOB, 0, 16);
    if (with4) {
      auto s0 = __builtin_amdgcn_permlane16_swap(c[0], c[0], false, false);
      auto s1 = __builtin_amdgcn_permlane16_swap(c[1], c[1], false, false);
      u32x4 p4 = {c[0], c[1], s0[1], s1[1]};
      __builtin_amdgcn_raw_buffer_store_b128(p4, rsws, (ok && !(g & 1)) ? base + (unsigned)(ks4 * 1024 + g4 * 256) : OOB, 0, 16);
    }
  };
  auto pack4 = [&](const float (&v)[4]) { return (u32x2){pk2(v[0], v[1]), pk2(v[2], v[3])}; };
  auto st_stats = [&](unsigned off, float s1, float s2) AI {
    s1 = xsum32(xsum16(s1)); s2 = xsum32(xsum16(s2));
    u32x2 v = {__float_as_uint(s1), __float_as_uint(s2)};
    __builtin_amdgcn_raw_buffer_store_b64(v, rsws, g == 0 ? off + (unsigned)(row * 128 + (2 * j + wk) * 8) : OOB, 0, 16);
  };
  f32x4 st0, st1;
  auto ld_stats = [&](unsigned off) AI {
    st0 = __builtin_bit_cast(f32x4, __builtin_amdgcn_raw_buffer_load_b128(rsws, off + (unsigned)(row * 128 + g * 32), 0, 16));
    st1 = __builtin_bit_cast(f32x4, __builtin_amdgcn_raw_buffer_load_b128(rsws, off + (unsigned)(row * 128 + g * 32 + 16), 0, 16));
  };
  auto fin_stats = [&](float& mean, float& var) AI {
    const float s1 = xsum32(xsum16((st0[0] + st0[2]) + (st1[0] + st1[2])));
    const float s2 = xsum32(xsum16((st0[1] + st0[3]) + (st1[1] + st1[3])));
    mean = s1 * (1.f / 640.f);
    var = fmaxf(s2 * (1.f / 640.f) - mean * mean, 0.f);
  };
  u32x2 hres[3], x1res[3];
  float m1 = 0.f, r1 = 0.f;
  const unsigned csA = l0 + L_CS + (unsigned)((32 * wk + 4 * g) * 4), csC = l0 + L_CS + (unsigned)((64 + 4 * g) * 4);
  auto act = [&](const f32x4& v, unsigned csaddr, float sc, float msc, float (&o)[4]) AI {
    const f32x4 c = *(const LAS f32x4*)(uintptr_t)csaddr;
#pragma unroll
    for (int r = 0; r < 4; ++r) o[r] = gelu(__builtin_fmaf(sc, v[r], msc * c[r]));
  };

#pragma unroll 1
  for (int p = 0; p < 3; ++p) {
    lds_wait_ge(l0 + C_GO, p + 1);
    load_x((p == 0 ? OFF_OB : p == 1 ? OFF_HB : OFF_X4) + (unsigned)((MT * 20 + 10 * wk) * 1024), 10);
    if (p == 1) ld_stats(OFF_ST1);
    zero_acc();
    gemm(10);
    reduce();
    if (p == 1) {
      float var;
      lds_wait_ge(l0 + C_CS1, 1);
      fin_stats(m1, var);
      r1 = rsqrtf(var + 1e-5f);
      const float r2 = rsqrtf(var * r1 * r1 + 1e-5f);
      const float sc = r1 * r2, msc = -sc * m1;
      float a[4], bb[4], c[4];
      act(vA, csA, sc, msc, a);
      act(vB, csA + 64, sc, msc, bb);
      act(vC, csC, sc, msc, c);
      const int dl = j < 2 ? 1 : j < 4 ? -1 : j < 6 ? 2 : -2;
      st_frag(OFF_X4, 20, 0, row + dl, pack4(a), pack4(bb), pack4(c), wk == 0);
      if ((cl == 0 && wm == 0 && dl > 0) || (cl == 31 && wm == 3 && dl < 0)) {
        const bool z = dl > 0 ? l15 < dl : l15 >= 16 + dl;
        const u32x2 zz = {0u, 0u};
        st_frag(OFF_X4, 20, 0, z ? row : -1, zz, zz, zz, wk == 0);
      }
    } else {
      float a[4], bb[4], c[4];
      if (p == 0) {
#pragma unroll
        for (int r = 0; r < 4; ++r) {
          a[r] = vA[r] + ((r & 1) ? bfhi(xr[0][r >> 1]) : bflo(xr[0][r >> 1]));
          bb[r] = vB[r] + ((r & 1) ? bfhi(xr[1][r >> 1]) : bflo(xr[1][r >> 1]));
          c[r] = vC[r] + ((r & 1) ? bfhi(xr[2][r >> 1]) : bflo(xr[2][r >> 1]));
        }
      } else {
        const float mr = -m1 * r1;
#pragma unroll
        for (int r = 0; r < 4; ++r) {
          a[r] = vA[r] + __builtin_fmaf((r & 1) ? bfhi(hres[0][r >> 1]) : bflo(hres[0][r >> 1]), r1, mr);
          bb[r] = vB[r] + __builtin_fmaf((r & 1) ? bfhi(hres[1][r >> 1]) : bflo(hres[1][r >> 1]), r1, mr);
          c[r] = vC[r] + __builtin_fmaf((r & 1) ? bfhi(hres[2][r >> 1]) : bflo(hres[2][r >> 1]), r1, mr);
        }
      }
      float s1 = 0.f, s2 = 0.f;
#pragma unroll
      for (int r = 0; r < 4; ++r) { s1 += a[r] + bb[r]; s2 += a[r] * a[r] + bb[r] * bb[r]; }
      if (wk == 0) {
#pragma unroll
        for (int r = 0; r < 4; ++r) { s1 += c[r]; s2 += c[r] * c[r]; }
      }
      const u32x2 pa = pack4(a), pb = pack4(bb), pc = pack4(c);
      if (p == 0) { hres[0] = pa; hres[1] = pb; hres[2] = pc; } else { x1res[0] = pa; x1res[1] = pb; x1res[2] = pc; }
      st_frag(p == 0 ? OFF_HB : OFF_X1, 20, 0, row, pa, pb, pc, wk == 0);
      st_stats(p == 0 ? OFF_ST1 : OFF_ST2, s1, s2);
    }
    publish();
  }
  {
    lds_wait_ge(l0 + C_GO, 4);
    load_x(OFF_X1 + (unsigned)((MT * 20 + 10 * wk) * 1024), 10);
    ld_stats(OFF_ST2);
    float sc = 0.f, msc = 0.f;
#pragma unroll 1
    for (int q = 0; q < 3; ++q) {
      zero_acc();
      gemm(10);
      reduce();
      if (q == 0) { float m3, var; lds_wait_ge(l0 + C_CS3, 1); fin_stats(m3, var); sc = rsqrtf(var + 1e-5f); msc = -sc * m3; }
      float a[4], bb[4], c[4];
      const unsigned co = (unsigned)(320 + 320 * q);
      act(vA, csA + co, sc, msc, a);
      act(vB, csA + co + 64, sc, msc, bb);
      act(vC, csC + co, sc, msc, c);
      if (q < 2) st_frag(OFF_HH, 56, 20 * q, row, pack4(a), pack4(bb), pack4(c), wk == 0);
      else {
        u32x2 pb = pack4(bb);
        if (wk == 1 && g >= 2) pb = (u32x2){0u, 0u};
        u32x4 pp = {pack4(a)[0], pack4(a)[1], pb[0], pb[1]};
        __builtin_amdgcn_raw_buffer_store_b128(pp, rsws, OFF_HH + (unsigned)((MT * 56 + 40 + 2 * j + wk) * 1024 + lane * 16), 0, 16);
      }
      publish();
    }
  }
  zero_acc();
#pragma unroll 1
  for (int q = 0; q < 3; ++q) {
    lds_wait_ge(l0 + C_GO, 5 + q);
    load_x(OFF_HH + (unsigned)((MT * 56 + 20 * q + (q < 2 ? 10 : 8) * wk) * 1024), q < 2 ? 10 : 8);
    gemm(q < 2 ? 10 : 8);
  }
  reduce();
  {
    const __amdgpu_buffer_rsrc_t rso = __builtin_amdgcn_make_buffer_rsrc((void*)out, 0, 2048u * 640u * 2u, 0x00020000);
    const unsigned o = (unsigned)((row * 640 + 80 * j + 4 * g) * 2);
    auto fin = [&](const f32x4& v, u32x2 h, u32x2 x1) AI {
      float t[4];
#pragma unroll
      for (int r = 0; r < 4; ++r)
        t[r] = ((r & 1) ? bfhi(h[r >> 1]) : bflo(h[r >> 1])) + (((r & 1) ? bfhi(x1[r >> 1]) : bflo(x1[r >> 1])) + v[r]);
      return pack4(t);
    };
    __builtin_amdgcn_raw_buffer_store_b64(fin(vA, hres[0], x1res[0]), rso, o + 64 * wk, 0, 0);
    __builtin_amdgcn_raw_buffer_store_b64(fin(vB, hres[1], x1res[1]), rso, o + 64 * wk + 32, 0, 0);
    if (wk == 0) __builtin_amdgcn_raw_buffer_store_b64(fin(vC, hres[2], x1res[2]), rso, o + 128, 0, 0);
  }
}


extern "C" void kernel_launch(void* const* d_in, const int* in_sizes, int n_in, void* d_out, int out_size, void* d_ws, size_t ws_size, hipStream_t stream) {
  (void)hipFuncSetAttribute((const void*)k1_qkv, hipFuncAttributeMaxDynamicSharedMemorySize, K1_LDS);
  (void)hipFuncSetAttribute((const void*)k2_main, hipFuncAttributeMaxDynamicSharedMemorySize, K2_LDS);
  k1_qkv<<<256, 768, K1_LDS, stream>>>((const u16*)d_in[0], (const u16*)d_in[1], (const u16*)d_in[2], (const u16*)d_in[4], (const u16*)d_in[5],
                                       (const u16*)d_in[6], (const u16*)d_in[7], (char*)d_ws);
  k2_main<<<256, 768, K2_LDS, stream>>>((const u16*)d_in[0], (const u16*)d_in[2], (const u16*)d_in[4], (const u16*)d_in[5],
                                        (const u16*)d_in[6], (const u16*)d_in[7], (u16*)d_out, (char*)d_ws);
}
```

```cpp
#define SIDE_C0 7
#define SIDE_N 6
#define SIDE_S0 9
#define SIDE_S1 9
#define SIDE_PRE 1
#define SIDE_P1 0
#define KCP 2
#define KCP_W 7
#define PF_C0 3
#define PF_LA 4
#define PRE 2
#define HPF 1
#define HPF_C 10
#include <hip/hip_runtime.h>
#include <stdint.h>
#include <type_traits>

typedef unsigned short u16;
typedef __attribute__((ext_vector_type(8))) short s16x8;
typedef __attribute__((ext_vector_type(4))) short s16x4;
typedef __attribute__((ext_vector_type(4))) float f32x4;
typedef __attribute__((ext_vector_type(2))) float f32x2;
typedef __attribute__((ext_vector_type(4))) unsigned u32x4;
typedef __attribute__((ext_vector_type(2))) unsigned u32x2;
typedef __attribute__((ext_vector_type(2))) __bf16 bf16x2;
#define GAS __attribute__((address_space(1)))
#define LAS __attribute__((address_space(3)))

static constexpr unsigned OFF_KVP   = 0;
static constexpr unsigned OFF_PQF   = OFF_KVP + 16 * 32 * 420 * 4;
static constexpr unsigned OFF_CS    = OFF_PQF + 32 * 128 * 768;
static constexpr unsigned OFF_WP    = OFF_CS + 20 * 2432 * 4;
static constexpr unsigned OFF_HH    = OFF_WP + 32 * 215 * 1024;
static constexpr unsigned OFF_X4    = OFF_HH + 128 * 56 * 1024 + 20480;
static constexpr unsigned OFF_OB    = OFF_X4 + 129 * 20480;
static constexpr unsigned OFF_HB    = OFF_OB + 128 * 20480;
static constexpr unsigned OFF_X1    = OFF_HB + 128 * 20480;
static constexpr unsigned OFF_FLG   = OFF_X1 + 128 * 20480;
static constexpr unsigned OFF_FILL_END = OFF_FLG + 128 * 3 * 128;
static constexpr unsigned WS_BYTES  = OFF_FILL_END;

template <int I, int N, class F>
__device__ __forceinline__ void sfor(F&& f) {
  if constexpr (I < N) { f(std::integral_constant<int, I>{}); sfor<I + 1, N>(f); }
}
template <int N> __device__ __forceinline__ void wait_vm() { asm volatile("s_waitcnt vmcnt(%0)" ::"n"(N) : "memory"); }
__device__ __forceinline__ void bar() { asm volatile("s_waitcnt lgkmcnt(0)\n\ts_barrier" ::: "memory"); }
__device__ __forceinline__ void bar_raw() { asm volatile("s_barrier" ::: "memory"); }
__device__ __forceinline__ float bflo(unsigned d) { return __uint_as_float(d << 16); }
__device__ __forceinline__ float bfhi(unsigned d) { return __uint_as_float(d & 0xffff0000u); }
__device__ __forceinline__ unsigned pk2(float lo, float hi) {
  bf16x2 v; v[0] = (__bf16)lo; v[1] = (__bf16)hi;
  return __builtin_bit_cast(unsigned, v);
}
__device__ __forceinline__ f32x4 mfma16(s16x8 a, s16x8 b, f32x4 c) { return __builtin_amdgcn_mfma_f32_16x16x32_bf16(a, b, c, 0, 0, 0); }
__device__ __forceinline__ s16x8 as_frag(u32x4 v) { return __builtin_bit_cast(s16x8, v); }
template <class RS>
__device__ __forceinline__ void dma16(RS rs, unsigned voff, unsigned soff, unsigned ldsaddr) {
  asm volatile("s_mov_b32 m0, %0\n\ts_nop 0\n\tbuffer_load_dwordx4 %1, %2, %3 offen lds"
               :: "s"(ldsaddr), "v"(voff), "s"(rs), "s"(soff) : "m0", "memory");
}
__device__ __forceinline__ unsigned lds_base(char* p) { return (unsigned)(uintptr_t)(LAS char*)p; }
__device__ __forceinline__ float clamp01(float v) { return __builtin_amdgcn_fmed3f(v, 0.f, 1.f); }
__device__ __forceinline__ float phi1(float t1) {
  const float L = 1.4426950408889634f;
  return fmaxf(t1, 1e-6f) + clamp01(__builtin_amdgcn_exp2f(__builtin_fmaf(t1, L, -1e-6f * L)));
}
__device__ __forceinline__ float xsum16(float v) {
  auto r = __builtin_amdgcn_permlane16_swap(__float_as_uint(v), __float_as_uint(v), false, false);
  return __uint_as_float(r[0]) + __uint_as_float(r[1]);
}
__device__ __forceinline__ float xsum32(float v) {
  auto r = __builtin_amdgcn_permlane32_swap(__float_as_uint(v), __float_as_uint(v), false, false);
  return __uint_as_float(r[0]) + __uint_as_float(r[1]);
}
__device__ __forceinline__ s16x8 lds_rd128(unsigned a) { return *(const LAS s16x8*)(uintptr_t)a; }
__device__ __forceinline__ s16x8 lds_rdtr(unsigned a0, unsigned a1, int off) {
  s16x4 lo = __builtin_amdgcn_ds_read_tr16_b64_v4i16((LAS s16x4*)(uintptr_t)(a0 + off));
  s16x4 hi = __builtin_amdgcn_ds_read_tr16_b64_v4i16((LAS s16x4*)(uintptr_t)(a1 + off));
  return __builtin_shufflevector(lo, hi, 0, 1, 2, 3, 4, 5, 6, 7);
}
#define AI __attribute__((always_inline))
#define SB0 __builtin_amdgcn_sched_barrier(0)
#define SGB(m, n) __builtin_amdgcn_sched_group_barrier(m, n, 0)

#define STAMP(i) do {} while (0)
static constexpr unsigned OFF_PROF = WS_BYTES;

static constexpr int K1_LDS = 5 * 32768, K1_THREADS = 768;
#define FILL_AUX 0
#define WP_AUX 0
#define TOUCH 0
#define TOUCH_W 5
#define PRIO_L 0
#define PRIO_C 0
#define HPF_STEP 128
#define KCP_TAIL 0
#define KCP_TOFF 16384
#define SIDE_P0 (SIDE_C0 - 1)
#define PF_AT 3
constexpr int k1_side_first(int k) { const int v = (k - SIDE_C0) * SIDE_N; return v < 0 ? 0 : v > 12 ? 12 : v; }
constexpr int k1_after(int c1) {
  int cnt = 0; bool found = false;
  for (int c = 0; c < PRE; ++c) { if (found) cnt += 8; if (c == c1) found = true; }
  for (int k = 0; k < c1; ++k) {
    for (int c = (k == 0 ? PRE : k + 3); c <= k + 3 && c <= 9; ++c) { if (found) cnt += 8; if (c == c1) found = true; }
    if (found) cnt += k1_side_first(k + 1) - k1_side_first(k) + (k == SIDE_S0 ? 7 : 0) + (k == SIDE_S1 ? 7 : 0);
  }
  return cnt;
}
#define PF_ON 1
#define PF_PRE 0
#define FILL_C0 0
__global__ __launch_bounds__(768) void k1_qkv(const u16* __restrict__ x, const u16* __restrict__ wqkv, const u16* __restrict__ wo, const u16* __restrict__ w1,
                                               const u16* __restrict__ w2, const u16* __restrict__ w3, const u16* __restrict__ w4, char* __restrict__ ws) {
  extern __shared__ __attribute__((aligned(16))) char lds[];
  const int tid = threadIdx.x, lane = tid & 63;
  const int w = __builtin_amdgcn_readfirstlane(tid >> 6);
  const int b = blockIdx.x, xcd = b & 7, idx = b >> 3;
  const int l15 = lane & 15, g = lane >> 4;
  unsigned long long kpc; unsigned kclen;
  asm volatile("s_getpc_b64 %0\n.Lk1c%=:\n\ts_mov_b32 %1, .Lk2_code_end-.Lk1c%=" : "=s"(kpc), "=s"(kclen));
  kclen = kclen > 0x10000u ? 0u : kclen;
  unsigned kcv = 0;
  const __amdgpu_buffer_rsrc_t rsc = __builtin_amdgcn_make_buffer_rsrc((void*)kpc, 0, kclen, 0x00020000);
  if (w == KCP_W) kcv = __builtin_amdgcn_raw_buffer_load_b32(rsc, (unsigned)((idx * 64 + lane) * 128), 0, 0);
  SB0;
  const int rg = (xcd >> 1) * 4 + (idx >> 3), hp = (xcd & 1) * 8 + (idx & 7), hq = hp ^ 1;
  const unsigned l0 = lds_base(lds);
  const int wq = w & 3;
  float* cst = (float*)lds;
  float* sm = (float*)(lds + 1024);
  float* sq = (float*)(lds + 1536);
  const unsigned frg = l0 + 4096;
  const s16x8 ones = {0x3F80, 0x3F80, 0x3F80, 0x3F80, 0x3F80, 0x3F80, 0x3F80, 0x3F80};
  unsigned xa[2];
  { const int row = 32 * wq + l15;
#pragma unroll
    for (int t = 0; t < 2; ++t) xa[t] = l0 + (unsigned)(row * 128 + (((4 * t + g) ^ ((row >> 1) & 7)) * 16)); }

  const unsigned long long ax = (unsigned long long)x, awq = (unsigned long long)wqkv;
  if (w >= 8) {
    unsigned xoff[4], woff[4];
#pragma unroll
    for (int q = 0; q < 4; ++q) {
      const int p = 4 * wq + q;
      { const int row = 8 * p + (lane >> 3), lc = (lane & 7) ^ ((row >> 1) & 7);
        xoff[q] = (unsigned)(((rg * 128 + row) * 640 + lc * 8) * 2); }
      { const int row = 4 * p + (lane >> 4), lc = (lane & 15) ^ (((row & 3) << 2) | ((row >> 2) & 3));
        const int col = lc < 5 ? 640 + 40 * hp + 8 * lc : lc < 10 ? 1280 + 40 * hp + 8 * (lc - 5) : 40 * hq + 8 * (lc < 15 ? lc - 10 : 4);
        woff[q] = (unsigned)((row * 1920 + col) * 2); }
    }
    const __amdgpu_buffer_rsrc_t rsx = __builtin_amdgcn_make_buffer_rsrc((void*)x, 0, 2048u * 640u * 2u, 0x00020000);
    const __amdgpu_buffer_rsrc_t rsw = __builtin_amdgcn_make_buffer_rsrc((void*)wqkv, 0, 640u * 1920u * 2u, 0x00020000);
    const unsigned ldsb = l0 + wq * 4096;
    auto issue1 = [&](int c, int q) AI {
      const unsigned sb = ldsb + (c % 5) * 32768;
      dma16(rsx, xoff[q], c * 128, sb + q * 1024);
      dma16(rsw, woff[q], (unsigned)c * (64 * 3840), sb + 16384 + q * 1024);
    };
#pragma unroll
    for (int c = 0; c < PRE; ++c) {
#pragma unroll
      for (int q = 0; q < 4; ++q) issue1(c, q);
    }
    const unsigned OOB = 0x80000000u;
    const __amdgpu_buffer_rsrc_t rsws = __builtin_amdgcn_make_buffer_rsrc((void*)ws, 0, WS_BYTES, 0x00020000);
    const bool light = ((hp ^ (hp >> 2)) & 1) == 0;
    const int G = rg * 8 + (hp >> 1);
    const int jp = G >> 4, Lp = (G >> 3) & 1;
    s16x8 sa[2][2][3];
    unsigned sdst[2], scs[2], svo[2], sld[2], sau[2];
    int sph[2], swc[2];
    unsigned long long sptr[2];
    const unsigned long long awo = (unsigned long long)wo, aw1 = (unsigned long long)w1, aw2 = (unsigned long long)w2, aw3 = (unsigned long long)w3, aw4 = (unsigned long long)w4;
    auto side_prep = [&, awo, aw1, aw2, aw3, aw4](auto REP) AI {
      constexpr int rep = REP;
      const int tl = wq + 4 * rep;
      const bool on = tl < (light ? 6 : 5);
      const int n = 11 * (G & 7) + (light ? 0 : 6) + (on ? tl : 0);
      const int ph = n < 80 ? n / 10 : 8, s = n - 10 * ph;
      sptr[rep] = ph == 0 ? awo : ph == 1 ? aw1 : ph == 2 ? aw2 : ph < 6 ? aw3 : aw4;
      const bool is3 = ph >= 3 && ph < 6;
      const unsigned ld = is3 ? 3456u : 1280u;
      const int colb = is3 ? 432 * jp + 160 * (ph - 3) : 160 * jp;
      const int KS = ph == 8 ? 8 * Lp + s : 10 * Lp + s;
      const int wc = ph == 8 ? KS >> 2 : KS / 5, ks = ph == 8 ? KS & 3 : KS - 5 * wc;
      int row, au = 4;
      bool rok = on;
      if (ph == 0) {
        au = 20;
        row = (KS < 16 ? 40 * KS + 8 * (l15 >> 3) + 4 * ((l15 >> 2) & 1) : 160 * (KS - 16) + 16 + 80 * (l15 >> 3) + 40 * ((l15 >> 2) & 1)) + (l15 & 3);
      } else if (ph < 6) {
        row = 32 * KS + 16 * (l15 >> 3) + 8 * ((l15 >> 2) & 1) + (l15 & 3);
      } else {
        const int hb = 2 * KS + (l15 >> 3);
        const int jj = ph == 8 ? hb >> 2 : (hb * 205) >> 10, c16 = ph == 8 ? hb & 3 : hb - 5 * jj;
        row = 216 * jj + 80 * (ph - 6) + 16 * c16 + 8 * ((l15 >> 2) & 1) + (l15 & 3);
        rok = on && !(ph == 8 && c16 == 3 && ((l15 >> 2) & 1));
      }
      svo[rep] = rok ? (unsigned)row * ld + (unsigned)(colb + 16 * g) : OOB;
      sld[rep] = ld; sau[rep] = (unsigned)au * ld;
      const int jbase = ph <= 5 ? 25 * ph : ph == 6 ? 145 : ph == 7 ? 170 : 195;
      sdst[rep] = on ? OFF_WP + (unsigned)(((jp * 4 + wc) * 215 + jbase + ks) * 1024 + lane * 16) : OOB;
      sph[rep] = ph; swc[rep] = wc;
      const int cc = ph == 1 ? 80 * jp : 640 + 216 * jp + 80 * (ph - 3);
      scs[rep] = (on && (ph == 1 || is3)) ? OFF_CS + (unsigned)((KS * 2432 + cc + lane) * 4) : OOB;
    };
    auto side_ld = [&](auto NN) AI {
      constexpr int n = NN, rep = n / 6, u = (n % 6) / 3, cgp = n % 3;
      const int ph = sph[rep];
      const __amdgpu_buffer_rsrc_t rs = __builtin_amdgcn_make_buffer_rsrc((void*)sptr[rep], 0, ph < 3 ? 640u * 1280u : 640u * 3456u, 0x00020000);
      sa[rep][u][cgp] = as_frag(__builtin_amdgcn_raw_buffer_load_b128(rs, ((cgp == 2 && g >= 2) || (ph == 5 && (cgp == 2 || (cgp == 1 && g == 3)))) ? OOB : svo[rep] + (unsigned)(cgp * 64), u ? sau[rep] : 0u, 0));
    };
    auto side_store = [&](auto REP) AI {
      constexpr int rep = REP;
      s16x8 sel[2];
#pragma unroll
      for (int hf = 0; hf < 2; ++hf)
#pragma unroll
        for (int i = 0; i < 8; ++i) sel[hf][i] = (8 * g + i == 16 * hf + l15) ? (short)0x3F80 : (short)0;
      const int ph = sph[rep], wc = swc[rep], nks = ph == 8 ? 4 : 5;
      const f32x4 z = {0, 0, 0, 0};
      float csv[5];
#pragma unroll
      for (int T = 0; T < 5; ++T) {
        const f32x4 d0 = mfma16(sa[rep][0][T >> 1], sel[T & 1], z), d1 = mfma16(sa[rep][1][T >> 1], sel[T & 1], z);
        u32x4 o = {pk2(d0[0], d0[1]), pk2(d0[2], d0[3]), pk2(d1[0], d1[1]), pk2(d1[2], d1[3])};
        const int Lq = (T - wc) & 3;
        const int pos = T == 4 ? 3 : ((ph == 5 ? 0x2103 : 0x2104) >> (4 * Lq)) & 15;
        __builtin_amdgcn_raw_buffer_store_b128(o, rsws, (T == 4 && ph == 5) ? OOB : sdst[rep], (unsigned)(pos * nks * 1024), WP_AUX);
        csv[T] = mfma16(ones, as_frag(o), z)[0];
      }
      const float c4 = g == 0 ? csv[0] : g == 1 ? csv[1] : g == 2 ? csv[2] : csv[3];
      __builtin_amdgcn_raw_buffer_store_b32(__float_as_uint(c4), rsws, (ph == 5 && lane >= 56) ? OOB : scs[rep], 0, WP_AUX);
      __builtin_amdgcn_raw_buffer_store_b32(__float_as_uint(csv[4]), rsws, (ph == 5 || g != 0) ? OOB : scs[rep], 256, WP_AUX);
    };
    side_prep(std::integral_constant<int, 0>{});
    SB0;
    wait_vm<k1_after(0)>();
    bar_raw();
    sfor<0, 9>([&](auto C) AI {
      constexpr int c = C;
      sfor<(c == 0 ? PRE : c + 3), (c + 3 <= 9 ? c + 4 : c + 3)>([&](auto CC) AI {
#pragma unroll
        for (int q = 0; q < 4; ++q) issue1(CC, q);
      });
      SB0;
      sfor<k1_side_first(c), k1_side_first(c + 1)>([&](auto NN) AI { side_ld(NN); SB0; });
      {
      if constexpr (c == SIDE_P0 && !(SIDE_PRE & 1)) side_prep(std::integral_constant<int, 0>{});
      if constexpr (c == SIDE_P1 && !(SIDE_PRE & 2)) side_prep(std::integral_constant<int, 1>{});
      if constexpr (c == SIDE_S0) side_store(std::integral_constant<int, 0>{});
      if constexpr (c == SIDE_S1) side_store(std::integral_constant<int, 1>{});
      }
      SB0;
      wait_vm<k1_after(c + 1)>();
      bar_raw();
    });
    sfor<k1_side_first(9), 12>([&](auto NN) AI { side_ld(NN); SB0; });
    if constexpr (SIDE_S0 == 9) side_store(std::integral_constant<int, 0>{});
    if constexpr (SIDE_S1 == 9) side_store(std::integral_constant<int, 1>{});
    return;
  }
  unsigned pfv = 0;
  auto prefetch = [&, ax, awq]() AI {
    const int c = PF_C0 + w, i = idx, s = (c <= 9 && i < 23) ? 0 : 230, l = i - 8, sg = l >= 10 ? 2 : l >= 5 ? 1 : 0;
    const bool isx = i < 8;
    const unsigned vo = isx ? (unsigned)((512 * (xcd >> 1) + 64 * i + lane) * 1280 + c * 128)
                            : (unsigned)((64 * c + lane) * 3840 + sg * 1280 + (xcd & 1) * 640 + (l - 5 * sg) * 128);
    const unsigned long long pa = isx ? ax : awq;
    const __amdgpu_buffer_rsrc_t rp = __builtin_amdgcn_make_buffer_rsrc((void*)pa, 0, s < 230 ? (isx ? 2048u * 1280u : 640u * 3840u) : 0u, 0x00020000);
    pfv = __builtin_amdgcn_raw_buffer_load_b32(rp, vo, 0, 0);
    SB0;
  };
  const int tq = (lane >> 2) & 3, tp = lane & 3;
  auto wtr_addr = [&](int cg, int u) {
    const int row = 8 * g + 4 * u + tq;
    const int f = (tq << 2) | ((2 * g + u) & 3);
    return l0 + (unsigned)(16384 + row * 256 + (((cg >> 1) ^ f) * 16) + (cg & 1) * 8);
  };
  const int cgA = 20 + (tp == 0 ? 0 : tp == 1 ? 2 : tp == 2 ? 5 : 7);
  const int cgC = 20 + ((tp & 1) ? 9 : 4);
  unsigned wc[2];
  { const int cg = w < 5 ? 4 * w + tp : w == 5 ? cgA : w == 6 ? cgA + 1 : cgC;
    wc[0] = wtr_addr(cg, 0); wc[1] = wtr_addr(cg, 1); }
  f32x4 cs = {0, 0, 0, 0};
  const int ta = w < 2 ? 0 : w < 5 ? 1 : 2, tb = w < 2 ? w : w < 5 ? w - 2 : w - 5;
  unsigned kvdst;
  { const int dcol = 16 * ta + 4 * g;
    const int hl = dcol >= 20 ? 1 : 0, d = dcol - 20 * hl;
    int e, hle;
    if (tb == 0) { e = l15 < 8 ? 20 : l15 - 8; hle = l15 < 8 ? hl : 0; }
    else { const int vc = 16 * tb - 8 + l15; hle = vc >= 20 ? 1 : 0; e = vc - 20 * hle; }
    const bool ok = dcol < 40 && hle == hl && !(tb == 0 && l15 >= 1 && l15 < 8);
    kvdst = ok ? OFF_KVP + (unsigned)((((rg * 32 + 2 * hp + hl) * 21 + e) * 20 + d) * 4) : 0x80000000u; }
  const __amdgpu_buffer_rsrc_t rsws = __builtin_amdgcn_make_buffer_rsrc((void*)ws, 0, WS_BYTES, 0x00020000);
  unsigned fillo, fillo1, fv0;
  { const int u0 = b * 8 + w, t0 = u0 / 20, k0 = u0 - 20 * t0, q0 = k0 / 5, u1 = u0 + 768;
    fillo = (u0 < 1280 ? OFF_X4 + (unsigned)((((t0 >> 1) * 4 + (t0 & 1) * 3) * 20 + k0) * 1024) : OFF_OB + (unsigned)((u0 - 1280) * 1024)) + (unsigned)(lane * 16);
    fillo1 = u1 < 2560 ? OFF_OB + (unsigned)(u1 * 1024 + lane * 16) : u1 < 2608 ? OFF_FLG + (unsigned)((u1 - 2560) * 1024 + lane * 16) : 0x80000000u;
    fv0 = ((t0 == 0 && ((q0 == 0 && l15 < 1) || (q0 == 2 && l15 < 2))) || (t0 == 63 && ((q0 == 1 && l15 >= 15) || (q0 == 3 && l15 >= 14)))) ? 0u : 0xffffffffu; }
  const __amdgpu_buffer_rsrc_t rshp = __builtin_amdgcn_make_buffer_rsrc((void*)(ws + OFF_HH), 0, HPF == 1 ? OFF_X4 - OFF_HH : HPF == 3 ? 0u : OFF_FLG - OFF_HH, 0x00020000);
  auto fill = [&](int c) AI {
    if (c == HPF_C)
      asm volatile("s_mov_b32 m0, %0\n\ts_nop 0\n\tbuffer_load_dword %1, %2, 0 offen lds"
                   :: "s"(l0 + 2048u + (unsigned)w * 256u), "v"((unsigned)(((w * 256 + b) * 64 + lane) * HPF_STEP)), "s"(rshp) : "m0", "memory");
    if (c == FILL_C0) __builtin_amdgcn_raw_buffer_store_b128((u32x4){fv0, fv0, fv0, fv0}, rsws, fillo, 0, FILL_AUX);
    if (c == FILL_C0 + 1) __builtin_amdgcn_raw_buffer_store_b128((u32x4){~0u, ~0u, ~0u, ~0u}, rsws, fillo1, 0, FILL_AUX);
  };

  if (w < 4) {
    unsigned wa[5][2];
#pragma unroll
    for (int T = 0; T < 5; ++T)
#pragma unroll
      for (int u = 0; u < 2; ++u) wa[T][u] = wtr_addr(4 * T + tp, u);
    f32x4 acc[2][5];
#pragma unroll
    for (int T = 0; T < 5; ++T) { acc[0][T] = (f32x4){0, 0, 0, 0}; acc[1][T] = (f32x4){0, 0, 0, 0}; }
    s16x8 xA[2], wA[6], xB[2], wB[6];
    auto rd = [&](int t, s16x8 (&xf)[2], s16x8 (&wf)[6]) {
      xf[0] = lds_rd128(xa[t]);
      wf[0] = lds_rdtr(wa[0][0], wa[0][1], t * 8192);
      xf[1] = lds_rd128(xa[t] + 2048);
#pragma unroll
      for (int T = 1; T < 5; ++T) wf[T] = lds_rdtr(wa[T][0], wa[T][1], t * 8192);
      wf[5] = lds_rdtr(wc[0], wc[1], t * 8192);
    };
    auto mm = [&](const s16x8 (&xf)[2], const s16x8 (&wf)[6]) {
#pragma unroll
      for (int T = 0; T < 5; ++T) {
        acc[0][T] = mfma16(xf[0], wf[T], acc[0][T]);
        acc[1][T] = mfma16(xf[1], wf[T], acc[1][T]);
      }
      cs = mfma16(ones, wf[5], cs);
    };
    auto adv = [&](unsigned d, int half) {
      xa[half] += d; wc[half] += d;
#pragma unroll
      for (int T = 0; T < 5; ++T) wa[T][half] += d;
    };
    auto half = [&](const s16x8 (&xc)[2], const s16x8 (&wcur)[6], s16x8 (&xn)[2], s16x8 (&wn)[6], int t, unsigned d, bool doadv) {
      s16x4 lo[6], hi[6];
      auto tr = [&](unsigned a) { return __builtin_amdgcn_ds_read_tr16_b64_v4i16((LAS s16x4*)(uintptr_t)(a + t * 8192)); };
      SB0; acc[0][0] = mfma16(xc[0], wcur[0], acc[0][0]); SB0; xn[0] = lds_rd128(xa[t]); lo[0] = tr(wa[0][0]);
      SB0; acc[1][0] = mfma16(xc[1], wcur[0], acc[1][0]); SB0; hi[0] = tr(wa[0][1]); xn[1] = lds_rd128(xa[t] + 2048);
      sfor<1, 5>([&](auto TT) {
        constexpr int T = TT;
        SB0; acc[0][T] = mfma16(xc[0], wcur[T], acc[0][T]); SB0; lo[T] = tr(wa[T][0]); hi[T] = tr(wa[T][1]);
        if (T == 4) { lo[5] = tr(wc[0]); hi[5] = tr(wc[1]); }
        SB0; acc[1][T] = mfma16(xc[1], wcur[T], acc[1][T]);
        if (doadv) { wa[T][0] += d; wa[T][1] += d;
                     if (T == 1) { wa[0][0] += d; wa[0][1] += d; }
                     if (T == 2) { xa[0] += d; xa[1] += d; } }
      });
      SB0; cs = mfma16(ones, wcur[5], cs);
      if (doadv) { wc[0] += d; wc[1] += d; }
      SB0;
#pragma unroll
      for (int T = 0; T < 6; ++T) wn[T] = __builtin_shufflevector(lo[T], hi[T], 0, 1, 2, 3, 4, 5, 6, 7);
    };
    if (PF_C0 + w - PF_LA < 0) prefetch();
    bar_raw();
    const int pfit = PF_C0 + w - PF_LA;
    if (pfit == 0) prefetch();
    rd(0, xA, wA);
    int slot = 0;
#pragma unroll 1
    for (int c = 0; c < 9; ++c) {
      const unsigned d = slot == 4 ? (unsigned)(-4 * 32768) : 32768u;
      slot = slot == 4 ? 0 : slot + 1;
      half(xA, wA, xB, wB, 1, d, true);
      bar_raw();
      if (pfit == c + 1) prefetch();
      fill(c);
      half(xB, wB, xA, wA, 0, 0u, false);
    }
    half(xA, wA, xB, wB, 1, 0u, false);
    mm(xB, wB);
    fill(9);
    if (g == 0) cst[w * 16 + l15] = cs[0];
    bar();
    fill(10);
    f32x4 mr[2], rr[2];
#pragma unroll
    for (int mt = 0; mt < 2; ++mt) {
      mr[mt] = *(const f32x4*)(sm + 32 * wq + 16 * mt + 4 * g);
      rr[mt] = *(const f32x4*)(sq + 32 * wq + 16 * mt + 4 * g);
    }
    s16x8 pk[5];
#pragma unroll
    for (int T = 0; T < 5; ++T) {
      const float c = cst[T * 16 + l15];
      const float eps = T < 2 ? 1e-6f : T == 2 ? (l15 < 8 ? 1e-6f : 0.f) : 0.f;
      float v[8];
#pragma unroll
      for (int mt = 0; mt < 2; ++mt)
#pragma unroll
        for (int r = 0; r < 4; ++r) {
          float val = rr[mt][r] * acc[mt][T][r] + (mr[mt][r] * c + eps);
          if (T < 2) val = phi1(val);
          if (T == 2) val = l15 < 8 ? phi1(val) : val;
          v[4 * mt + r] = val;
        }
      u32x4 pp = {pk2(v[0], v[1]), pk2(v[2], v[3]), pk2(v[4], v[5]), pk2(v[6], v[7])};
      pk[T] = as_frag(pp);
      *(LAS s16x8*)(uintptr_t)(frg + (wq * 6 + (T < 3 ? T : T + 1)) * 1024 + lane * 16) = pk[T];
      if (T == 2) *(LAS s16x8*)(uintptr_t)(frg + (wq * 6 + 3) * 1024 + lane * 16) = l15 < 8 ? ones : pk[2];
    }
  } else {
    unsigned wa[3][2];
#pragma unroll
    for (int u = 0; u < 2; ++u) { wa[0][u] = wtr_addr(cgA, u); wa[1][u] = wtr_addr(cgA + 1, u); wa[2][u] = wtr_addr(cgC, u); }
    f32x4 acc[2][3];
#pragma unroll
    for (int T = 0; T < 3; ++T) { acc[0][T] = (f32x4){0, 0, 0, 0}; acc[1][T] = (f32x4){0, 0, 0, 0}; }
    s16x8 xA[2], wA[4], xB[2], wB[4];
    f32x2 s2[2][2] = {{{0.f, 0.f}, {0.f, 0.f}}, {{0.f, 0.f}, {0.f, 0.f}}};
    auto st = [&](const s16x8& xf, int mt, int i) AI {
      const unsigned v = __builtin_bit_cast(u32x4, xf)[i];
      f32x2 p = {bflo(v), bfhi(v)};
      asm volatile("v_pk_fma_f32 %0, %1, %1, %0" : "+v"(s2[mt][i & 1]) : "v"(p));
    };
    const unsigned long long m8 = 0x00FF00FF00FF00FFull;
    const unsigned one2 = 0x3F803F80u;
    auto one8 = [&](s16x8 f) AI {
      u32x4 v = __builtin_bit_cast(u32x4, f);
#pragma unroll
      for (int i = 0; i < 4; ++i) { unsigned o; asm volatile("v_cndmask_b32 %0, %1, %2, %3" : "=v"(o) : "v"(one2), "v"(v[i]), "s"(m8)); v[i] = o; }
      return __builtin_bit_cast(s16x8, v);
    };
    auto rd = [&](int t, s16x8 (&xf)[2], s16x8 (&wf)[4]) {
      xf[0] = lds_rd128(xa[t]);
      wf[0] = lds_rdtr(wa[0][0], wa[0][1], t * 8192);
      xf[1] = lds_rd128(xa[t] + 2048);
      wf[1] = lds_rdtr(wa[1][0], wa[1][1], t * 8192);
      wf[2] = lds_rdtr(wa[2][0], wa[2][1], t * 8192);
      wf[3] = lds_rdtr(wc[0], wc[1], t * 8192);
    };
    auto mm = [&](const s16x8 (&xf)[2], const s16x8 (&wf)[4]) {
#pragma unroll
      for (int T = 0; T < 3; ++T) {
        const s16x8 wt = T == 2 ? one8(wf[2]) : wf[T];
        acc[0][T] = mfma16(wt, xf[0], acc[0][T]);
        acc[1][T] = mfma16(wt, xf[1], acc[1][T]);
      }
      cs = mfma16(ones, wf[3], cs);
#pragma unroll
      for (int i = 0; i < 4; ++i) { st(xf[0], 0, i); st(xf[1], 1, i); }
    };
    auto adv = [&](unsigned d) {
      xa[0] += d; xa[1] += d; wc[0] += d; wc[1] += d;
#pragma unroll
      for (int T = 0; T < 3; ++T) { wa[T][0] += d; wa[T][1] += d; }
    };
    auto half = [&](const s16x8 (&xc)[2], const s16x8 (&wcur)[4], s16x8 (&xn)[2], s16x8 (&wn)[4], int t, unsigned d, bool doadv) {
      s16x4 lo[4], hi[4];
      auto tr = [&](unsigned a) { return __builtin_amdgcn_ds_read_tr16_b64_v4i16((LAS s16x4*)(uintptr_t)(a + t * 8192)); };
      SB0; acc[0][0] = mfma16(wcur[0], xc[0], acc[0][0]); SB0; xn[0] = lds_rd128(xa[t]); lo[0] = tr(wa[0][0]); st(xc[0], 0, 0);
      SB0; acc[1][0] = mfma16(wcur[0], xc[1], acc[1][0]); SB0; hi[0] = tr(wa[0][1]); xn[1] = lds_rd128(xa[t] + 2048); st(xc[1], 1, 0);
      SB0; acc[0][1] = mfma16(wcur[1], xc[0], acc[0][1]); SB0; lo[1] = tr(wa[1][0]); hi[1] = tr(wa[1][1]); st(xc[0], 0, 1);
      SB0; acc[1][1] = mfma16(wcur[1], xc[1], acc[1][1]); SB0; lo[2] = tr(wa[2][0]); hi[2] = tr(wa[2][1]); st(xc[1], 1, 1); const s16x8 w2 = one8(wcur[2]);
      SB0; acc[0][2] = mfma16(w2, xc[0], acc[0][2]); SB0; lo[3] = tr(wc[0]); hi[3] = tr(wc[1]); st(xc[0], 0, 2); st(xc[1], 1, 2);
      SB0; acc[1][2] = mfma16(w2, xc[1], acc[1][2]);
      if (doadv) { wa[0][0] += d; wa[0][1] += d; wa[1][0] += d; wa[1][1] += d; xa[0] += d; xa[1] += d; }
      SB0; st(xc[0], 0, 3);
      SB0; cs = mfma16(ones, wcur[3], cs);
      st(xc[1], 1, 3);
      if (doadv) { wa[2][0] += d; wa[2][1] += d; wc[0] += d; wc[1] += d; }
      SB0;
#pragma unroll
      for (int T = 0; T < 4; ++T) wn[T] = __builtin_shufflevector(lo[T], hi[T], 0, 1, 2, 3, 4, 5, 6, 7);
    };
    if (PF_C0 + w - PF_LA < 0) prefetch();
    bar_raw();
    const int pfit = PF_C0 + w - PF_LA;
    if (pfit == 0) prefetch();
    rd(0, xA, wA);
    int slot = 0;
#pragma unroll 1
    for (int c = 0; c < 9; ++c) {
      const unsigned d = slot == 4 ? (unsigned)(-4 * 32768) : 32768u;
      slot = slot == 4 ? 0 : slot + 1;
      half(xA, wA, xB, wB, 1, d, true);
      bar_raw();
      if (pfit == c + 1) prefetch();
      fill(c);
      half(xB, wB, xA, wA, 0, 0u, false);
    }
    half(xA, wA, xB, wB, 1, 0u, false);
    mm(xB, wB);
    fill(9);
    if (g == 0) cst[w * 16 + l15] = cs[0];
    float rsv[2], mrv[2];
#pragma unroll
    for (int mt = 0; mt < 2; ++mt) {
      const float mm_ = xsum32(g >= 2 ? acc[mt][2][0] : 0.f) * (1.f / 640.f);
      const f32x2 sp = s2[mt][0] + s2[mt][1];
      const float q2 = xsum32(xsum16(sp[0] + sp[1])) * (1.f / 640.f);
      const float var = fmaxf(q2 - mm_ * mm_, 0.f);
      rsv[mt] = rsqrtf(var + 1e-5f); mrv[mt] = -mm_ * rsv[mt];
      if (g == 0) { sm[32 * wq + 16 * mt + l15] = mrv[mt]; sq[32 * wq + 16 * mt + l15] = rsv[mt]; }
    }
    bar();
    fill(10);
    f32x4 c4[3];
#pragma unroll
    for (int T = 0; T < 3; ++T) c4[T] = *(const f32x4*)(cst + (5 + T) * 16 + 4 * g);
#pragma unroll
    for (int mt = 0; mt < 2; ++mt) {
      const float rs = rsv[mt], mrs = mrv[mt];
      float v[3][4];
#pragma unroll
      for (int T = 0; T < 3; ++T)
#pragma unroll
        for (int r = 0; r < 4; ++r) v[T][r] = phi1(rs * acc[mt][T][r] + (mrs * c4[T][r] + 1e-6f));
      const int MT = rg * 8 + 2 * wq + mt;
      const int h = 2 * hq + (g >> 1), ch = g & 1;
      u32x4 o = {pk2(v[0][0], v[0][1]), pk2(v[0][2], v[0][3]), pk2(v[1][0], v[1][1]), pk2(v[1][2], v[1][3])};
      *(u32x4*)(ws + OFF_PQF + (unsigned)(((h * 128 + MT) * 3 + ch) * 256 + l15 * 16)) = o;
      if (g < 2) {
        u32x4 o2 = {pk2(v[2][0], v[2][1]), pk2(v[2][2], v[2][3]), 0u, 0u};
        *(u32x4*)(ws + OFF_PQF + (unsigned)((((2 * hq + g) * 128 + MT) * 3 + 2) * 256 + l15 * 16)) = o2;
      }
    }
  }
  bar();
  {
    f32x4 kvd = {0, 0, 0, 0};
    s16x8 af[4], bf[4];
#pragma unroll
    for (int r = 0; r < 4; ++r) {
      af[r] = lds_rd128(frg + (r * 6 + ta) * 1024 + lane * 16);
      bf[r] = lds_rd128(frg + (r * 6 + 3 + tb) * 1024 + lane * 16);
    }
#pragma unroll
    for (int r = 0; r < 4; ++r) kvd = mfma16(af[r], bf[r], kvd);
    __builtin_amdgcn_raw_buffer_store_b128(__builtin_bit_cast(u32x4, kvd), rsws, kvdst, 0, 0);
  }
  asm volatile("s_waitcnt vmcnt(0)" :: "v"(pfv), "v"(kcv) : "memory");
}

static constexpr int K2_LDS = 131072, K2_THREADS = 256;
static constexpr unsigned L_ST = 49152, L_CS = L_ST + 1024, L_KV = L_CS + 2560, L_DUM = 57344, L_KVP = 65536;
__global__ __launch_bounds__(256, 1) void k2_main(const u16* __restrict__ x, u16* __restrict__ out, char* __restrict__ ws) {
  extern __shared__ __attribute__((aligned(16))) char lds[];
  const int tid = threadIdx.x, lane = tid & 63;
  const int w = __builtin_amdgcn_readfirstlane(tid >> 6);
  const int b = blockIdx.x, xcd = b & 7, idx = b >> 3;
  const unsigned l0 = lds_base(lds);
  const unsigned OOB = 0x80000000u;
  const __amdgpu_buffer_rsrc_t rsws = __builtin_amdgcn_make_buffer_rsrc((void*)ws, 0, WS_BYTES, 0x00020000);
  const int hp = idx & 15, rh = idx >> 4, MT1 = 16 * xcd + 8 * rh + 2 * w;
  {
    const unsigned okv = tid < 210 ? OFF_KVP + (unsigned)(2 * hp * 1680 + tid * 16) : OOB;
#pragma unroll
    for (int rg = 0; rg < 16; ++rg) dma16(rsws, okv, (unsigned)(rg * 53760), l0 + L_KVP + (unsigned)(rg * 4096 + w * 1024));
  }
  const int cl = xcd * 4 + (idx >> 3), j = idx & 7;
  const int l15 = lane & 15, g = lane >> 4;
  const __amdgpu_buffer_rsrc_t rsx = __builtin_amdgcn_make_buffer_rsrc((void*)x, 0, 2048u * 640u * 2u, 0x00020000);
  const __amdgpu_buffer_rsrc_t rso = __builtin_amdgcn_make_buffer_rsrc((void*)out, 0, 2048u * 640u * 2u, 0x00020000);
  {
    const unsigned fo = (unsigned)(lane * 16 + j * 2560 + (4 * cl + w) * 20480), fo3 = lane < 32 ? fo : OOB;
    const bool x4 = w == 1 || w == 2;
    const u32x4 on = {~0u, ~0u, ~0u, ~0u};
#pragma unroll
    for (int k = 0; k < 3; ++k) {
      __builtin_amdgcn_raw_buffer_store_b128(on, rsws, k == 2 ? fo3 : fo, OFF_HB + (unsigned)(k * 1024), 16);
      __builtin_amdgcn_raw_buffer_store_b128(on, rsws, x4 ? (k == 2 ? fo3 : fo) : OOB, OFF_X4 + (unsigned)(k * 1024), 16);
      __builtin_amdgcn_raw_buffer_store_b128(on, rsws, k == 2 ? fo3 : fo, OFF_X1 + (unsigned)(k * 1024), 16);
    }
  }
  const unsigned opq = g < 3 ? OFF_PQF + (unsigned)(((2 * hp * 128 + MT1) * 3 + g) * 256 + l15 * 16) : OOB;
  const unsigned lane16 = (unsigned)(lane * 16);
  const int csL = w < 2 ? 1 : 0;
  const unsigned vocs = (csL ? (lane < 54 ? OFF_CS + (unsigned)((640 + 216 * j + 4 * lane) * 4) : OOB) : (lane < 20 ? OFF_CS + (unsigned)((80 * j + 4 * lane) * 4) : OOB)) + (unsigned)((w & 1) * 10 * 9728);
  {
    unsigned t[32];
    const unsigned pc = (unsigned)((g >> 1) * 256 + (g & 1) * 8), p4 = (unsigned)((5 * j + 4) * 512 + (w >> 1) * 256 + (w & 1) * 8);
    const int dl = j < 2 ? 1 : j < 4 ? -1 : j < 6 ? 2 : -2;
    const unsigned sh = (unsigned)(((l15 + dl + 16) >> 4) * 20480 + ((l15 + dl) & 15) * 16);
    t[0] = pc + (unsigned)(l15 * 16 + j * 2560 + w * 512);
    t[1] = g == 0 ? p4 + (unsigned)(l15 * 16) : OOB;
    t[2] = p4 + (unsigned)(l15 * 16 + g * 2);
    t[3] = pc + (unsigned)(l15 * 16 + (4 * j + w) * 512);
    t[4] = pc + sh + (unsigned)(j * 2560 + w * 512);
    t[5] = g == 0 ? p4 + sh : OOB;
    t[6] = (unsigned)(((64 * cl + l15) * 640 + 80 * j + 16 * w + 4 * g) * 2);
    t[7] = (unsigned)(((64 * cl + l15) * 640 + 80 * j + 64 + 4 * w + g) * 2);
#pragma unroll
    for (int i = 0; i < 3; ++i) t[8 + i] = l0 + (unsigned)(((w + 1 + i) & 3) * 3072 + i * 1024) + lane16;
    t[11] = l0 + (unsigned)(w * 3072) + lane16;
    t[12] = l0 + (unsigned)(12288 + w * 1024) + lane16;
    t[13] = l0 + (unsigned)(12288 + (w * 16 + l15) * 16 + g * 4);
    t[14] = g == (l15 >> 2) ? l0 + L_ST + (unsigned)(l15 * 32 + w * 8) : l0 + L_DUM + (unsigned)(lane * 8);
    t[15] = l0 + L_ST + (unsigned)(l15 * 32);
    t[16] = l0 + L_CS + (unsigned)((16 * w + 4 * g) * 4);
    t[17] = l0 + L_CS + (unsigned)((64 + 4 * w + g) * 4);
    t[18] = lane < (csL ? 60 : 20) ? l0 + L_CS + (unsigned)((w & 1) * 1280 + csL * 320) + lane16 : l0 + L_DUM + lane16;
    t[19] = (unsigned)((8 * w + (lane & 7)) * 4);
    t[20] = lane == 0 ? (unsigned)((4 * j + w) * 4) : OOB;
    t[21] = (w == 3 && g >= 2) ? 0u : 0xffffffffu;
    t[22] = l0 + L_KVP + (unsigned)(tid * 16);
    t[23] = g == 0 ? (unsigned)(((hp & 3) * 16 + l15) * 16) : OOB;
    t[24] = l0 + L_KV + (unsigned)(l15 * 80 + g * 16);
    t[25] = l0 + L_KV + (unsigned)((16 + (l15 & 3)) * 80 + g * 16);
    t[26] = l0 + L_KV + (unsigned)(20 * 80 + g * 16);
    { const int a = tid >= 105 ? 1 : 0, r = tid - 105 * a, e = r / 5, d4 = r - 5 * e;
      t[27] = tid < 210 ? l0 + L_KV + (unsigned)((a * 21 + e) * 80 + d4 * 8) : l0 + L_DUM + (unsigned)(tid * 8); }
    t[28] = tid < 42 ? l0 + L_KV + (unsigned)(tid * 80 + 40) : l0 + L_DUM + (unsigned)(tid * 8);
    t[29] = 0u; t[30] = 0u; t[31] = 0u;
#pragma unroll
    for (int q = 0; q < 8; ++q) *(LAS u32x4*)(uintptr_t)(l0 + (unsigned)(q * 4096 + tid * 16)) = (u32x4){t[4 * q], t[4 * q + 1], t[4 * q + 2], t[4 * q + 3]};
  }
  const unsigned vtab = l0 + (unsigned)(tid * 16);
  const unsigned sW = OFF_WP + (unsigned)((j * 4 + w) * 215 * 1024);
  const unsigned sB20 = (unsigned)(4 * cl * 20480 + w * 5120), sB56 = (unsigned)(4 * cl * 57344 + w * 5120), sB56q = (unsigned)(4 * cl * 57344 + 40960 + w * 4096);
  const unsigned sP20 = (unsigned)(4 * cl * 20480), sP56 = (unsigned)(4 * cl * 57344), sFL = (unsigned)(cl * 384);
  const unsigned sOBx = OFF_OB + (unsigned)((MT1 * 20 + hp) * 1024), sOBy = OFF_OB + (unsigned)((MT1 * 20 + 16 + (hp >> 2)) * 1024);
  asm volatile(
    "s_nop 4\n\t"
    "v_mov_b32 v8, %5\n\t"
    "s_mov_b32 s46, 98304\n\t"
    "buffer_load_dwordx4 v[176:179], %3, %0, 0 offen offset:0\n\t"
    "buffer_load_dwordx4 v[180:183], %3, %0, s46 offen offset:0\n\t"
    "buffer_load_dwordx4 v[184:187], %3, %0, 0 offen offset:768\n\t"
    "buffer_load_dwordx4 v[188:191], %3, %0, s46 offen offset:768\n\t"
    "ds_read_b128 v[16:19], %4 offset:0\n\t"
    "ds_read_b128 v[20:23], %4 offset:4096\n\t"
    "ds_read_b128 v[24:27], %4 offset:8192\n\t"
    "ds_read_b128 v[28:31], %4 offset:12288\n\t"
    "ds_read_b128 v[32:35], %4 offset:16384\n\t"
    "ds_read_b128 v[36:39], %4 offset:20480\n\t"
    "ds_read_b128 v[40:43], %4 offset:24576\n\t"
    "ds_read_b128 v[44:47], %4 offset:28672\n\t"
    "v_add_u32 v9, 0x1000, v8\n\t"
    "s_mov_b32 s40, %7\n\t"
    "buffer_load_dwordx4 a[64:67], v8, %0, s40 offen offset:0\n\t"
    "buffer_load_dwordx4 a[68:71], v8, %0, s40 offen offset:1024\n\t"
    "buffer_load_dwordx4 a[72:75], v8, %0, s40 offen offset:2048\n\t"
    "buffer_load_dwordx4 a[76:79], v8, %0, s40 offen offset:3072\n\t"
    "s_mov_b32 s54, 0xffff0000\n\t"
    "s_mov_b32 s45, 0x40000\n\t"
    "s_mov_b32 s48, 0x22222222\n\t"
    "s_mov_b32 s49, 0x22222222\n\t"
    "s_mov_b32 s50, 0x44444444\n\t"
    "s_mov_b32 s51, 0x44444444\n\t"
    "s_mov_b32 s52, 0x88888888\n\t"
    "s_mov_b32 s53, 0x88888888\n\t"
    "v_mov_b32 v10, 0xc0135761\n\t"
    "v_mov_b32 v11, 0xbdd2d3e8\n\t"
    "v_mov_b32 v12, 0\n\t"
    "v_mov_b32 v13, 0\n\t"
    "s_waitcnt lgkmcnt(0)\n\t"
    "ds_write_b64 v44, v[12:13] offset:0\n\t"
    "ds_write_b64 v44, v[12:13] offset:8\n\t"
    "ds_write_b64 v44, v[12:13] offset:16\n\t"
    "s_waitcnt vmcnt(8)\n\t"
    "ds_read_b128 v[48:51], v38 offset:0\n\t"
    "ds_read_b128 v[52:55], v38 offset:4096\n\t"
    "ds_read_b128 v[56:59], v38 offset:8192\n\t"
    "ds_read_b128 v[60:63], v38 offset:12288\n\t"
    "ds_read_b128 v[64:67], v38 offset:16384\n\t"
    "ds_read_b128 v[68:71], v38 offset:20480\n\t"
    "ds_read_b128 v[72:75], v38 offset:24576\n\t"
    "ds_read_b128 v[76:79], v38 offset:28672\n\t"
    "ds_read_b128 v[80:83], v38 offset:32768\n\t"
    "ds_read_b128 v[84:87], v38 offset:36864\n\t"
    "ds_read_b128 v[88:91], v38 offset:40960\n\t"
    "ds_read_b128 v[92:95], v38 offset:45056\n\t"
    "ds_read_b128 v[96:99], v38 offset:49152\n\t"
    "ds_read_b128 v[100:103], v38 offset:53248\n\t"
    "ds_read_b128 v[104:107], v38 offset:57344\n\t"
    "ds_read_b128 v[108:111], v38 offset:61440\n\t"
    "s_waitcnt lgkmcnt(15)\n\t"
    "s_waitcnt lgkmcnt(7)\n\t"
    "v_pk_add_f32 v[48:49], v[48:49], v[80:81]\n\t"
    "v_pk_add_f32 v[50:51], v[50:51], v[82:83]\n\t"
    "s_waitcnt lgkmcnt(6)\n\t"
    "v_pk_add_f32 v[52:53], v[52:53], v[84:85]\n\t"
    "v_pk_add_f32 v[54:55], v[54:55], v[86:87]\n\t"
    "s_waitcnt lgkmcnt(5)\n\t"
    "v_pk_add_f32 v[56:57], v[56:57], v[88:89]\n\t"
    "v_pk_add_f32 v[58:59], v[58:59], v[90:91]\n\t"
    "s_waitcnt lgkmcnt(4)\n\t"
    "v_pk_add_f32 v[60:61], v[60:61], v[92:93]\n\t"
    "v_pk_add_f32 v[62:63], v[62:63], v[94:95]\n\t"
    "s_waitcnt lgkmcnt(3)\n\t"
    "v_pk_add_f32 v[64:65], v[64:65], v[96:97]\n\t"
    "v_pk_add_f32 v[66:67], v[66:67], v[98:99]\n\t"
    "s_waitcnt lgkmcnt(2)\n\t"
    "v_pk_add_f32 v[68:69], v[68:69], v[100:101]\n\t"
    "v_pk_add_f32 v[70:71], v[70:71], v[102:103]\n\t"
    "s_waitcnt lgkmcnt(1)\n\t"
    "v_pk_add_f32 v[72:73], v[72:73], v[104:105]\n\t"
    "v_pk_add_f32 v[74:75], v[74:75], v[106:107]\n\t"
    "s_waitcnt lgkmcnt(0)\n\t"
    "v_pk_add_f32 v[76:77], v[76:77], v[108:109]\n\t"
    "v_pk_add_f32 v[78:79], v[78:79], v[110:111]\n\t"
    "v_pk_add_f32 v[48:49], v[48:49], v[64:65]\n\t"
    "v_pk_add_f32 v[50:51], v[50:51], v[66:67]\n\t"
    "v_pk_add_f32 v[52:53], v[52:53], v[68:69]\n\t"
    "v_pk_add_f32 v[54:55], v[54:55], v[70:71]\n\t"
    "v_pk_add_f32 v[56:57], v[56:57], v[72:73]\n\t"
    "v_pk_add_f32 v[58:59], v[58:59], v[74:75]\n\t"
    "v_pk_add_f32 v[60:61], v[60:61], v[76:77]\n\t"
    "v_pk_add_f32 v[62:63], v[62:63], v[78:79]\n\t"
    "v_pk_add_f32 v[48:49], v[48:49], v[56:57]\n\t"
    "v_pk_add_f32 v[50:51], v[50:51], v[58:59]\n\t"
    "v_pk_add_f32 v[52:53], v[52:53], v[60:61]\n\t"
    "v_pk_add_f32 v[54:55], v[54:55], v[62:63]\n\t"
    "v_pk_add_f32 v[48:49], v[48:49], v[52:53]\n\t"
    "v_pk_add_f32 v[50:51], v[50:51], v[54:55]\n\t"
    "v_cvt_pk_bf16_f32 v52, v48, v49\n\t"
    "v_cvt_pk_bf16_f32 v53, v50, v51\n\t"
    "ds_write_b64 v43, v[52:53]\n\t"
    "s_waitcnt lgkmcnt(0)\n\t"
    "s_barrier\n\t"
    "ds_read_b128 v[232:235], v40 offset:0\n\t"
    "ds_read_b128 v[236:239], v41 offset:0\n\t"
    "ds_read_b128 v[240:243], v42 offset:0\n\t"
    "ds_read_b128 v[244:247], v40 offset:1680\n\t"
    "ds_read_b128 v[248:251], v41 offset:1680\n\t"
    "ds_read_b128 v[252:255], v42 offset:1680\n\t"
    "s_waitcnt vmcnt(7)\n\t"
    "s_waitcnt lgkmcnt(3)\n\t"
    "v_mfma_f32_16x16x32_bf16 v[56:59], v[240:243], v[176:179], 0\n\t"
    "v_mfma_f32_16x16x32_bf16 v[48:51], v[232:235], v[176:179], 0\n\t"
    "v_mfma_f32_16x16x32_bf16 v[52:55], v[236:239], v[176:179], 0\n\t"
    "s_waitcnt vmcnt(6)\n\t"
    "s_waitcnt lgkmcnt(0)\n\t"
    "v_mfma_f32_16x16x32_bf16 v[68:71], v[252:255], v[180:183], 0\n\t"
    "v_mfma_f32_16x16x32_bf16 v[60:63], v[244:247], v[180:183], 0\n\t"
    "v_mfma_f32_16x16x32_bf16 v[64:67], v[248:251], v[180:183], 0\n\t"
    "s_waitcnt vmcnt(5)\n\t"
    "v_mfma_f32_16x16x32_bf16 v[80:83], v[240:243], v[184:187], 0\n\t"
    "v_mfma_f32_16x16x32_bf16 v[72:75], v[232:235], v[184:187], 0\n\t"
    "v_mfma_f32_16x16x32_bf16 v[76:79], v[236:239], v[184:187], 0\n\t"
    "s_waitcnt vmcnt(4)\n\t"
    "v_mfma_f32_16x16x32_bf16 v[92:95], v[252:255], v[188:191], 0\n\t"
    "v_mfma_f32_16x16x32_bf16 v[84:87], v[244:247], v[188:191], 0\n\t"
    "v_mfma_f32_16x16x32_bf16 v[88:91], v[248:251], v[188:191], 0\n\t"
    "v_add_f32 v56, 0x358637bd, v56\n\t"
    "v_rcp_f32 v56, v56\n\t"
    "v_add_f32 v68, 0x358637bd, v68\n\t"
    "v_rcp_f32 v68, v68\n\t"
    "v_pk_mul_f32 v[48:49], v[48:49], v[56:57] op_sel_hi:[1,0]\n\t"
    "v_pk_mul_f32 v[50:51], v[50:51], v[56:57] op_sel_hi:[1,0]\n\t"
    "v_pk_mul_f32 v[52:53], v[52:53], v[56:57] op_sel_hi:[1,0]\n\t"
    "v_pk_mul_f32 v[54:55], v[54:55], v[56:57] op_sel_hi:[1,0]\n\t"
    "v_pk_mul_f32 v[60:61], v[60:61], v[68:69] op_sel_hi:[1,0]\n\t"
    "v_pk_mul_f32 v[62:63], v[62:63], v[68:69] op_sel_hi:[1,0]\n\t"
    "v_pk_mul_f32 v[64:65], v[64:65], v[68:69] op_sel_hi:[1,0]\n\t"
    "v_pk_mul_f32 v[66:67], v[66:67], v[68:69] op_sel_hi:[1,0]\n\t"
    "v_cvt_pk_bf16_f32 v96, v48, v49\n\t"
    "v_cvt_pk_bf16_f32 v100, v52, v53\n\t"
    "v_cvt_pk_bf16_f32 v97, v50, v51\n\t"
    "v_cvt_pk_bf16_f32 v101, v54, v55\n\t"
    "v_cvt_pk_bf16_f32 v98, v60, v61\n\t"
    "v_cvt_pk_bf16_f32 v102, v64, v65\n\t"
    "v_cvt_pk_bf16_f32 v99, v62, v63\n\t"
    "v_cvt_pk_bf16_f32 v103, v66, v67\n\t"
    "v_min_u32 v96, -2, v96\n\t"
    "v_min_u32 v100, -2, v100\n\t"
    "buffer_store_dwordx4 v[96:99], v8, %0, %14 offen sc1\n\t"
    "buffer_store_dwordx4 v[100:103], v39, %0, %15 offen sc1\n\t"
    "v_add_f32 v80, 0x358637bd, v80\n\t"
    "v_rcp_f32 v80, v80\n\t"
    "v_add_f32 v92, 0x358637bd, v92\n\t"
    "v_rcp_f32 v92, v92\n\t"
    "v_pk_mul_f32 v[72:73], v[72:73], v[80:81] op_sel_hi:[1,0]\n\t"
    "v_pk_mul_f32 v[74:75], v[74:75], v[80:81] op_sel_hi:[1,0]\n\t"
    "v_pk_mul_f32 v[76:77], v[76:77], v[80:81] op_sel_hi:[1,0]\n\t"
    "v_pk_mul_f32 v[78:79], v[78:79], v[80:81] op_sel_hi:[1,0]\n\t"
    "v_pk_mul_f32 v[84:85], v[84:85], v[92:93] op_sel_hi:[1,0]\n\t"
    "v_pk_mul_f32 v[86:87], v[86:87], v[92:93] op_sel_hi:[1,0]\n\t"
    "v_pk_mul_f32 v[88:89], v[88:89], v[92:93] op_sel_hi:[1,0]\n\t"
    "v_pk_mul_f32 v[90:91], v[90:91], v[92:93] op_sel_hi:[1,0]\n\t"
    "v_cvt_pk_bf16_f32 v104, v72, v73\n\t"
    "v_cvt_pk_bf16_f32 v108, v76, v77\n\t"
    "v_cvt_pk_bf16_f32 v105, v74, v75\n\t"
    "v_cvt_pk_bf16_f32 v109, v78, v79\n\t"
    "v_cvt_pk_bf16_f32 v106, v84, v85\n\t"
    "v_cvt_pk_bf16_f32 v110, v88, v89\n\t"
    "v_cvt_pk_bf16_f32 v107, v86, v87\n\t"
    "v_cvt_pk_bf16_f32 v111, v90, v91\n\t"
    "v_min_u32 v104, -2, v104\n\t"
    "v_min_u32 v108, -2, v108\n\t"
    "s_add_u32 s43, %14, 20480\n\t"
    "s_add_u32 s44, %15, 20480\n\t"
    "buffer_store_dwordx4 v[104:107], v8, %0, s43 offen sc1\n\t"
    "buffer_store_dwordx4 v[108:111], v39, %0, s44 offen sc1\n\t"
    "s_mov_b32 s46, 0\n\t"
    "buffer_load_dwordx2 v[130:131], v22, %1, s46 offen\n\t"
    "buffer_load_ushort v144, v23, %1, s46 offen\n\t"
    "s_mov_b32 s46, 20480\n\t"
    "buffer_load_dwordx2 v[134:135], v22, %1, s46 offen\n\t"
    "buffer_load_ushort v145, v23, %1, s46 offen\n\t"
    "s_mov_b32 s46, 40960\n\t"
    "buffer_load_dwordx2 v[138:139], v22, %1, s46 offen\n\t"
    "buffer_load_ushort v146, v23, %1, s46 offen\n\t"
    "s_mov_b32 s46, 61440\n\t"
    "buffer_load_dwordx2 v[142:143], v22, %1, s46 offen\n\t"
    "buffer_load_ushort v147, v23, %1, s46 offen\n\t"
    "buffer_load_dwordx4 v[192:195], %6, %0, 0 offen\n\t"
    "s_mov_b32 s46, 9728\n\t"
    "buffer_load_dwordx4 v[196:199], %6, %0, s46 offen\n\t"
    "s_add_u32 s46, s46, 9728\n\t"
    "buffer_load_dwordx4 v[200:203], %6, %0, s46 offen\n\t"
    "s_add_u32 s46, s46, 9728\n\t"
    "buffer_load_dwordx4 v[204:207], %6, %0, s46 offen\n\t"
    "s_add_u32 s46, s46, 9728\n\t"
    "buffer_load_dwordx4 v[208:211], %6, %0, s46 offen\n\t"
    "s_add_u32 s46, s46, 9728\n\t"
    "buffer_load_dwordx4 v[212:215], %6, %0, s46 offen\n\t"
    "s_add_u32 s46, s46, 9728\n\t"
    "buffer_load_dwordx4 v[216:219], %6, %0, s46 offen\n\t"
    "s_add_u32 s46, s46, 9728\n\t"
    "buffer_load_dwordx4 v[220:223], %6, %0, s46 offen\n\t"
    "s_add_u32 s46, s46, 9728\n\t"
    "buffer_load_dwordx4 v[224:227], %6, %0, s46 offen\n\t"
    "s_add_u32 s46, s46, 9728\n\t"
    "buffer_load_dwordx4 v[228:231], %6, %0, s46 offen\n\t"
    "s_add_u32 s42, %8, 0x1443800\n\t"
    "buffer_load_dwordx4 v[48:51], v8, %0, s42 offen offset:0 sc1\n\t"
    "buffer_load_dwordx4 v[52:55], v8, %0, s42 offen offset:1024 sc1\n\t"
    "buffer_load_dwordx4 v[56:59], v8, %0, s42 offen offset:2048 sc1\n\t"
    "buffer_load_dwordx4 v[60:63], v8, %0, s42 offen offset:3072 sc1\n\t"
    "buffer_load_dwordx4 v[64:67], v9, %0, s42 offen offset:0 sc1\n\t"
    "s_add_u32 s40, s40, 4096\n\t"
    "buffer_load_dwordx4 a[80:83], v8, %0, s40 offen offset:0\n\t"
    "buffer_load_dwordx4 a[84:87], v8, %0, s40 offen offset:1024\n\t"
    "s_waitcnt vmcnt(24)\n\t"
    "v_lshlrev_b32 v128, 16, v130\n\t"
    "v_and_b32 v129, s54, v130\n\t"
    "v_lshlrev_b32 v130, 16, v131\n\t"
    "v_and_b32 v131, s54, v131\n\t"
    "s_waitcnt vmcnt(23)\n\t"
    "v_lshlrev_b32 v144, 16, v144\n\t"
    "s_waitcnt vmcnt(22)\n\t"
    "v_lshlrev_b32 v132, 16, v134\n\t"
    "v_and_b32 v133, s54, v134\n\t"
    "v_lshlrev_b32 v134, 16, v135\n\t"
    "v_and_b32 v135, s54, v135\n\t"
    "s_waitcnt vmcnt(21)\n\t"
    "v_lshlrev_b32 v145, 16, v145\n\t"
    "s_waitcnt vmcnt(20)\n\t"
    "v_lshlrev_b32 v136, 16, v138\n\t"
    "v_and_b32 v137, s54, v138\n\t"
    "v_lshlrev_b32 v138, 16, v139\n\t"
    "v_and_b32 v139, s54, v139\n\t"
    "s_waitcnt vmcnt(19)\n\t"
    "v_lshlrev_b32 v146, 16, v146\n\t"
    "s_waitcnt vmcnt(18)\n\t"
    "v_lshlrev_b32 v140, 16, v142\n\t"
    "v_and_b32 v141, s54, v142\n\t"
    "v_lshlrev_b32 v142, 16, v143\n\t"
    "v_and_b32 v143, s54, v143\n\t"
    "s_waitcnt vmcnt(17)\n\t"
    "v_lshlrev_b32 v147, 16, v147\n\t"
    "s_add_u32 s41, %8, 0x1448800\n\t"
    "buffer_load_dwordx4 v[68:71], v8, %0, s41 offen offset:0 sc1\n\t"
    "buffer_load_dwordx4 v[72:75], v8, %0, s41 offen offset:1024 sc1\n\t"
    "buffer_load_dwordx4 v[76:79], v8, %0, s41 offen offset:2048 sc1\n\t"
    "buffer_load_dwordx4 v[80:83], v8, %0, s41 offen offset:3072 sc1\n\t"
    "buffer_load_dwordx4 v[84:87], v9, %0, s41 offen offset:0 sc1\n\t"
    "buffer_load_dwordx4 a[88:91], v8, %0, s40 offen offset:2048\n\t"
    "buffer_load_dwordx4 a[92:95], v8, %0, s40 offen offset:3072\n\t"
    "s_add_u32 s40, s40, 4096\n\t"
    "buffer_load_dwordx4 a[96:99], v8, %0, s40 offen offset:0\n\t"
    "buffer_load_dwordx4 a[100:103], v8, %0, s40 offen offset:1024\n\t"
    "s_waitcnt vmcnt(25)\n\t"
    "s_waitcnt vmcnt(20)\n\t"
    "v_pk_add_f32 v[192:193], v[192:193], v[212:213]\n\t"
    "v_pk_add_f32 v[194:195], v[194:195], v[214:215]\n\t"
    "s_waitcnt vmcnt(19)\n\t"
    "v_pk_add_f32 v[196:197], v[196:197], v[216:217]\n\t"
    "v_pk_add_f32 v[198:199], v[198:199], v[218:219]\n\t"
    "s_waitcnt vmcnt(18)\n\t"
    "v_pk_add_f32 v[200:201], v[200:201], v[220:221]\n\t"
    "v_pk_add_f32 v[202:203], v[202:203], v[222:223]\n\t"
    "s_waitcnt vmcnt(17)\n\t"
    "v_pk_add_f32 v[204:205], v[204:205], v[224:225]\n\t"
    "v_pk_add_f32 v[206:207], v[206:207], v[226:227]\n\t"
    "s_waitcnt vmcnt(16)\n\t"
    "v_pk_add_f32 v[208:209], v[208:209], v[228:229]\n\t"
    "v_pk_add_f32 v[210:211], v[210:211], v[230:231]\n\t"
    "v_pk_add_f32 v[192:193], v[192:193], v[200:201]\n\t"
    "v_pk_add_f32 v[194:195], v[194:195], v[202:203]\n\t"
    "v_pk_add_f32 v[196:197], v[196:197], v[204:205]\n\t"
    "v_pk_add_f32 v[198:199], v[198:199], v[206:207]\n\t"
    "v_pk_add_f32 v[192:193], v[192:193], v[196:197]\n\t"
    "v_pk_add_f32 v[194:195], v[194:195], v[198:199]\n\t"
    "v_pk_add_f32 v[192:193], v[192:193], v[208:209]\n\t"
    "v_pk_add_f32 v[194:195], v[194:195], v[210:211]\n\t"
    "ds_write_b128 v34, v[192:195]\n\t"
    "v_mov_b32 v208, 0x3f803f80\n\t"
    "v_mov_b32 v209, 0x3f803f80\n\t"
    "v_mov_b32 v210, 0x3f803f80\n\t"
    "v_mov_b32 v211, 0x3f803f80\n\t"
    "s_waitcnt vmcnt(15)\n\t"
    "s_waitcnt vmcnt(14)\n\t"
    "s_waitcnt vmcnt(13)\n\t"
    "v_max3_u32 v227, v48, v52, v56\n\t"
    "s_waitcnt vmcnt(12)\n\t"
    "s_waitcnt vmcnt(11)\n\t"
    "v_max3_u32 v227, v227, v60, v64\n\t"
    "v_cmp_eq_u32 vcc, -1, v227\n\t"
    "s_cbranch_vccnz H1_%=\n\t"
    "R2_%=:\n\t"
    "v_mfma_f32_16x16x32_bf16 a[0:3], a[64:67], v[48:51], 0\n\t"
    "buffer_load_dwordx4 a[104:107], v8, %0, s40 offen offset:2048\n\t"
    "v_mfma_f32_16x16x32_bf16 a[0:3], a[68:71], v[52:55], a[0:3]\n\t"
    "buffer_load_dwordx4 a[108:111], v8, %0, s40 offen offset:3072\n\t"
    "v_mfma_f32_16x16x32_bf16 a[0:3], a[72:75], v[56:59], a[0:3]\n\t"
    "v_mfma_f32_16x16x32_bf16 a[0:3], a[76:79], v[60:63], a[0:3]\n\t"
    "s_add_u32 s40, s40, 4096\n\t"
    "buffer_load_dwordx4 a[112:115], v8, %0, s40 offen offset:0\n\t"
    "s_waitcnt vmcnt(13)\n\t"
    "v_mfma_f32_16x16x32_bf16 a[0:3], a[80:83], v[64:67], a[0:3]\n\t"
    "s_waitcnt vmcnt(12)\n\t"
    "v_mfma_f32_16x16x32_bf16 a[4:7], a[84:87], v[48:51], 0\n\t"
    "s_waitcnt vmcnt(6)\n\t"
    "v_mfma_f32_16x16x32_bf16 a[4:7], a[88:91], v[52:55], a[4:7]\n\t"
    "buffer_load_dwordx4 a[116:119], v8, %0, s40 offen offset:1024\n\t"
    "s_waitcnt vmcnt(6)\n\t"
    "v_mfma_f32_16x16x32_bf16 a[4:7], a[92:95], v[56:59], a[4:7]\n\t"
    "buffer_load_dwordx4 a[120:123], v8, %0, s40 offen offset:2048\n\t"
    "ds_write_b128 v24, a[0:3] offset:0\n\t"
    "s_waitcnt vmcnt(6)\n\t"
    "v_mfma_f32_16x16x32_bf16 a[4:7], a[96:99], v[60:63], a[4:7]\n\t"
    "buffer_load_dwordx4 a[124:127], v8, %0, s40 offen offset:3072\n\t"
    "s_waitcnt vmcnt(6)\n\t"
    "v_mfma_f32_16x16x32_bf16 a[4:7], a[100:103], v[64:67], a[4:7]\n\t"
    "s_add_u32 s40, s40, 4096\n\t"
    "buffer_load_dwordx4 a[128:131], v8, %0, s40 offen offset:0\n\t"
    "s_waitcnt vmcnt(6)\n\t"
    "v_mfma_f32_16x16x32_bf16 a[8:11], a[104:107], v[48:51], 0\n\t"
    "buffer_load_dwordx4 a[132:135], v8, %0, s40 offen offset:1024\n\t"
    "s_waitcnt vmcnt(6)\n\t"
    "v_mfma_f32_16x16x32_bf16 a[8:11], a[108:111], v[52:55], a[8:11]\n\t"
    "buffer_load_dwordx4 a[136:139], v8, %0, s40 offen offset:2048\n\t"
    "ds_write_b128 v25, a[4:7] offset:0\n\t"
    "s_waitcnt vmcnt(6)\n\t"
    "v_mfma_f32_16x16x32_bf16 a[8:11], a[112:115], v[56:59], a[8:11]\n\t"
    "buffer_load_dwordx4 a[140:143], v8, %0, s40 offen offset:3072\n\t"
    "s_waitcnt vmcnt(6)\n\t"
    "v_mfma_f32_16x16x32_bf16 a[8:11], a[116:119], v[60:63], a[8:11]\n\t"
    "s_add_u32 s40, s40, 4096\n\t"
    "buffer_load_dwordx4 a[144:147], v8, %0, s40 offen offset:0\n\t"
    "s_waitcnt vmcnt(6)\n\t"
    "v_mfma_f32_16x16x32_bf16 a[8:11], a[120:123], v[64:67], a[8:11]\n\t"
    "buffer_load_dwordx4 a[148:151], v8, %0, s40 offen offset:1024\n\t"
    "s_waitcnt vmcnt(6)\n\t"
    "v_mfma_f32_16x16x32_bf16 a[12:15], a[124:127], v[48:51], 0\n\t"
    "buffer_load_dwordx4 a[152:155], v8, %0, s40 offen offset:2048\n\t"
    "s_waitcnt vmcnt(6)\n\t"
    "v_mfma_f32_16x16x32_bf16 a[12:15], a[128:131], v[52:55], a[12:15]\n\t"
    "buffer_load_dwordx4 a[156:159], v8, %0, s40 offen offset:3072\n\t"
    "s_waitcnt vmcnt(6)\n\t"
    "v_mfma_f32_16x16x32_bf16 a[12:15], a[132:135], v[56:59], a[12:15]\n\t"
    "s_add_u32 s40, s40, 4096\n\t"
    "buffer_load_dwordx4 a[160:163], v8, %0, s40 offen offset:0\n\t"
    "ds_write_b128 v26, a[8:11] offset:0\n\t"
    "s_waitcnt vmcnt(6)\n\t"
    "v_mfma_f32_16x16x32_bf16 a[12:15], a[136:139], v[60:63], a[12:15]\n\t"
    "s_add_u32 s42, %8, 0x144d800\n\t"
    "buffer_load_dwordx4 v[88:91], v8, %0, s42 offen offset:0 sc1\n\t"
    "s_waitcnt vmcnt(6)\n\t"
    "v_mfma_f32_16x16x32_bf16 a[12:15], a[140:143], v[64:67], a[12:15]\n\t"
    "buffer_load_dwordx4 v[92:95], v8, %0, s42 offen offset:1024 sc1\n\t"
    "s_waitcnt vmcnt(6)\n\t"
    "v_mfma_f32_16x16x32_bf16 v[128:131], a[144:147], v[48:51], v[128:131]\n\t"
    "buffer_load_dwordx4 v[96:99], v8, %0, s42 offen offset:2048 sc1\n\t"
    "s_waitcnt vmcnt(6)\n\t"
    "v_mfma_f32_16x16x32_bf16 v[128:131], a[148:151], v[52:55], v[128:131]\n\t"
    "buffer_load_dwordx4 v[100:103], v8, %0, s42 offen offset:3072 sc1\n\t"
    "s_waitcnt vmcnt(6)\n\t"
    "v_mfma_f32_16x16x32_bf16 v[128:131], a[152:155], v[56:59], v[128:131]\n\t"
    "buffer_load_dwordx4 v[104:107], v9, %0, s42 offen offset:0 sc1\n\t"
    "ds_write_b128 v28, a[12:15] offset:0\n\t"
    "s_waitcnt vmcnt(6)\n\t"
    "v_mfma_f32_16x16x32_bf16 v[128:131], a[156:159], v[60:63], v[128:131]\n\t"
    "s_add_u32 s41, %8, 0x1452800\n\t"
    "buffer_load_dwordx4 v[108:111], v8, %0, s41 offen offset:0 sc1\n\t"
    "s_waitcnt vmcnt(6)\n\t"
    "v_mfma_f32_16x16x32_bf16 v[128:131], a[160:163], v[64:67], v[128:131]\n\t"
    "buffer_load_dwordx4 v[112:115], v8, %0, s41 offen offset:1024 sc1\n\t"
    "s_waitcnt lgkmcnt(0)\n\t"
    "s_barrier\n\t"
    "v_max3_u32 v227, v68, v72, v76\n\t"
    "v_max3_u32 v227, v227, v80, v84\n\t"
    "v_cmp_eq_u32 vcc, -1, v227\n\t"
    "s_cbranch_vccnz H3_%=\n\t"
    "R4_%=:\n\t"
    "v_mfma_f32_16x16x32_bf16 a[0:3], a[64:67], v[68:71], 0\n\t"
    "buffer_load_dwordx4 v[116:119], v8, %0, s41 offen offset:2048 sc1\n\t"
    "ds_read_b128 v[176:179], v27 offset:0\n\t"
    "ds_read_b128 v[180:183], v27 offset:1024\n\t"
    "ds_read_b128 v[184:187], v27 offset:2048\n\t"
    "v_mfma_f32_16x16x32_bf16 a[0:3], a[68:71], v[72:75], a[0:3]\n\t"
    "ds_read_b32 v188, v29 offset:0\n\t"
    "ds_read_b32 v189, v29 offset:1024\n\t"
    "ds_read_b32 v190, v29 offset:2048\n\t"
    "v_mfma_f32_16x16x32_bf16 a[0:3], a[72:75], v[76:79], a[0:3]\n\t"
    "ds_read_b32 v191, v29 offset:3072\n\t"
    "v_mfma_f32_16x16x32_bf16 a[0:3], a[76:79], v[80:83], a[0:3]\n\t"
    "v_mfma_f32_16x16x32_bf16 a[0:3], a[80:83], v[84:87], a[0:3]\n\t"
    "v_mfma_f32_16x16x32_bf16 a[4:7], a[84:87], v[68:71], 0\n\t"
    "v_mfma_f32_16x16x32_bf16 a[4:7], a[88:91], v[72:75], a[4:7]\n\t"
    "buffer_load_dwordx4 v[120:123], v8, %0, s41 offen offset:3072 sc1\n\t"
    "v_mfma_f32_16x16x32_bf16 a[4:7], a[92:95], v[76:79], a[4:7]\n\t"
    "v_mfma_f32_16x16x32_bf16 a[4:7], a[96:99], v[80:83], a[4:7]\n\t"
    "v_mfma_f32_16x16x32_bf16 a[4:7], a[100:103], v[84:87], a[4:7]\n\t"
    "v_mfma_f32_16x16x32_bf16 a[8:11], a[104:107], v[68:71], 0\n\t"
    "s_add_u32 s43, %11, 0x16c3800\n\t"
    "ds_write_b128 v24, a[0:3] offset:16384\n\t"
    "s_waitcnt lgkmcnt(7)\n\t"
    "s_waitcnt lgkmcnt(6)\n\t"
    "v_add_f32 v176, v176, v180\n\t"
    "v_mfma_f32_16x16x32_bf16 a[8:11], a[108:111], v[72:75], a[8:11]\n\t"
    "buffer_load_dwordx4 v[124:127], v9, %0, s41 offen offset:0 sc1\n\t"
    "ds_write_b128 v25, a[4:7] offset:16384\n\t"
    "v_add_f32 v177, v177, v181\n\t"
    "v_add_f32 v178, v178, v182\n\t"
    "v_mfma_f32_16x16x32_bf16 a[8:11], a[112:115], v[76:79], a[8:11]\n\t"
    "v_add_f32 v179, v179, v183\n\t"
    "s_waitcnt lgkmcnt(6)\n\t"
    "v_add_f32 v128, v128, v184\n\t"
    "v_add_f32 v129, v129, v185\n\t"
    "v_mfma_f32_16x16x32_bf16 a[8:11], a[116:119], v[80:83], a[8:11]\n\t"
    "v_add_f32 v130, v130, v186\n\t"
    "v_add_f32 v131, v131, v187\n\t"
    "s_waitcnt lgkmcnt(5)\n\t"
    "s_waitcnt lgkmcnt(4)\n\t"
    "v_add_f32 v188, v188, v189\n\t"
    "v_mfma_f32_16x16x32_bf16 a[8:11], a[120:123], v[84:87], a[8:11]\n\t"
    "s_waitcnt lgkmcnt(3)\n\t"
    "s_waitcnt lgkmcnt(2)\n\t"
    "v_add_f32 v190, v190, v191\n\t"
    "v_add_f32 v128, v128, v176\n\t"
    "v_add_f32 v129, v129, v177\n\t"
    "v_mfma_f32_16x16x32_bf16 a[12:15], a[124:127], v[68:71], 0\n\t"
    "buffer_load_dwordx4 a[164:167], v8, %0, s40 offen offset:1024\n\t"
    "v_add_f32 v130, v130, v178\n\t"
    "ds_write_b128 v26, a[8:11] offset:16384\n\t"
    "v_add_f32 v131, v131, v179\n\t"
    "v_mfma_f32_16x16x32_bf16 a[12:15], a[128:131], v[72:75], a[12:15]\n\t"
    "v_add_f32 v188, v188, v190\n\t"
    "v_cvt_pk_bf16_f32 v212, v128, v129\n\t"
    "v_cvt_pk_bf16_f32 v213, v130, v131\n\t"
    "v_mfma_f32_16x16x32_bf16 a[12:15], a[132:135], v[76:79], a[12:15]\n\t"
    "v_add_f32 v144, v144, v188\n\t"
    "v_min_u32 v212, -2, v212\n\t"
    "buffer_store_dwordx2 v[212:213], v16, %0, s43 offen sc1\n\t"
    "v_mfma_f32_16x16x32_bf16 a[12:15], a[136:139], v[80:83], a[12:15]\n\t"
    "v_mov_b32 v214, v144\n\t"
    "v_mov_b32 v215, v144\n\t"
    "s_nop 1\n\t"
    "v_permlane16_swap_b32 v214, v215\n\t"
    "v_mfma_f32_16x16x32_bf16 a[12:15], a[140:143], v[84:87], a[12:15]\n\t"
    "buffer_load_dwordx4 a[168:171], v8, %0, s40 offen offset:2048\n\t"
    "v_cvt_pk_bf16_f32 v216, v214, v215\n\t"
    "v_mov_b32 v217, v216\n\t"
    "s_nop 1\n\t"
    "v_permlane32_swap_b32 v216, v217\n\t"
    "v_mfma_f32_16x16x32_bf16 v[132:135], a[144:147], v[68:71], v[132:135]\n\t"
    "v_min_u32 v216, -2, v216\n\t"
    "ds_write_b128 v28, a[12:15] offset:16384\n\t"
    "buffer_store_dwordx2 v[216:217], v17, %0, s43 offen sc1\n\t"
    "v_mfma_f32_16x16x32_bf16 v[132:135], a[148:151], v[72:75], v[132:135]\n\t"
    "v_mfma_f32_16x16x32_bf16 v[132:135], a[152:155], v[76:79], v[132:135]\n\t"
    "buffer_load_dwordx4 a[172:175], v8, %0, s40 offen offset:3072\n\t"
    "v_mfma_f32_16x16x32_bf16 v[132:135], a[156:159], v[80:83], v[132:135]\n\t"
    "v_mfma_f32_16x16x32_bf16 v[132:135], a[160:163], v[84:87], v[132:135]\n\t"
    "s_waitcnt lgkmcnt(0)\n\t"
    "s_barrier\n\t"
    "s_waitcnt vmcnt(14)\n\t"
    "s_waitcnt vmcnt(13)\n\t"
    "s_waitcnt vmcnt(12)\n\t"
    "v_max3_u32 v227, v88, v92, v96\n\t"
    "s_waitcnt vmcnt(11)\n\t"
    "s_waitcnt vmcnt(10)\n\t"
    "v_max3_u32 v227, v227, v100, v104\n\t"
    "v_cmp_eq_u32 vcc, -1, v227\n\t"
    "s_cbranch_vccnz H5_%=\n\t"
    "R6_%=:\n\t"
    "v_mfma_f32_16x16x32_bf16 a[0:3], a[64:67], v[88:91], 0\n\t"
    "s_add_u32 s40, s40, 4096\n\t"
    "buffer_load_dwordx4 a[176:179], v8, %0, s40 offen offset:0\n\t"
    "ds_read_b128 v[176:179], v27 offset:16384\n\t"
    "ds_read_b128 v[180:183], v27 offset:17408\n\t"
    "ds_read_b128 v[184:187], v27 offset:18432\n\t"
    "v_mfma_f32_16x16x32_bf16 a[0:3], a[68:71], v[92:95], a[0:3]\n\t"
    "ds_read_b32 v188, v29 offset:16384\n\t"
    "ds_read_b32 v189, v29 offset:17408\n\t"
    "ds_read_b32 v190, v29 offset:18432\n\t"
    "v_mfma_f32_16x16x32_bf16 a[0:3], a[72:75], v[96:99], a[0:3]\n\t"
    "ds_read_b32 v191, v29 offset:19456\n\t"
    "v_mfma_f32_16x16x32_bf16 a[0:3], a[76:79], v[100:103], a[0:3]\n\t"
    "buffer_load_dwordx4 a[180:183], v8, %0, s40 offen offset:1024\n\t"
    "v_mfma_f32_16x16x32_bf16 a[0:3], a[80:83], v[104:107], a[0:3]\n\t"
    "v_mfma_f32_16x16x32_bf16 a[4:7], a[84:87], v[88:91], 0\n\t"
    "v_mfma_f32_16x16x32_bf16 a[4:7], a[88:91], v[92:95], a[4:7]\n\t"
    "v_mfma_f32_16x16x32_bf16 a[4:7], a[92:95], v[96:99], a[4:7]\n\t"
    "v_mfma_f32_16x16x32_bf16 a[4:7], a[96:99], v[100:103], a[4:7]\n\t"
    "buffer_load_dwordx4 a[184:187], v8, %0, s40 offen offset:2048\n\t"
    "v_mfma_f32_16x16x32_bf16 a[4:7], a[100:103], v[104:107], a[4:7]\n\t"
    "v_mfma_f32_16x16x32_bf16 a[8:11], a[104:107], v[88:91], 0\n\t"
    "s_add_u32 s43, %11, 0x16c8800\n\t"
    "ds_write_b128 v24, a[0:3] offset:0\n\t"
    "s_waitcnt lgkmcnt(7)\n\t"
    "s_waitcnt lgkmcnt(6)\n\t"
    "v_add_f32 v176, v176, v180\n\t"
    "v_mfma_f32_16x16x32_bf16 a[8:11], a[108:111], v[92:95], a[8:11]\n\t"
    "v_add_f32 v177, v177, v181\n\t"
    "ds_write_b128 v25, a[4:7] offset:0\n\t"
    "v_add_f32 v178, v178, v182\n\t"
    "v_mfma_f32_16x16x32_bf16 a[8:11], a[112:115], v[96:99], a[8:11]\n\t"
    "v_add_f32 v179, v179, v183\n\t"
    "s_waitcnt lgkmcnt(6)\n\t"
    "v_add_f32 v132, v132, v184\n\t"
    "v_add_f32 v133, v133, v185\n\t"
    "v_mfma_f32_16x16x32_bf16 a[8:11], a[116:119], v[100:103], a[8:11]\n\t"
    "v_add_f32 v134, v134, v186\n\t"
    "v_add_f32 v135, v135, v187\n\t"
    "s_waitcnt lgkmcnt(5)\n\t"
    "s_waitcnt lgkmcnt(4)\n\t"
    "v_add_f32 v188, v188, v189\n\t"
    "v_mfma_f32_16x16x32_bf16 a[8:11], a[120:123], v[104:107], a[8:11]\n\t"
    "buffer_load_dwordx4 a[188:191], v8, %0, s40 offen offset:3072\n\t"
    "s_waitcnt lgkmcnt(3)\n\t"
    "s_waitcnt lgkmcnt(2)\n\t"
    "v_add_f32 v190, v190, v191\n\t"
    "v_add_f32 v132, v132, v176\n\t"
    "v_add_f32 v133, v133, v177\n\t"
    "v_mfma_f32_16x16x32_bf16 a[12:15], a[124:127], v[88:91], 0\n\t"
    "v_add_f32 v134, v134, v178\n\t"
    "ds_write_b128 v26, a[8:11] offset:0\n\t"
    "v_add_f32 v135, v135, v179\n\t"
    "v_mfma_f32_16x16x32_bf16 a[12:15], a[128:131], v[92:95], a[12:15]\n\t"
    "v_add_f32 v188, v188, v190\n\t"
    "v_cvt_pk_bf16_f32 v212, v132, v133\n\t"
    "v_cvt_pk_bf16_f32 v213, v134, v135\n\t"
    "v_mfma_f32_16x16x32_bf16 a[12:15], a[132:135], v[96:99], a[12:15]\n\t"
    "v_add_f32 v145, v145, v188\n\t"
    "v_min_u32 v212, -2, v212\n\t"
    "buffer_store_dwordx2 v[212:213], v16, %0, s43 offen sc1\n\t"
    "v_mfma_f32_16x16x32_bf16 a[12:15], a[136:139], v[100:103], a[12:15]\n\t"
    "s_add_u32 s40, s40, 4096\n\t"
    "buffer_load_dwordx4 a[192:195], v8, %0, s40 offen offset:0\n\t"
    "v_mov_b32 v214, v145\n\t"
    "v_mov_b32 v215, v145\n\t"
    "s_nop 1\n\t"
    "v_permlane16_swap_b32 v214, v215\n\t"
    "v_mfma_f32_16x16x32_bf16 a[12:15], a[140:143], v[104:107], a[12:15]\n\t"
    "v_cvt_pk_bf16_f32 v216, v214, v215\n\t"
    "v_mov_b32 v217, v216\n\t"
    "s_nop 1\n\t"
    "v_permlane32_swap_b32 v216, v217\n\t"
    "v_mfma_f32_16x16x32_bf16 v[136:139], a[144:147], v[88:91], v[136:139]\n\t"
    "buffer_load_dwordx4 a[196:199], v8, %0, s40 offen offset:1024\n\t"
    "v_min_u32 v216, -2, v216\n\t"
    "ds_write_b128 v28, a[12:15] offset:0\n\t"
    "buffer_store_dwordx2 v[216:217], v17, %0, s43 offen sc1\n\t"
    "v_mfma_f32_16x16x32_bf16 v[136:139], a[148:151], v[92:95], v[136:139]\n\t"
    "v_mfma_f32_16x16x32_bf16 v[136:139], a[152:155], v[96:99], v[136:139]\n\t"
    "v_mfma_f32_16x16x32_bf16 v[136:139], a[156:159], v[100:103], v[136:139]\n\t"
    "v_mfma_f32_16x16x32_bf16 v[136:139], a[160:163], v[104:107], v[136:139]\n\t"
    "s_waitcnt lgkmcnt(0)\n\t"
    "s_barrier\n\t"
    "s_waitcnt vmcnt(17)\n\t"
    "s_waitcnt vmcnt(16)\n\t"
    "s_waitcnt vmcnt(15)\n\t"
    "v_max3_u32 v227, v108, v112, v116\n\t"
    "s_waitcnt vmcnt(14)\n\t"
    "s_waitcnt vmcnt(13)\n\t"
    "v_max3_u32 v227, v227, v120, v124\n\t"
    "v_cmp_eq_u32 vcc, -1, v227\n\t"
    "s_cbranch_vccnz H7_%=\n\t"
    "R8_%=:\n\t"
    "v_mfma_f32_16x16x32_bf16 a[0:3], a[64:67], v[108:111], 0\n\t"
    "buffer_load_dwordx4 a[200:203], v8, %0, s40 offen offset:2048\n\t"
    "ds_read_b128 v[176:179], v27 offset:0\n\t"
    "ds_read_b128 v[180:183], v27 offset:1024\n\t"
    "ds_read_b128 v[184:187], v27 offset:2048\n\t"
    "v_mfma_f32_16x16x32_bf16 a[0:3], a[68:71], v[112:115], a[0:3]\n\t"
    "buffer_load_dwordx4 a[204:207], v8, %0, s40 offen offset:3072\n\t"
    "ds_read_b32 v188, v29 offset:0\n\t"
    "ds_read_b32 v189, v29 offset:1024\n\t"
    "ds_read_b32 v190, v29 offset:2048\n\t"
    "v_mfma_f32_16x16x32_bf16 a[0:3], a[72:75], v[116:119], a[0:3]\n\t"
    "ds_read_b32 v191, v29 offset:3072\n\t"
    "v_mfma_f32_16x16x32_bf16 a[0:3], a[76:79], v[120:123], a[0:3]\n\t"
    "v_mfma_f32_16x16x32_bf16 a[0:3], a[80:83], v[124:127], a[0:3]\n\t"
    "s_add_u32 s40, s40, 4096\n\t"
    "buffer_load_dwordx4 a[208:211], v8, %0, s40 offen offset:0\n\t"
    "v_mfma_f32_16x16x32_bf16 a[4:7], a[84:87], v[108:111], 0\n\t"
    "v_mfma_f32_16x16x32_bf16 a[4:7], a[88:91], v[112:115], a[4:7]\n\t"
    "v_mfma_f32_16x16x32_bf16 a[4:7], a[92:95], v[116:119], a[4:7]\n\t"
    "v_mfma_f32_16x16x32_bf16 a[4:7], a[96:99], v[120:123], a[4:7]\n\t"
    "v_mfma_f32_16x16x32_bf16 a[4:7], a[100:103], v[124:127], a[4:7]\n\t"
    "buffer_load_dwordx4 a[212:215], v8, %0, s40 offen offset:1024\n\t"
    "ds_write_b128 v24, a[0:3] offset:16384\n\t"
    "s_add_u32 s43, %11, 0x16cd800\n\t"
    "s_waitcnt lgkmcnt(7)\n\t"
    "s_waitcnt lgkmcnt(6)\n\t"
    "v_add_f32 v176, v176, v180\n\t"
    "v_mfma_f32_16x16x32_bf16 a[8:11], a[104:107], v[108:111], 0\n\t"
    "v_add_f32 v177, v177, v181\n\t"
    "ds_write_b128 v25, a[4:7] offset:16384\n\t"
    "v_add_f32 v178, v178, v182\n\t"
    "v_mfma_f32_16x16x32_bf16 a[8:11], a[108:111], v[112:115], a[8:11]\n\t"
    "v_add_f32 v179, v179, v183\n\t"
    "s_waitcnt lgkmcnt(6)\n\t"
    "v_add_f32 v136, v136, v184\n\t"
    "v_add_f32 v137, v137, v185\n\t"
    "v_mfma_f32_16x16x32_bf16 a[8:11], a[112:115], v[116:119], a[8:11]\n\t"
    "v_add_f32 v138, v138, v186\n\t"
    "v_add_f32 v139, v139, v187\n\t"
    "s_waitcnt lgkmcnt(5)\n\t"
    "s_waitcnt lgkmcnt(4)\n\t"
    "v_add_f32 v188, v188, v189\n\t"
    "v_mfma_f32_16x16x32_bf16 a[8:11], a[116:119], v[120:123], a[8:11]\n\t"
    "buffer_load_dwordx4 a[216:219], v8, %0, s40 offen offset:2048\n\t"
    "s_waitcnt lgkmcnt(3)\n\t"
    "s_waitcnt lgkmcnt(2)\n\t"
    "v_add_f32 v190, v190, v191\n\t"
    "v_add_f32 v136, v136, v176\n\t"
    "v_add_f32 v137, v137, v177\n\t"
    "v_mfma_f32_16x16x32_bf16 a[8:11], a[120:123], v[124:127], a[8:11]\n\t"
    "v_add_f32 v138, v138, v178\n\t"
    "v_add_f32 v139, v139, v179\n\t"
    "v_add_f32 v188, v188, v190\n\t"
    "v_mfma_f32_16x16x32_bf16 a[12:15], a[124:127], v[108:111], 0\n\t"
    "v_cvt_pk_bf16_f32 v212, v136, v137\n\t"
    "v_cvt_pk_bf16_f32 v213, v138, v139\n\t"
    "v_add_f32 v146, v146, v188\n\t"
    "v_mfma_f32_16x16x32_bf16 a[12:15], a[128:131], v[112:115], a[12:15]\n\t"
    "buffer_load_dwordx4 a[220:223], v8, %0, s40 offen offset:3072\n\t"
    "ds_write_b128 v26, a[8:11] offset:16384\n\t"
    "v_min_u32 v212, -2, v212\n\t"
    "buffer_store_dwordx2 v[212:213], v16, %0, s43 offen sc1\n\t"
    "v_mfma_f32_16x16x32_bf16 a[12:15], a[132:135], v[116:119], a[12:15]\n\t"
    "v_mov_b32 v214, v146\n\t"
    "v_mov_b32 v215, v146\n\t"
    "s_nop 1\n\t"
    "v_permlane16_swap_b32 v214, v215\n\t"
    "v_mfma_f32_16x16x32_bf16 a[12:15], a[136:139], v[120:123], a[12:15]\n\t"
    "v_cvt_pk_bf16_f32 v216, v214, v215\n\t"
    "v_mov_b32 v217, v216\n\t"
    "s_nop 1\n\t"
    "v_permlane32_swap_b32 v216, v217\n\t"
    "v_mfma_f32_16x16x32_bf16 a[12:15], a[140:143], v[124:127], a[12:15]\n\t"
    "s_add_u32 s40, s40, 4096\n\t"
    "buffer_load_dwordx4 a[224:227], v8, %0, s40 offen offset:0\n\t"
    "v_min_u32 v216, -2, v216\n\t"
    "buffer_store_dwordx2 v[216:217], v17, %0, s43 offen sc1\n\t"
    "v_mfma_f32_16x16x32_bf16 v[140:143], a[144:147], v[108:111], v[140:143]\n\t"
    "v_mfma_f32_16x16x32_bf16 v[140:143], a[148:151], v[112:115], v[140:143]\n\t"
    "v_mfma_f32_16x16x32_bf16 v[140:143], a[152:155], v[116:119], v[140:143]\n\t"
    "v_mfma_f32_16x16x32_bf16 v[140:143], a[156:159], v[120:123], v[140:143]\n\t"
    "ds_write_b128 v28, a[12:15] offset:16384\n\t"
    "v_mfma_f32_16x16x32_bf16 v[140:143], a[160:163], v[124:127], v[140:143]\n\t"
    "buffer_load_dwordx4 a[228:231], v8, %0, s40 offen offset:1024\n\t"
    "s_waitcnt lgkmcnt(0)\n\t"
    "s_barrier\n\t"
    "s_add_u32 s42, %8, 0x16c3800\n\t"
    "buffer_load_dwordx4 v[48:51], v8, %0, s42 offen offset:0 sc1\n\t"
    "ds_read_b128 v[176:179], v27 offset:16384\n\t"
    "ds_read_b128 v[180:183], v27 offset:17408\n\t"
    "ds_read_b128 v[184:187], v27 offset:18432\n\t"
    "ds_read_b32 v188, v29 offset:16384\n\t"
    "ds_read_b32 v189, v29 offset:17408\n\t"
    "ds_read_b32 v190, v29 offset:18432\n\t"
    "ds_read_b32 v191, v29 offset:19456\n\t"
    "s_add_u32 s43, %11, 0x16d2800\n\t"
    "s_waitcnt lgkmcnt(6)\n\t"
    "s_waitcnt lgkmcnt(5)\n\t"
    "v_add_f32 v176, v176, v180\n\t"
    "buffer_load_dwordx4 v[52:55], v8, %0, s42 offen offset:1024 sc1\n\t"
    "v_add_f32 v177, v177, v181\n\t"
    "v_add_f32 v178, v178, v182\n\t"
    "v_add_f32 v179, v179, v183\n\t"
    "s_waitcnt lgkmcnt(4)\n\t"
    "v_add_f32 v140, v140, v184\n\t"
    "v_add_f32 v141, v141, v185\n\t"
    "v_add_f32 v142, v142, v186\n\t"
    "v_add_f32 v143, v143, v187\n\t"
    "buffer_load_dwordx4 v[56:59], v8, %0, s42 offen offset:2048 sc1\n\t"
    "s_waitcnt lgkmcnt(3)\n\t"
    "s_waitcnt lgkmcnt(2)\n\t"
    "v_add_f32 v188, v188, v189\n\t"
    "s_waitcnt lgkmcnt(1)\n\t"
    "s_waitcnt lgkmcnt(0)\n\t"
    "v_add_f32 v190, v190, v191\n\t"
    "v_add_f32 v140, v140, v176\n\t"
    "v_add_f32 v141, v141, v177\n\t"
    "v_add_f32 v142, v142, v178\n\t"
    "v_add_f32 v143, v143, v179\n\t"
    "v_add_f32 v188, v188, v190\n\t"
    "v_cvt_pk_bf16_f32 v212, v140, v141\n\t"
    "v_cvt_pk_bf16_f32 v213, v142, v143\n\t"
    "v_add_f32 v147, v147, v188\n\t"
    "v_min_u32 v212, -2, v212\n\t"
    "buffer_store_dwordx2 v[212:213], v16, %0, s43 offen sc1\n\t"
    "v_mov_b32 v214, v147\n\t"
    "v_mov_b32 v215, v147\n\t"
    "s_nop 1\n\t"
    "v_permlane16_swap_b32 v214, v215\n\t"
    "v_cvt_pk_bf16_f32 v216, v214, v215\n\t"
    "buffer_load_dwordx4 v[60:63], v8, %0, s42 offen offset:3072 sc1\n\t"
    "v_mov_b32 v217, v216\n\t"
    "s_nop 1\n\t"
    "v_permlane32_swap_b32 v216, v217\n\t"
    "v_min_u32 v216, -2, v216\n\t"
    "buffer_store_dwordx2 v[216:217], v17, %0, s43 offen sc1\n\t"
    "ds_read_b128 v[164:167], v32 offset:0\n\t"
    "ds_read_b128 v[172:175], v32 offset:1280\n\t"
    "ds_read_b32 v168, v33 offset:0\n\t"
    "ds_read_b32 v169, v33 offset:1280\n\t"
    "s_waitcnt lgkmcnt(3)\n\t"
    "s_waitcnt lgkmcnt(2)\n\t"
    "v_add_f32 v164, v164, v172\n\t"
    "buffer_load_dwordx4 v[64:67], v9, %0, s42 offen offset:0 sc1\n\t"
    "v_add_f32 v165, v165, v173\n\t"
    "v_add_f32 v166, v166, v174\n\t"
    "buffer_load_dwordx4 a[232:235], v8, %0, s40 offen offset:2048\n\t"
    "v_add_f32 v167, v167, v175\n\t"
    "s_waitcnt lgkmcnt(1)\n\t"
    "s_waitcnt lgkmcnt(0)\n\t"
    "v_add_f32 v168, v168, v169\n\t"
    "s_waitcnt vmcnt(7)\n\t"
    "s_waitcnt vmcnt(6)\n\t"
    "s_waitcnt vmcnt(5)\n\t"
    "v_max3_u32 v227, v48, v52, v56\n\t"
    "s_waitcnt vmcnt(3)\n\t"
    "s_waitcnt vmcnt(1)\n\t"
    "v_max3_u32 v227, v227, v60, v64\n\t"
    "v_max3_u32 v227, v227, v50, v54\n\t"
    "v_max3_u32 v227, v227, v58, v62\n\t"
    "v_max_u32 v227, v227, v66\n\t"
    "v_cmp_eq_u32 vcc, -1, v227\n\t"
    "s_cbranch_vccnz H9_%=\n\t"
    "R10_%=:\n\t"
    "v_mfma_f32_16x16x32_bf16 v[200:203], v[48:51], v[48:51], 0\n\t"
    "buffer_load_dwordx4 a[236:239], v8, %0, s40 offen offset:3072\n\t"
    "s_add_u32 s40, s40, 4096\n\t"
    "buffer_load_dwordx4 a[240:243], v8, %0, s40 offen offset:0\n\t"
    "v_mfma_f32_16x16x32_bf16 v[204:207], v[208:211], v[48:51], 0\n\t"
    "buffer_load_dwordx4 a[244:247], v8, %0, s40 offen offset:1024\n\t"
    "buffer_load_dwordx4 a[248:251], v8, %0, s40 offen offset:2048\n\t"
    "v_mfma_f32_16x16x32_bf16 v[200:203], v[52:55], v[52:55], v[200:203]\n\t"
    "buffer_load_dwordx4 a[252:255], v8, %0, s40 offen offset:3072\n\t"
    "v_mfma_f32_16x16x32_bf16 v[204:207], v[208:211], v[52:55], v[204:207]\n\t"
    "s_add_u32 s40, s40, 4096\n\t"
    "buffer_load_dwordx4 v[228:231], v8, %0, s40 offen offset:0\n\t"
    "v_mfma_f32_16x16x32_bf16 v[200:203], v[56:59], v[56:59], v[200:203]\n\t"
    "buffer_load_dwordx4 v[232:235], v8, %0, s40 offen offset:1024\n\t"
    "v_mfma_f32_16x16x32_bf16 v[204:207], v[208:211], v[56:59], v[204:207]\n\t"
    "s_add_u32 s41, %8, 0x16c8800\n\t"
    "buffer_load_dwordx4 v[68:71], v8, %0, s41 offen offset:0 sc1\n\t"
    "v_mfma_f32_16x16x32_bf16 v[200:203], v[60:63], v[60:63], v[200:203]\n\t"
    "v_mfma_f32_16x16x32_bf16 v[204:207], v[208:211], v[60:63], v[204:207]\n\t"
    "v_mfma_f32_16x16x32_bf16 v[200:203], v[64:67], v[64:67], v[200:203]\n\t"
    "v_mfma_f32_16x16x32_bf16 v[204:207], v[208:211], v[64:67], v[204:207]\n\t"
    "v_mfma_f32_16x16x32_bf16 a[0:3], a[164:167], v[48:51], 0\n\t"
    "v_mfma_f32_16x16x32_bf16 a[0:3], a[168:171], v[52:55], a[0:3]\n\t"
    "v_mfma_f32_16x16x32_bf16 a[0:3], a[172:175], v[56:59], a[0:3]\n\t"
    "v_mfma_f32_16x16x32_bf16 a[0:3], a[176:179], v[60:63], a[0:3]\n\t"
    "v_mfma_f32_16x16x32_bf16 a[0:3], a[180:183], v[64:67], a[0:3]\n\t"
    "v_mfma_f32_16x16x32_bf16 a[4:7], a[184:187], v[48:51], 0\n\t"
    "v_mfma_f32_16x16x32_bf16 a[4:7], a[188:191], v[52:55], a[4:7]\n\t"
    "v_mfma_f32_16x16x32_bf16 a[4:7], a[192:195], v[56:59], a[4:7]\n\t"
    "v_cndmask_b32_e64 v205, v200, v201, s[48:49]\n\t"
    "v_cndmask_b32_e64 v205, v205, v202, s[50:51]\n\t"
    "v_mfma_f32_16x16x32_bf16 a[4:7], a[196:199], v[60:63], a[4:7]\n\t"
    "v_cndmask_b32_e64 v205, v205, v203, s[52:53]\n\t"
    "v_mfma_f32_16x16x32_bf16 a[4:7], a[200:203], v[64:67], a[4:7]\n\t"
    "ds_write_b64 v30, v[204:205] offset:0\n\t"
    "ds_write_b128 v24, a[0:3] offset:0\n\t"
    "v_mfma_f32_16x16x32_bf16 a[8:11], a[204:207], v[48:51], 0\n\t"
    "v_mfma_f32_16x16x32_bf16 a[8:11], a[208:211], v[52:55], a[8:11]\n\t"
    "v_mfma_f32_16x16x32_bf16 a[8:11], a[212:215], v[56:59], a[8:11]\n\t"
    "v_mfma_f32_16x16x32_bf16 a[8:11], a[216:219], v[60:63], a[8:11]\n\t"
    "v_mfma_f32_16x16x32_bf16 a[8:11], a[220:223], v[64:67], a[8:11]\n\t"
    "v_mfma_f32_16x16x32_bf16 a[12:15], a[224:227], v[48:51], 0\n\t"
    "ds_write_b128 v25, a[4:7] offset:0\n\t"
    "v_mfma_f32_16x16x32_bf16 a[12:15], a[228:231], v[52:55], a[12:15]\n\t"
    "s_waitcnt vmcnt(8)\n\t"
    "v_mfma_f32_16x16x32_bf16 a[12:15], a[232:235], v[56:59], a[12:15]\n\t"
    "s_waitcnt vmcnt(7)\n\t"
    "v_mfma_f32_16x16x32_bf16 a[12:15], a[236:239], v[60:63], a[12:15]\n\t"
    "buffer_load_dwordx4 v[72:75], v8, %0, s41 offen offset:1024 sc1\n\t"
    "ds_write_b128 v26, a[8:11] offset:0\n\t"
    "s_waitcnt vmcnt(7)\n\t"
    "v_mfma_f32_16x16x32_bf16 a[12:15], a[240:243], v[64:67], a[12:15]\n\t"
    "buffer_load_dwordx4 v[76:79], v8, %0, s41 offen offset:2048 sc1\n\t"
    "s_waitcnt vmcnt(7)\n\t"
    "v_mfma_f32_16x16x32_bf16 v[148:151], a[244:247], v[48:51], 0\n\t"
    "buffer_load_dwordx4 v[80:83], v8, %0, s41 offen offset:3072 sc1\n\t"
    "s_waitcnt vmcnt(7)\n\t"
    "v_mfma_f32_16x16x32_bf16 v[148:151], a[248:251], v[52:55], v[148:151]\n\t"
    "buffer_load_dwordx4 v[84:87], v9, %0, s41 offen offset:0 sc1\n\t"
    "s_waitcnt vmcnt(7)\n\t"
    "v_mfma_f32_16x16x32_bf16 v[148:151], a[252:255], v[56:59], v[148:151]\n\t"
    "s_add_u32 s42, %8, 0x16cd800\n\t"
    "buffer_load_dwordx4 v[88:91], v8, %0, s42 offen offset:0 sc1\n\t"
    "ds_write_b128 v28, a[12:15] offset:0\n\t"
    "s_waitcnt vmcnt(7)\n\t"
    "v_mfma_f32_16x16x32_bf16 v[148:151], v[228:231], v[60:63], v[148:151]\n\t"
    "buffer_load_dwordx4 v[92:95], v8, %0, s42 offen offset:1024 sc1\n\t"
    "s_waitcnt vmcnt(7)\n\t"
    "v_mfma_f32_16x16x32_bf16 v[148:151], v[232:235], v[64:67], v[148:151]\n\t"
    "buffer_load_dwordx4 v[96:99], v8, %0, s42 offen offset:2048 sc1\n\t"
    "s_waitcnt lgkmcnt(0)\n\t"
    "s_barrier\n\t"
    "ds_read_b128 v[176:179], v27 offset:0\n\t"
    "ds_read_b128 v[180:183], v27 offset:1024\n\t"
    "ds_read_b128 v[184:187], v27 offset:2048\n\t"
    "ds_read_b32 v188, v29 offset:0\n\t"
    "ds_read_b32 v189, v29 offset:1024\n\t"
    "ds_read_b32 v190, v29 offset:2048\n\t"
    "ds_read_b32 v191, v29 offset:3072\n\t"
    "buffer_load_dwordx4 v[100:103], v8, %0, s42 offen offset:3072 sc1\n\t"
    "ds_read_b128 v[192:195], v31 offset:0\n\t"
    "ds_read_b128 v[196:199], v31 offset:16\n\t"
    "s_waitcnt vmcnt(8)\n\t"
    "s_waitcnt vmcnt(7)\n\t"
    "s_waitcnt vmcnt(6)\n\t"
    "v_max3_u32 v227, v68, v72, v76\n\t"
    "s_waitcnt vmcnt(5)\n\t"
    "s_waitcnt vmcnt(4)\n\t"
    "v_max3_u32 v227, v227, v80, v84\n\t"
    "v_max3_u32 v227, v227, v70, v74\n\t"
    "v_max3_u32 v227, v227, v78, v82\n\t"
    "v_max_u32 v227, v227, v86\n\t"
    "v_cmp_eq_u32 vcc, -1, v227\n\t"
    "s_cbranch_vccnz H11_%=\n\t"
    "R12_%=:\n\t"
    "v_mfma_f32_16x16x32_bf16 v[200:203], v[68:71], v[68:71], 0\n\t"
    "buffer_load_dwordx4 v[104:107], v9, %0, s42 offen offset:0 sc1\n\t"
    "s_waitcnt lgkmcnt(1)\n\t"
    "v_add_f32 v192, v192, v194\n\t"
    "v_add_f32 v193, v193, v195\n\t"
    "s_waitcnt lgkmcnt(0)\n\t"
    "v_add_f32 v196, v196, v198\n\t"
    "v_mfma_f32_16x16x32_bf16 v[204:207], v[208:211], v[68:71], 0\n\t"
    "s_add_u32 s41, %8, 0x16d2800\n\t"
    "buffer_load_dwordx4 v[108:111], v8, %0, s41 offen offset:0 sc1\n\t"
    "v_add_f32 v197, v197, v199\n\t"
    "v_add_f32 v192, v192, v196\n\t"
    "v_add_f32 v193, v193, v197\n\t"
    "v_mfma_f32_16x16x32_bf16 v[200:203], v[72:75], v[72:75], v[200:203]\n\t"
    "buffer_load_dwordx4 v[112:115], v8, %0, s41 offen offset:1024 sc1\n\t"
    "v_mul_f32 v222, 0xbacccccd, v192\n\t"
    "v_mul_f32 v223, 0x3acccccd, v193\n\t"
    "v_fma_f32 v223, -v222, v222, v223\n\t"
    "v_mfma_f32_16x16x32_bf16 v[204:207], v[208:211], v[72:75], v[204:207]\n\t"
    "buffer_load_dwordx4 v[116:119], v8, %0, s41 offen offset:2048 sc1\n\t"
    "v_max_f32 v223, 0, v223\n\t"
    "v_add_f32 v226, 0x3727c5ac, v223\n\t"
    "v_rsq_f32 v156, v226\n\t"
    "v_mfma_f32_16x16x32_bf16 v[200:203], v[76:79], v[76:79], v[200:203]\n\t"
    "buffer_load_dwordx4 v[120:123], v8, %0, s41 offen offset:3072 sc1\n\t"
    "v_add_f32 v176, v176, v180\n\t"
    "v_add_f32 v177, v177, v181\n\t"
    "v_mul_f32 v157, v222, v156\n\t"
    "v_mfma_f32_16x16x32_bf16 v[204:207], v[208:211], v[76:79], v[204:207]\n\t"
    "v_mul_f32 v223, v223, v156\n\t"
    "v_add_f32 v178, v178, v182\n\t"
    "v_mul_f32 v223, v223, v156\n\t"
    "v_mfma_f32_16x16x32_bf16 v[200:203], v[80:83], v[80:83], v[200:203]\n\t"
    "buffer_load_dwordx4 v[124:127], v9, %0, s41 offen offset:0 sc1\n\t"
    "v_add_f32 v179, v179, v183\n\t"
    "v_add_f32 v223, 0x3727c5ac, v223\n\t"
    "v_add_f32 v148, v148, v184\n\t"
    "v_mfma_f32_16x16x32_bf16 v[204:207], v[208:211], v[80:83], v[204:207]\n\t"
    "v_rsq_f32 v223, v223\n\t"
    "v_add_f32 v149, v149, v185\n\t"
    "v_add_f32 v150, v150, v186\n\t"
    "v_mfma_f32_16x16x32_bf16 v[200:203], v[84:87], v[84:87], v[200:203]\n\t"
    "v_mul_f32 v224, v156, v223\n\t"
    "v_add_f32 v151, v151, v187\n\t"
    "v_mul_f32 v225, v222, v224\n\t"
    "v_mfma_f32_16x16x32_bf16 v[204:207], v[208:211], v[84:87], v[204:207]\n\t"
    "buffer_load_dwordx4 v[236:239], v8, %0, s40 offen offset:2048\n\t"
    "v_add_f32 v188, v188, v189\n\t"
    "v_add_f32 v190, v190, v191\n\t"
    "v_add_f32 v148, v148, v176\n\t"
    "v_mfma_f32_16x16x32_bf16 a[0:3], a[164:167], v[68:71], 0\n\t"
    "v_add_f32 v149, v149, v177\n\t"
    "v_add_f32 v150, v150, v178\n\t"
    "v_add_f32 v151, v151, v179\n\t"
    "v_mfma_f32_16x16x32_bf16 a[0:3], a[168:171], v[72:75], a[0:3]\n\t"
    "v_cndmask_b32_e64 v205, v200, v201, s[48:49]\n\t"
    "v_cndmask_b32_e64 v205, v205, v202, s[50:51]\n\t"
    "v_cndmask_b32_e64 v205, v205, v203, s[52:53]\n\t"
    "v_mfma_f32_16x16x32_bf16 a[0:3], a[172:175], v[76:79], a[0:3]\n\t"
    "ds_write_b64 v30, v[204:205] offset:512\n\t"
    "v_add_f32 v188, v188, v190\n\t"
    "v_mul_f32 v212, v164, v225\n\t"
    "v_mfma_f32_16x16x32_bf16 a[0:3], a[176:179], v[80:83], a[0:3]\n\t"
    "buffer_load_dwordx4 v[240:243], v8, %0, s40 offen offset:3072\n\t"
    "v_mul_f32 v213, v165, v225\n\t"
    "v_mul_f32 v214, v166, v225\n\t"
    "v_mul_f32 v215, v167, v225\n\t"
    "v_mfma_f32_16x16x32_bf16 a[0:3], a[180:183], v[84:87], a[0:3]\n\t"
    "v_mul_f32 v216, v168, v225\n\t"
    "v_fma_f32 v148, v148, v224, v212\n\t"
    "v_fma_f32 v149, v149, v224, v213\n\t"
    "v_mfma_f32_16x16x32_bf16 a[4:7], a[184:187], v[68:71], 0\n\t"
    "v_fma_f32 v150, v150, v224, v214\n\t"
    "v_fma_f32 v151, v151, v224, v215\n\t"
    "v_fma_f32 v188, v188, v224, v216\n\t"
    "v_mfma_f32_16x16x32_bf16 a[4:7], a[188:191], v[72:75], a[4:7]\n\t"
    "ds_write_b128 v24, a[0:3] offset:16384\n\t"
    "v_mul_f32 v212, v148, v148\n\t"
    "v_mul_f32 v213, v149, v149\n\t"
    "v_mfma_f32_16x16x32_bf16 a[4:7], a[192:195], v[76:79], a[4:7]\n\t"
    "v_mul_f32 v214, v150, v150\n\t"
    "v_mul_f32 v215, v151, v151\n\t"
    "v_mul_f32 v216, v188, v188\n\t"
    "v_mfma_f32_16x16x32_bf16 a[4:7], a[196:199], v[80:83], a[4:7]\n\t"
    "v_fma_f32 v212, v212, v11, v10\n\t"
    "v_fma_f32 v213, v213, v11, v10\n\t"
    "v_fma_f32 v214, v214, v11, v10\n\t"
    "v_mfma_f32_16x16x32_bf16 a[4:7], a[200:203], v[84:87], a[4:7]\n\t"
    "v_fma_f32 v215, v215, v11, v10\n\t"
    "v_fma_f32 v216, v216, v11, v10\n\t"
    "v_mul_f32 v212, v212, v148\n\t"
    "v_mfma_f32_16x16x32_bf16 a[8:11], a[204:207], v[68:71], 0\n\t"
    "v_mul_f32 v213, v213, v149\n\t"
    "v_mul_f32 v214, v214, v150\n\t"
    "v_mul_f32 v215, v215, v151\n\t"
    "v_mfma_f32_16x16x32_bf16 a[8:11], a[208:211], v[72:75], a[8:11]\n\t"
    "ds_write_b128 v25, a[4:7] offset:16384\n\t"
    "v_mul_f32 v216, v216, v188\n\t"
    "v_exp_f32 v212, v212\n\t"
    "v_mfma_f32_16x16x32_bf16 a[8:11], a[212:215], v[76:79], a[8:11]\n\t"
    "v_exp_f32 v213, v213\n\t"
    "v_exp_f32 v214, v214\n\t"
    "v_exp_f32 v215, v215\n\t"
    "v_mfma_f32_16x16x32_bf16 a[8:11], a[216:219], v[80:83], a[8:11]\n\t"
    "v_exp_f32 v216, v216\n\t"
    "v_add_f32 v212, 1.0, v212\n\t"
    "v_add_f32 v213, 1.0, v213\n\t"
    "v_mfma_f32_16x16x32_bf16 a[8:11], a[220:223], v[84:87], a[8:11]\n\t"
    "v_add_f32 v214, 1.0, v214\n\t"
    "v_add_f32 v215, 1.0, v215\n\t"
    "v_add_f32 v216, 1.0, v216\n\t"
    "v_mfma_f32_16x16x32_bf16 a[12:15], a[224:227], v[68:71], 0\n\t"
    "v_rcp_f32 v212, v212\n\t"
    "v_rcp_f32 v213, v213\n\t"
    "v_rcp_f32 v214, v214\n\t"
    "v_mfma_f32_16x16x32_bf16 a[12:15], a[228:231], v[72:75], a[12:15]\n\t"
    "ds_write_b128 v26, a[8:11] offset:16384\n\t"
    "v_rcp_f32 v215, v215\n\t"
    "v_rcp_f32 v216, v216\n\t"
    "v_mfma_f32_16x16x32_bf16 a[12:15], a[232:235], v[76:79], a[12:15]\n\t"
    "v_mul_f32 v148, v148, v212\n\t"
    "v_mul_f32 v149, v149, v213\n\t"
    "v_mul_f32 v150, v150, v214\n\t"
    "v_mfma_f32_16x16x32_bf16 a[12:15], a[236:239], v[80:83], a[12:15]\n\t"
    "v_mul_f32 v151, v151, v215\n\t"
    "v_mul_f32 v188, v188, v216\n\t"
    "v_cvt_pk_bf16_f32 v212, v148, v149\n\t"
    "v_mfma_f32_16x16x32_bf16 a[12:15], a[240:243], v[84:87], a[12:15]\n\t"
    "v_cvt_pk_bf16_f32 v213, v150, v151\n\t"
    "s_add_u32 s43, %11, 0x11b9800\n\t"
    "v_min_u32 v212, -2, v212\n\t"
    "v_mfma_f32_16x16x32_bf16 v[152:155], a[244:247], v[68:71], 0\n\t"
    "buffer_store_dwordx2 v[212:213], v20, %0, s43 offen sc1\n\t"
    "v_mov_b32 v214, v188\n\t"
    "v_mov_b32 v215, v188\n\t"
    "v_mfma_f32_16x16x32_bf16 v[152:155], a[248:251], v[72:75], v[152:155]\n\t"
    "s_add_u32 s40, s40, 4096\n\t"
    "buffer_load_dwordx4 v[244:247], v8, %0, s40 offen offset:0\n\t"
    "ds_write_b128 v28, a[12:15] offset:16384\n\t"
    "v_permlane16_swap_b32 v214, v215\n\t"
    "v_cvt_pk_bf16_f32 v216, v214, v215\n\t"
    "v_mfma_f32_16x16x32_bf16 v[152:155], a[252:255], v[76:79], v[152:155]\n\t"
    "v_mov_b32 v217, v216\n\t"
    "s_nop 1\n\t"
    "v_permlane32_swap_b32 v216, v217\n\t"
    "v_min_u32 v216, -2, v216\n\t"
    "v_mfma_f32_16x16x32_bf16 v[152:155], v[228:231], v[80:83], v[152:155]\n\t"
    "buffer_store_dwordx2 v[216:217], v21, %0, s43 offen sc1\n\t"
    "v_mfma_f32_16x16x32_bf16 v[152:155], v[232:235], v[84:87], v[152:155]\n\t"
    "s_waitcnt lgkmcnt(0)\n\t"
    "s_barrier\n\t"
    "s_waitcnt vmcnt(14)\n\t"
    "s_waitcnt vmcnt(13)\n\t"
    "s_waitcnt vmcnt(12)\n\t"
    "v_max3_u32 v227, v88, v92, v96\n\t"
    "s_waitcnt vmcnt(11)\n\t"
    "s_waitcnt vmcnt(10)\n\t"
    "v_max3_u32 v227, v227, v100, v104\n\t"
    "v_max3_u32 v227, v227, v90, v94\n\t"
    "v_max3_u32 v227, v227, v98, v102\n\t"
    "v_max_u32 v227, v227, v106\n\t"
    "v_cmp_eq_u32 vcc, -1, v227\n\t"
    "s_cbranch_vccnz H13_%=\n\t"
    "R14_%=:\n\t"
    "v_mfma_f32_16x16x32_bf16 v[200:203], v[88:91], v[88:91], 0\n\t"
    "buffer_load_dwordx4 v[248:251], v8, %0, s40 offen offset:1024\n\t"
    "ds_read_b128 v[176:179], v27 offset:16384\n\t"
    "ds_read_b128 v[180:183], v27 offset:17408\n\t"
    "ds_read_b128 v[184:187], v27 offset:18432\n\t"
    "v_mfma_f32_16x16x32_bf16 v[204:207], v[208:211], v[88:91], 0\n\t"
    "buffer_load_dwordx4 v[252:255], v8, %0, s40 offen offset:2048\n\t"
    "ds_read_b32 v188, v29 offset:16384\n\t"
    "ds_read_b32 v189, v29 offset:17408\n\t"
    "ds_read_b32 v190, v29 offset:18432\n\t"
    "v_mfma_f32_16x16x32_bf16 v[200:203], v[92:95], v[92:95], v[200:203]\n\t"
    "ds_read_b32 v191, v29 offset:19456\n\t"
    "ds_read_b128 v[192:195], v31 offset:512\n\t"
    "ds_read_b128 v[196:199], v31 offset:528\n\t"
    "v_mfma_f32_16x16x32_bf16 v[204:207], v[208:211], v[92:95], v[204:207]\n\t"
    "buffer_load_dwordx4 a[64:67], v8, %0, s40 offen offset:3072\n\t"
    "v_mfma_f32_16x16x32_bf16 v[200:203], v[96:99], v[96:99], v[200:203]\n\t"
    "v_mfma_f32_16x16x32_bf16 v[204:207], v[208:211], v[96:99], v[204:207]\n\t"
    "v_mfma_f32_16x16x32_bf16 v[200:203], v[100:103], v[100:103], v[200:203]\n\t"
    "v_mfma_f32_16x16x32_bf16 v[204:207], v[208:211], v[100:103], v[204:207]\n\t"
    "s_add_u32 s40, s40, 4096\n\t"
    "buffer_load_dwordx4 a[68:71], v8, %0, s40 offen offset:0\n\t"
    "v_mfma_f32_16x16x32_bf16 v[200:203], v[104:107], v[104:107], v[200:203]\n\t"
    "v_mfma_f32_16x16x32_bf16 v[204:207], v[208:211], v[104:107], v[204:207]\n\t"
    "v_mfma_f32_16x16x32_bf16 a[0:3], a[164:167], v[88:91], 0\n\t"
    "v_mfma_f32_16x16x32_bf16 a[0:3], a[168:171], v[92:95], a[0:3]\n\t"
    "s_waitcnt lgkmcnt(1)\n\t"
    "v_add_f32 v192, v192, v194\n\t"
    "v_add_f32 v193, v193, v195\n\t"
    "s_waitcnt lgkmcnt(0)\n\t"
    "v_add_f32 v196, v196, v198\n\t"
    "v_mfma_f32_16x16x32_bf16 a[0:3], a[172:175], v[96:99], a[0:3]\n\t"
    "buffer_load_dwordx4 a[72:75], v8, %0, s40 offen offset:1024\n\t"
    "v_cndmask_b32_e64 v205, v200, v201, s[48:49]\n\t"
    "v_cndmask_b32_e64 v205, v205, v202, s[50:51]\n\t"
    "v_cndmask_b32_e64 v205, v205, v203, s[52:53]\n\t"
    "v_mfma_f32_16x16x32_bf16 a[0:3], a[176:179], v[100:103], a[0:3]\n\t"
    "ds_write_b64 v30, v[204:205] offset:0\n\t"
    "v_add_f32 v197, v197, v199\n\t"
    "v_add_f32 v192, v192, v196\n\t"
    "v_mfma_f32_16x16x32_bf16 a[0:3], a[180:183], v[104:107], a[0:3]\n\t"
    "v_add_f32 v193, v193, v197\n\t"
    "v_mul_f32 v222, 0xbacccccd, v192\n\t"
    "v_mul_f32 v223, 0x3acccccd, v193\n\t"
    "v_mfma_f32_16x16x32_bf16 a[4:7], a[184:187], v[88:91], 0\n\t"
    "v_fma_f32 v223, -v222, v222, v223\n\t"
    "v_max_f32 v223, 0, v223\n\t"
    "v_add_f32 v226, 0x3727c5ac, v223\n\t"
    "v_mfma_f32_16x16x32_bf16 a[4:7], a[188:191], v[92:95], a[4:7]\n\t"
    "buffer_load_dwordx4 a[76:79], v8, %0, s40 offen offset:2048\n\t"
    "ds_write_b128 v24, a[0:3] offset:0\n\t"
    "v_rsq_f32 v158, v226\n\t"
    "v_add_f32 v176, v176, v180\n\t"
    "v_mfma_f32_16x16x32_bf16 a[4:7], a[192:195], v[96:99], a[4:7]\n\t"
    "v_add_f32 v177, v177, v181\n\t"
    "v_mul_f32 v159, v222, v158\n\t"
    "v_mul_f32 v223, v223, v158\n\t"
    "v_mfma_f32_16x16x32_bf16 a[4:7], a[196:199], v[100:103], a[4:7]\n\t"
    "v_add_f32 v178, v178, v182\n\t"
    "v_mul_f32 v223, v223, v158\n\t"
    "v_add_f32 v179, v179, v183\n\t"
    "v_mfma_f32_16x16x32_bf16 a[4:7], a[200:203], v[104:107], a[4:7]\n\t"
    "v_add_f32 v223, 0x3727c5ac, v223\n\t"
    "v_add_f32 v152, v152, v184\n\t"
    "v_rsq_f32 v223, v223\n\t"
    "v_mfma_f32_16x16x32_bf16 a[8:11], a[204:207], v[88:91], 0\n\t"
    "buffer_load_dwordx4 a[80:83], v8, %0, s40 offen offset:3072\n\t"
    "v_add_f32 v153, v153, v185\n\t"
    "v_add_f32 v154, v154, v186\n\t"
    "v_mul_f32 v224, v158, v223\n\t"
    "v_mfma_f32_16x16x32_bf16 a[8:11], a[208:211], v[92:95], a[8:11]\n\t"
    "ds_write_b128 v25, a[4:7] offset:0\n\t"
    "v_add_f32 v155, v155, v187\n\t"
    "v_mul_f32 v225, v222, v224\n\t"
    "v_mfma_f32_16x16x32_bf16 a[8:11], a[212:215], v[96:99], a[8:11]\n\t"
    "v_add_f32 v188, v188, v189\n\t"
    "v_add_f32 v190, v190, v191\n\t"
    "v_add_f32 v152, v152, v176\n\t"
    "v_mfma_f32_16x16x32_bf16 a[8:11], a[216:219], v[100:103], a[8:11]\n\t"
    "s_add_u32 s40, s40, 4096\n\t"
    "buffer_load_dwordx4 a[84:87], v8, %0, s40 offen offset:0\n\t"
    "v_add_f32 v153, v153, v177\n\t"
    "v_add_f32 v154, v154, v178\n\t"
    "v_add_f32 v155, v155, v179\n\t"
    "v_mfma_f32_16x16x32_bf16 a[8:11], a[220:223], v[104:107], a[8:11]\n\t"
    "v_add_f32 v188, v188, v190\n\t"
    "v_mul_f32 v212, v164, v225\n\t"
    "v_mul_f32 v213, v165, v225\n\t"
    "v_mfma_f32_16x16x32_bf16 a[12:15], a[224:227], v[88:91], 0\n\t"
    "v_mul_f32 v214, v166, v225\n\t"
    "v_mul_f32 v215, v167, v225\n\t"
    "v_mul_f32 v216, v168, v225\n\t"
    "v_mfma_f32_16x16x32_bf16 a[12:15], a[228:231], v[92:95], a[12:15]\n\t"
    "ds_write_b128 v26, a[8:11] offset:0\n\t"
    "v_fma_f32 v152, v152, v224, v212\n\t"
    "v_fma_f32 v153, v153, v224, v213\n\t"
    "v_mfma_f32_16x16x32_bf16 a[12:15], a[232:235], v[96:99], a[12:15]\n\t"
    "v_fma_f32 v154, v154, v224, v214\n\t"
    "v_fma_f32 v155, v155, v224, v215\n\t"
    "v_fma_f32 v188, v188, v224, v216\n\t"
    "v_mfma_f32_16x16x32_bf16 a[12:15], a[236:239], v[100:103], a[12:15]\n\t"
    "v_mul_f32 v212, v152, v152\n\t"
    "v_mul_f32 v213, v153, v153\n\t"
    "v_mul_f32 v214, v154, v154\n\t"
    "v_mfma_f32_16x16x32_bf16 a[12:15], a[240:243], v[104:107], a[12:15]\n\t"
    "v_mul_f32 v215, v155, v155\n\t"
    "v_mul_f32 v216, v188, v188\n\t"
    "v_fma_f32 v212, v212, v11, v10\n\t"
    "v_mfma_f32_16x16x32_bf16 v[148:151], a[244:247], v[88:91], 0\n\t"
    "buffer_load_dwordx4 a[88:91], v8, %0, s40 offen offset:1024\n\t"
    "v_fma_f32 v213, v213, v11, v10\n\t"
    "v_fma_f32 v214, v214, v11, v10\n\t"
    "v_fma_f32 v215, v215, v11, v10\n\t"
    "v_mfma_f32_16x16x32_bf16 v[148:151], a[248:251], v[92:95], v[148:151]\n\t"
    "ds_write_b128 v28, a[12:15] offset:0\n\t"
    "v_fma_f32 v216, v216, v11, v10\n\t"
    "v_mul_f32 v212, v212, v152\n\t"
    "v_mfma_f32_16x16x32_bf16 v[148:151], a[252:255], v[96:99], v[148:151]\n\t"
    "v_mul_f32 v213, v213, v153\n\t"
    "v_mul_f32 v214, v214, v154\n\t"
    "v_mul_f32 v215, v215, v155\n\t"
    "v_mfma_f32_16x16x32_bf16 v[148:151], v[228:231], v[100:103], v[148:151]\n\t"
    "v_mul_f32 v216, v216, v188\n\t"
    "v_exp_f32 v212, v212\n\t"
    "v_exp_f32 v213, v213\n\t"
    "v_mfma_f32_16x16x32_bf16 v[148:151], v[232:235], v[104:107], v[148:151]\n\t"
    "buffer_load_dwordx4 a[92:95], v8, %0, s40 offen offset:2048\n\t"
    "v_exp_f32 v214, v214\n\t"
    "v_exp_f32 v215, v215\n\t"
    "v_exp_f32 v216, v216\n\t"
    "v_add_f32 v212, 1.0, v212\n\t"
    "v_add_f32 v213, 1.0, v213\n\t"
    "v_add_f32 v214, 1.0, v214\n\t"
    "v_add_f32 v215, 1.0, v215\n\t"
    "v_add_f32 v216, 1.0, v216\n\t"
    "v_rcp_f32 v212, v212\n\t"
    "v_rcp_f32 v213, v213\n\t"
    "v_rcp_f32 v214, v214\n\t"
    "v_rcp_f32 v215, v215\n\t"
    "v_rcp_f32 v216, v216\n\t"
    "v_mul_f32 v152, v152, v212\n\t"
    "v_mul_f32 v153, v153, v213\n\t"
    "v_mul_f32 v154, v154, v214\n\t"
    "v_mul_f32 v155, v155, v215\n\t"
    "v_mul_f32 v188, v188, v216\n\t"
    "v_cvt_pk_bf16_f32 v212, v152, v153\n\t"
    "v_cvt_pk_bf16_f32 v213, v154, v155\n\t"
    "s_add_u32 s43, %11, 0x11be800\n\t"
    "v_min_u32 v212, -2, v212\n\t"
    "buffer_store_dwordx2 v[212:213], v20, %0, s43 offen sc1\n\t"
    "v_mov_b32 v214, v188\n\t"
    "v_mov_b32 v215, v188\n\t"
    "s_nop 1\n\t"
    "v_permlane16_swap_b32 v214, v215\n\t"
    "v_cvt_pk_bf16_f32 v216, v214, v215\n\t"
    "v_mov_b32 v217, v216\n\t"
    "s_nop 1\n\t"
    "v_permlane32_swap_b32 v216, v217\n\t"
    "v_min_u32 v216, -2, v216\n\t"
    "buffer_store_dwordx2 v[216:217], v21, %0, s43 offen sc1\n\t"
    "s_waitcnt lgkmcnt(0)\n\t"
    "s_barrier\n\t"
    "s_waitcnt vmcnt(21)\n\t"
    "s_waitcnt vmcnt(20)\n\t"
    "s_waitcnt vmcnt(19)\n\t"
    "v_max3_u32 v227, v108, v112, v116\n\t"
    "s_waitcnt vmcnt(18)\n\t"
    "s_waitcnt vmcnt(17)\n\t"
    "v_max3_u32 v227, v227, v120, v124\n\t"
    "v_max3_u32 v227, v227, v110, v114\n\t"
    "v_max3_u32 v227, v227, v118, v122\n\t"
    "v_max_u32 v227, v227, v126\n\t"
    "v_cmp_eq_u32 vcc, -1, v227\n\t"
    "s_cbranch_vccnz H15_%=\n\t"
    "R16_%=:\n\t"
    "v_mfma_f32_16x16x32_bf16 v[200:203], v[108:111], v[108:111], 0\n\t"
    "buffer_load_dwordx4 a[96:99], v8, %0, s40 offen offset:3072\n\t"
    "ds_read_b128 v[176:179], v27 offset:0\n\t"
    "ds_read_b128 v[180:183], v27 offset:1024\n\t"
    "ds_read_b128 v[184:187], v27 offset:2048\n\t"
    "v_mfma_f32_16x16x32_bf16 v[204:207], v[208:211], v[108:111], 0\n\t"
    "s_add_u32 s40, s40, 4096\n\t"
    "buffer_load_dwordx4 a[100:103], v8, %0, s40 offen offset:0\n\t"
    "ds_read_b32 v188, v29 offset:0\n\t"
    "ds_read_b32 v189, v29 offset:1024\n\t"
    "ds_read_b32 v190, v29 offset:2048\n\t"
    "v_mfma_f32_16x16x32_bf16 v[200:203], v[112:115], v[112:115], v[200:203]\n\t"
    "buffer_load_dwordx4 a[104:107], v8, %0, s40 offen offset:1024\n\t"
    "ds_read_b32 v191, v29 offset:3072\n\t"
    "ds_read_b128 v[192:195], v31 offset:0\n\t"
    "ds_read_b128 v[196:199], v31 offset:16\n\t"
    "v_mfma_f32_16x16x32_bf16 v[204:207], v[208:211], v[112:115], v[204:207]\n\t"
    "buffer_load_dwordx4 a[108:111], v8, %0, s40 offen offset:2048\n\t"
    "v_mfma_f32_16x16x32_bf16 v[200:203], v[116:119], v[116:119], v[200:203]\n\t"
    "v_mfma_f32_16x16x32_bf16 v[204:207], v[208:211], v[116:119], v[204:207]\n\t"
    "v_mfma_f32_16x16x32_bf16 v[200:203], v[120:123], v[120:123], v[200:203]\n\t"
    "v_mfma_f32_16x16x32_bf16 v[204:207], v[208:211], v[120:123], v[204:207]\n\t"
    "buffer_load_dwordx4 a[112:115], v8, %0, s40 offen offset:3072\n\t"
    "v_mfma_f32_16x16x32_bf16 v[200:203], v[124:127], v[124:127], v[200:203]\n\t"
    "v_mfma_f32_16x16x32_bf16 v[204:207], v[208:211], v[124:127], v[204:207]\n\t"
    "v_mfma_f32_16x16x32_bf16 a[0:3], a[164:167], v[108:111], 0\n\t"
    "v_mfma_f32_16x16x32_bf16 a[0:3], a[168:171], v[112:115], a[0:3]\n\t"
    "s_waitcnt lgkmcnt(1)\n\t"
    "v_add_f32 v192, v192, v194\n\t"
    "v_add_f32 v193, v193, v195\n\t"
    "s_waitcnt lgkmcnt(0)\n\t"
    "v_add_f32 v196, v196, v198\n\t"
    "v_mfma_f32_16x16x32_bf16 a[0:3], a[172:175], v[116:119], a[0:3]\n\t"
    "s_add_u32 s40, s40, 4096\n\t"
    "buffer_load_dwordx4 a[116:119], v8, %0, s40 offen offset:0\n\t"
    "v_cndmask_b32_e64 v205, v200, v201, s[48:49]\n\t"
    "v_cndmask_b32_e64 v205, v205, v202, s[50:51]\n\t"
    "v_cndmask_b32_e64 v205, v205, v203, s[52:53]\n\t"
    "v_mfma_f32_16x16x32_bf16 a[0:3], a[176:179], v[120:123], a[0:3]\n\t"
    "ds_write_b64 v30, v[204:205] offset:512\n\t"
    "v_add_f32 v197, v197, v199\n\t"
    "v_add_f32 v192, v192, v196\n\t"
    "v_mfma_f32_16x16x32_bf16 a[0:3], a[180:183], v[124:127], a[0:3]\n\t"
    "v_add_f32 v193, v193, v197\n\t"
    "v_mul_f32 v222, 0xbacccccd, v192\n\t"
    "v_mul_f32 v223, 0x3acccccd, v193\n\t"
    "v_mfma_f32_16x16x32_bf16 a[4:7], a[184:187], v[108:111], 0\n\t"
    "buffer_load_dwordx4 a[120:123], v8, %0, s40 offen offset:1024\n\t"
    "v_fma_f32 v223, -v222, v222, v223\n\t"
    "v_max_f32 v223, 0, v223\n\t"
    "v_add_f32 v226, 0x3727c5ac, v223\n\t"
    "v_mfma_f32_16x16x32_bf16 a[4:7], a[188:191], v[112:115], a[4:7]\n\t"
    "ds_write_b128 v24, a[0:3] offset:16384\n\t"
    "v_rsq_f32 v160, v226\n\t"
    "v_add_f32 v176, v176, v180\n\t"
    "v_mfma_f32_16x16x32_bf16 a[4:7], a[192:195], v[116:119], a[4:7]\n\t"
    "v_add_f32 v177, v177, v181\n\t"
    "v_mul_f32 v161, v222, v160\n\t"
    "v_mul_f32 v223, v223, v160\n\t"
    "v_mfma_f32_16x16x32_bf16 a[4:7], a[196:199], v[120:123], a[4:7]\n\t"
    "v_add_f32 v178, v178, v182\n\t"
    "v_mul_f32 v223, v223, v160\n\t"
    "v_add_f32 v179, v179, v183\n\t"
    "v_mfma_f32_16x16x32_bf16 a[4:7], a[200:203], v[124:127], a[4:7]\n\t"
    "buffer_load_dwordx4 a[124:127], v8, %0, s40 offen offset:2048\n\t"
    "v_add_f32 v223, 0x3727c5ac, v223\n\t"
    "v_add_f32 v148, v148, v184\n\t"
    "v_rsq_f32 v223, v223\n\t"
    "v_mfma_f32_16x16x32_bf16 a[8:11], a[204:207], v[108:111], 0\n\t"
    "v_add_f32 v149, v149, v185\n\t"
    "v_add_f32 v150, v150, v186\n\t"
    "v_mul_f32 v224, v160, v223\n\t"
    "v_mfma_f32_16x16x32_bf16 a[8:11], a[208:211], v[112:115], a[8:11]\n\t"
    "ds_write_b128 v25, a[4:7] offset:16384\n\t"
    "v_add_f32 v151, v151, v187\n\t"
    "v_mul_f32 v225, v222, v224\n\t"
    "v_mfma_f32_16x16x32_bf16 a[8:11], a[212:215], v[116:119], a[8:11]\n\t"
    "v_add_f32 v188, v188, v189\n\t"
    "v_add_f32 v190, v190, v191\n\t"
    "v_add_f32 v148, v148, v176\n\t"
    "v_mfma_f32_16x16x32_bf16 a[8:11], a[216:219], v[120:123], a[8:11]\n\t"
    "v_add_f32 v149, v149, v177\n\t"
    "v_add_f32 v150, v150, v178\n\t"
    "v_add_f32 v151, v151, v179\n\t"
    "v_mfma_f32_16x16x32_bf16 a[8:11], a[220:223], v[124:127], a[8:11]\n\t"
    "v_add_f32 v188, v188, v190\n\t"
    "v_mul_f32 v212, v164, v225\n\t"
    "v_mul_f32 v213, v165, v225\n\t"
    "v_mfma_f32_16x16x32_bf16 a[12:15], a[224:227], v[108:111], 0\n\t"
    "v_mul_f32 v214, v166, v225\n\t"
    "v_mul_f32 v215, v167, v225\n\t"
    "v_mul_f32 v216, v168, v225\n\t"
    "v_mfma_f32_16x16x32_bf16 a[12:15], a[228:231], v[112:115], a[12:15]\n\t"
    "ds_write_b128 v26, a[8:11] offset:16384\n\t"
    "v_fma_f32 v148, v148, v224, v212\n\t"
    "v_fma_f32 v149, v149, v224, v213\n\t"
    "v_mfma_f32_16x16x32_bf16 a[12:15], a[232:235], v[116:119], a[12:15]\n\t"
    "v_fma_f32 v150, v150, v224, v214\n\t"
    "v_fma_f32 v151, v151, v224, v215\n\t"
    "v_fma_f32 v188, v188, v224, v216\n\t"
    "v_mfma_f32_16x16x32_bf16 a[12:15], a[236:239], v[120:123], a[12:15]\n\t"
    "v_mul_f32 v212, v148, v148\n\t"
    "v_mul_f32 v213, v149, v149\n\t"
    "v_mul_f32 v214, v150, v150\n\t"
    "v_mfma_f32_16x16x32_bf16 a[12:15], a[240:243], v[124:127], a[12:15]\n\t"
    "v_mul_f32 v215, v151, v151\n\t"
    "v_mul_f32 v216, v188, v188\n\t"
    "v_fma_f32 v212, v212, v11, v10\n\t"
    "v_mfma_f32_16x16x32_bf16 v[152:155], a[244:247], v[108:111], 0\n\t"
    "buffer_load_dwordx4 a[128:131], v8, %0, s40 offen offset:3072\n\t"
    "v_fma_f32 v213, v213, v11, v10\n\t"
    "v_fma_f32 v214, v214, v11, v10\n\t"
    "v_fma_f32 v215, v215, v11, v10\n\t"
    "v_mfma_f32_16x16x32_bf16 v[152:155], a[248:251], v[112:115], v[152:155]\n\t"
    "ds_write_b128 v28, a[12:15] offset:16384\n\t"
    "v_fma_f32 v216, v216, v11, v10\n\t"
    "v_mul_f32 v212, v212, v148\n\t"
    "v_mfma_f32_16x16x32_bf16 v[152:155], a[252:255], v[116:119], v[152:155]\n\t"
    "v_mul_f32 v213, v213, v149\n\t"
    "v_mul_f32 v214, v214, v150\n\t"
    "v_mul_f32 v215, v215, v151\n\t"
    "v_mfma_f32_16x16x32_bf16 v[152:155], v[228:231], v[120:123], v[152:155]\n\t"
    "v_mul_f32 v216, v216, v188\n\t"
    "v_exp_f32 v212, v212\n\t"
    "v_exp_f32 v213, v213\n\t"
    "v_mfma_f32_16x16x32_bf16 v[152:155], v[232:235], v[124:127], v[152:155]\n\t"
    "s_add_u32 s40, s40, 4096\n\t"
    "buffer_load_dwordx4 a[132:135], v8, %0, s40 offen offset:0\n\t"
    "v_exp_f32 v214, v214\n\t"
    "v_exp_f32 v215, v215\n\t"
    "v_exp_f32 v216, v216\n\t"
    "v_add_f32 v212, 1.0, v212\n\t"
    "v_add_f32 v213, 1.0, v213\n\t"
    "v_add_f32 v214, 1.0, v214\n\t"
    "v_add_f32 v215, 1.0, v215\n\t"
    "v_add_f32 v216, 1.0, v216\n\t"
    "v_rcp_f32 v212, v212\n\t"
    "v_rcp_f32 v213, v213\n\t"
    "v_rcp_f32 v214, v214\n\t"
    "v_rcp_f32 v215, v215\n\t"
    "v_rcp_f32 v216, v216\n\t"
    "v_mul_f32 v148, v148, v212\n\t"
    "v_mul_f32 v149, v149, v213\n\t"
    "v_mul_f32 v150, v150, v214\n\t"
    "v_mul_f32 v151, v151, v215\n\t"
    "v_mul_f32 v188, v188, v216\n\t"
    "v_cvt_pk_bf16_f32 v212, v148, v149\n\t"
    "v_cvt_pk_bf16_f32 v213, v150, v151\n\t"
    "s_add_u32 s43, %11, 0x11c3800\n\t"
    "v_min_u32 v212, -2, v212\n\t"
    "buffer_store_dwordx2 v[212:213], v20, %0, s43 offen sc1\n\t"
    "v_mov_b32 v214, v188\n\t"
    "v_mov_b32 v215, v188\n\t"
    "s_nop 1\n\t"
    "v_permlane16_swap_b32 v214, v215\n\t"
    "v_cvt_pk_bf16_f32 v216, v214, v215\n\t"
    "v_mov_b32 v217, v216\n\t"
    "s_nop 1\n\t"
    "v_permlane32_swap_b32 v216, v217\n\t"
    "v_min_u32 v216, -2, v216\n\t"
    "buffer_store_dwordx2 v[216:217], v21, %0, s43 offen sc1\n\t"
    "s_waitcnt lgkmcnt(0)\n\t"
    "s_barrier\n\t"
    "buffer_load_dwordx4 a[136:139], v8, %0, s40 offen offset:1024\n\t"
    "buffer_load_dwordx4 a[140:143], v8, %0, s40 offen offset:2048\n\t"
    "buffer_load_dwordx4 a[144:147], v8, %0, s40 offen offset:3072\n\t"
    "ds_read_b128 v[176:179], v27 offset:16384\n\t"
    "ds_read_b128 v[180:183], v27 offset:17408\n\t"
    "ds_read_b128 v[184:187], v27 offset:18432\n\t"
    "ds_read_b32 v188, v29 offset:16384\n\t"
    "ds_read_b32 v189, v29 offset:17408\n\t"
    "ds_read_b32 v190, v29 offset:18432\n\t"
    "ds_read_b32 v191, v29 offset:19456\n\t"
    "ds_read_b128 v[192:195], v31 offset:512\n\t"
    "ds_read_b128 v[196:199], v31 offset:528\n\t"
    "s_waitcnt lgkmcnt(1)\n\t"
    "v_add_f32 v192, v192, v194\n\t"
    "s_add_u32 s40, s40, 4096\n\t"
    "buffer_load_dwordx4 a[148:151], v8, %0, s40 offen offset:0\n\t"
    "buffer_load_dwordx4 a[152:155], v8, %0, s40 offen offset:1024\n\t"
    "v_add_f32 v193, v193, v195\n\t"
    "s_waitcnt lgkmcnt(0)\n\t"
    "v_add_f32 v196, v196, v198\n\t"
    "v_add_f32 v197, v197, v199\n\t"
    "v_add_f32 v192, v192, v196\n\t"
    "v_add_f32 v193, v193, v197\n\t"
    "v_mul_f32 v222, 0xbacccccd, v192\n\t"
    "v_mul_f32 v223, 0x3acccccd, v193\n\t"
    "v_fma_f32 v223, -v222, v222, v223\n\t"
    "buffer_load_dwordx4 a[156:159], v8, %0, s40 offen offset:2048\n\t"
    "v_max_f32 v223, 0, v223\n\t"
    "v_add_f32 v226, 0x3727c5ac, v223\n\t"
    "v_rsq_f32 v162, v226\n\t"
    "v_add_f32 v176, v176, v180\n\t"
    "v_add_f32 v177, v177, v181\n\t"
    "v_mul_f32 v163, v222, v162\n\t"
    "v_mul_f32 v223, v223, v162\n\t"
    "v_add_f32 v178, v178, v182\n\t"
    "v_mul_f32 v223, v223, v162\n\t"
    "v_add_f32 v179, v179, v183\n\t"
    "v_add_f32 v223, 0x3727c5ac, v223\n\t"
    "v_add_f32 v152, v152, v184\n\t"
    "v_rsq_f32 v223, v223\n\t"
    "v_add_f32 v153, v153, v185\n\t"
    "v_add_f32 v154, v154, v186\n\t"
    "v_mul_f32 v224, v162, v223\n\t"
    "v_add_f32 v155, v155, v187\n\t"
    "buffer_load_dwordx4 a[160:163], v8, %0, s40 offen offset:3072\n\t"
    "v_mul_f32 v225, v222, v224\n\t"
    "v_add_f32 v188, v188, v189\n\t"
    "v_add_f32 v190, v190, v191\n\t"
    "v_add_f32 v152, v152, v176\n\t"
    "v_add_f32 v153, v153, v177\n\t"
    "v_add_f32 v154, v154, v178\n\t"
    "v_add_f32 v155, v155, v179\n\t"
    "v_add_f32 v188, v188, v190\n\t"
    "v_mul_f32 v212, v164, v225\n\t"
    "v_mul_f32 v213, v165, v225\n\t"
    "v_mul_f32 v214, v166, v225\n\t"
    "v_mul_f32 v215, v167, v225\n\t"
    "v_mul_f32 v216, v168, v225\n\t"
    "v_fma_f32 v152, v152, v224, v212\n\t"
    "v_fma_f32 v153, v153, v224, v213\n\t"
    "v_fma_f32 v154, v154, v224, v214\n\t"
    "v_fma_f32 v155, v155, v224, v215\n\t"
    "v_fma_f32 v188, v188, v224, v216\n\t"
    "v_mul_f32 v212, v152, v152\n\t"
    "s_add_u32 s40, s40, 4096\n\t"
    "buffer_load_dwordx4 a[164:167], v8, %0, s40 offen offset:0\n\t"
    "v_mul_f32 v213, v153, v153\n\t"
    "v_mul_f32 v214, v154, v154\n\t"
    "v_mul_f32 v215, v155, v155\n\t"
    "v_mul_f32 v216, v188, v188\n\t"
    "v_fma_f32 v212, v212, v11, v10\n\t"
    "v_fma_f32 v213, v213, v11, v10\n\t"
    "v_fma_f32 v214, v214, v11, v10\n\t"
    "v_fma_f32 v215, v215, v11, v10\n\t"
    "v_fma_f32 v216, v216, v11, v10\n\t"
    "v_mul_f32 v212, v212, v152\n\t"
    "v_mul_f32 v213, v213, v153\n\t"
    "v_mul_f32 v214, v214, v154\n\t"
    "v_mul_f32 v215, v215, v155\n\t"
    "v_mul_f32 v216, v216, v188\n\t"
    "v_exp_f32 v212, v212\n\t"
    "v_exp_f32 v213, v213\n\t"
    "v_exp_f32 v214, v214\n\t"
    "v_exp_f32 v215, v215\n\t"
    "v_exp_f32 v216, v216\n\t"
    "v_add_f32 v212, 1.0, v212\n\t"
    "v_add_f32 v213, 1.0, v213\n\t"
    "v_add_f32 v214, 1.0, v214\n\t"
    "v_add_f32 v215, 1.0, v215\n\t"
    "v_add_f32 v216, 1.0, v216\n\t"
    "v_rcp_f32 v212, v212\n\t"
    "v_rcp_f32 v213, v213\n\t"
    "v_rcp_f32 v214, v214\n\t"
    "v_rcp_f32 v215, v215\n\t"
    "v_rcp_f32 v216, v216\n\t"
    "v_mul_f32 v152, v152, v212\n\t"
    "v_mul_f32 v153, v153, v213\n\t"
    "v_mul_f32 v154, v154, v214\n\t"
    "v_mul_f32 v155, v155, v215\n\t"
    "v_mul_f32 v188, v188, v216\n\t"
    "v_cvt_pk_bf16_f32 v212, v152, v153\n\t"
    "v_cvt_pk_bf16_f32 v213, v154, v155\n\t"
    "s_add_u32 s43, %11, 0x11c8800\n\t"
    "v_min_u32 v212, -2, v212\n\t"
    "buffer_load_dwordx4 a[168:171], v8, %0, s40 offen offset:1024\n\t"
    "buffer_store_dwordx2 v[212:213], v20, %0, s43 offen sc1\n\t"
    "v_mov_b32 v214, v188\n\t"
    "v_mov_b32 v215, v188\n\t"
    "s_nop 1\n\t"
    "v_permlane16_swap_b32 v214, v215\n\t"
    "v_cvt_pk_bf16_f32 v216, v214, v215\n\t"
    "v_mov_b32 v217, v216\n\t"
    "s_nop 1\n\t"
    "v_permlane32_swap_b32 v216, v217\n\t"
    "v_min_u32 v216, -2, v216\n\t"
    "buffer_store_dwordx2 v[216:217], v21, %0, s43 offen sc1\n\t"
    "buffer_load_dwordx4 a[172:175], v8, %0, s40 offen offset:2048\n\t"
    "buffer_load_dwordx4 a[176:179], v8, %0, s40 offen offset:3072\n\t"
    "s_add_u32 s40, s40, 4096\n\t"
    "buffer_load_dwordx4 a[180:183], v8, %0, s40 offen offset:0\n\t"
    "buffer_load_dwordx4 a[184:187], v8, %0, s40 offen offset:1024\n\t"
    "buffer_load_dwordx4 a[188:191], v8, %0, s40 offen offset:2048\n\t"
    "buffer_load_dwordx4 a[192:195], v8, %0, s40 offen offset:3072\n\t"
    "s_add_u32 s40, s40, 4096\n\t"
    "buffer_load_dwordx4 a[196:199], v8, %0, s40 offen offset:0\n\t"
    "buffer_load_dwordx4 a[200:203], v8, %0, s40 offen offset:1024\n\t"
    "s_add_u32 s42, %8, 0x11c3800\n\t"
    "buffer_load_dwordx4 v[48:51], v8, %0, s42 offen offset:0 sc1\n\t"
    "buffer_load_dwordx4 v[52:55], v8, %0, s42 offen offset:1024 sc1\n\t"
    "buffer_load_dwordx4 v[56:59], v8, %0, s42 offen offset:2048 sc1\n\t"
    "buffer_load_dwordx4 v[60:63], v8, %0, s42 offen offset:3072 sc1\n\t"
    "buffer_load_dwordx4 v[64:67], v9, %0, s42 offen offset:0 sc1\n\t"
    "s_waitcnt vmcnt(4)\n\t"
    "s_waitcnt vmcnt(3)\n\t"
    "s_waitcnt vmcnt(2)\n\t"
    "v_max3_u32 v227, v48, v52, v56\n\t"
    "s_waitcnt vmcnt(1)\n\t"
    "s_waitcnt vmcnt(0)\n\t"
    "v_max3_u32 v227, v227, v60, v64\n\t"
    "v_max3_u32 v227, v227, v50, v54\n\t"
    "v_max3_u32 v227, v227, v58, v62\n\t"
    "v_max_u32 v227, v227, v66\n\t"
    "v_cmp_eq_u32 vcc, -1, v227\n\t"
    "s_cbranch_vccnz H17_%=\n\t"
    "R18_%=:\n\t"
    "v_mfma_f32_16x16x32_bf16 a[0:3], v[236:239], v[48:51], 0\n\t"
    "s_add_u32 s41, %8, 0x11c8800\n\t"
    "buffer_load_dwordx4 v[68:71], v8, %0, s41 offen offset:0 sc1\n\t"
    "buffer_load_dwordx4 v[72:75], v8, %0, s41 offen offset:1024 sc1\n\t"
    "v_fma_f32 v148, v132, v158, v159\n\t"
    "v_fma_f32 v149, v133, v158, v159\n\t"
    "v_mfma_f32_16x16x32_bf16 a[0:3], v[240:243], v[52:55], a[0:3]\n\t"
    "buffer_load_dwordx4 v[76:79], v8, %0, s41 offen offset:2048 sc1\n\t"
    "buffer_load_dwordx4 v[80:83], v8, %0, s41 offen offset:3072 sc1\n\t"
    "v_fma_f32 v150, v134, v158, v159\n\t"
    "v_fma_f32 v151, v135, v158, v159\n\t"
    "v_mfma_f32_16x16x32_bf16 a[0:3], v[244:247], v[56:59], a[0:3]\n\t"
    "buffer_load_dwordx4 v[84:87], v9, %0, s41 offen offset:0 sc1\n\t"
    "v_mfma_f32_16x16x32_bf16 a[0:3], v[248:251], v[60:63], a[0:3]\n\t"
    "s_add_u32 s42, %8, 0x11be800\n\t"
    "buffer_load_dwordx4 v[88:91], v8, %0, s42 offen offset:0 sc1\n\t"
    "v_mfma_f32_16x16x32_bf16 a[0:3], v[252:255], v[64:67], a[0:3]\n\t"
    "buffer_load_dwordx4 v[92:95], v8, %0, s42 offen offset:1024 sc1\n\t"
    "v_mfma_f32_16x16x32_bf16 a[4:7], a[64:67], v[48:51], 0\n\t"
    "buffer_load_dwordx4 v[96:99], v8, %0, s42 offen offset:2048 sc1\n\t"
    "v_mfma_f32_16x16x32_bf16 a[4:7], a[68:71], v[52:55], a[4:7]\n\t"
    "v_mfma_f32_16x16x32_bf16 a[4:7], a[72:75], v[56:59], a[4:7]\n\t"
    "v_mfma_f32_16x16x32_bf16 a[4:7], a[76:79], v[60:63], a[4:7]\n\t"
    "v_mfma_f32_16x16x32_bf16 a[4:7], a[80:83], v[64:67], a[4:7]\n\t"
    "v_mfma_f32_16x16x32_bf16 a[8:11], a[84:87], v[48:51], 0\n\t"
    "ds_write_b128 v24, a[0:3] offset:0\n\t"
    "v_mfma_f32_16x16x32_bf16 a[8:11], a[88:91], v[52:55], a[8:11]\n\t"
    "v_mfma_f32_16x16x32_bf16 a[8:11], a[92:95], v[56:59], a[8:11]\n\t"
    "v_mfma_f32_16x16x32_bf16 a[8:11], a[96:99], v[60:63], a[8:11]\n\t"
    "v_mfma_f32_16x16x32_bf16 a[8:11], a[100:103], v[64:67], a[8:11]\n\t"
    "v_mfma_f32_16x16x32_bf16 a[12:15], a[104:107], v[48:51], 0\n\t"
    "v_mfma_f32_16x16x32_bf16 a[12:15], a[108:111], v[52:55], a[12:15]\n\t"
    "ds_write_b128 v25, a[4:7] offset:0\n\t"
    "v_mfma_f32_16x16x32_bf16 a[12:15], a[112:115], v[56:59], a[12:15]\n\t"
    "v_mfma_f32_16x16x32_bf16 a[12:15], a[116:119], v[60:63], a[12:15]\n\t"
    "v_mfma_f32_16x16x32_bf16 a[12:15], a[120:123], v[64:67], a[12:15]\n\t"
    "v_mfma_f32_16x16x32_bf16 v[148:151], a[124:127], v[48:51], v[148:151]\n\t"
    "v_mfma_f32_16x16x32_bf16 v[148:151], a[128:131], v[52:55], v[148:151]\n\t"
    "ds_write_b128 v26, a[8:11] offset:0\n\t"
    "v_mfma_f32_16x16x32_bf16 v[148:151], a[132:135], v[56:59], v[148:151]\n\t"
    "v_mfma_f32_16x16x32_bf16 v[148:151], a[136:139], v[60:63], v[148:151]\n\t"
    "v_mfma_f32_16x16x32_bf16 v[148:151], a[140:143], v[64:67], v[148:151]\n\t"
    "s_nop 1\n\t"
    "ds_write_b128 v28, a[12:15] offset:0\n\t"
    "s_waitcnt lgkmcnt(0)\n\t"
    "s_barrier\n\t"
    "ds_read_b128 v[176:179], v27 offset:0\n\t"
    "ds_read_b128 v[180:183], v27 offset:1024\n\t"
    "ds_read_b128 v[184:187], v27 offset:2048\n\t"
    "ds_read_b32 v188, v29 offset:0\n\t"
    "ds_read_b32 v189, v29 offset:1024\n\t"
    "ds_read_b32 v190, v29 offset:2048\n\t"
    "ds_read_b32 v191, v29 offset:3072\n\t"
    "s_waitcnt vmcnt(7)\n\t"
    "s_waitcnt vmcnt(6)\n\t"
    "s_waitcnt vmcnt(5)\n\t"
    "v_max3_u32 v227, v68, v72, v76\n\t"
    "s_waitcnt vmcnt(4)\n\t"
    "s_waitcnt vmcnt(3)\n\t"
    "v_max3_u32 v227, v227, v80, v84\n\t"
    "v_max3_u32 v227, v227, v70, v74\n\t"
    "v_max3_u32 v227, v227, v78, v82\n\t"
    "v_max_u32 v227, v227, v86\n\t"
    "v_cmp_eq_u32 vcc, -1, v227\n\t"
    "s_cbranch_vccnz H19_%=\n\t"
    "R20_%=:\n\t"
    "v_mfma_f32_16x16x32_bf16 a[0:3], v[236:239], v[68:71], 0\n\t"
    "buffer_load_dwordx4 v[100:103], v8, %0, s42 offen offset:3072 sc1\n\t"
    "s_add_u32 s43, %11, 0x1948800\n\t"
    "s_waitcnt lgkmcnt(6)\n\t"
    "s_waitcnt lgkmcnt(5)\n\t"
    "v_add_f32 v176, v176, v180\n\t"
    "v_add_f32 v177, v177, v181\n\t"
    "v_mfma_f32_16x16x32_bf16 a[0:3], v[240:243], v[72:75], a[0:3]\n\t"
    "buffer_load_dwordx4 v[104:107], v9, %0, s42 offen offset:0 sc1\n\t"
    "v_add_f32 v178, v178, v182\n\t"
    "v_add_f32 v179, v179, v183\n\t"
    "s_waitcnt lgkmcnt(4)\n\t"
    "v_add_f32 v148, v148, v184\n\t"
    "v_mfma_f32_16x16x32_bf16 a[0:3], v[244:247], v[76:79], a[0:3]\n\t"
    "s_add_u32 s41, %8, 0x11cd800\n\t"
    "buffer_load_dwordx4 v[108:111], v8, %0, s41 offen offset:0 sc1\n\t"
    "v_add_f32 v149, v149, v185\n\t"
    "v_add_f32 v150, v150, v186\n\t"
    "v_add_f32 v151, v151, v187\n\t"
    "v_mfma_f32_16x16x32_bf16 a[0:3], v[248:251], v[80:83], a[0:3]\n\t"
    "buffer_load_dwordx4 v[112:115], v8, %0, s41 offen offset:1024 sc1\n\t"
    "s_waitcnt lgkmcnt(3)\n\t"
    "s_waitcnt lgkmcnt(2)\n\t"
    "v_add_f32 v188, v188, v189\n\t"
    "s_waitcnt lgkmcnt(1)\n\t"
    "s_waitcnt lgkmcnt(0)\n\t"
    "v_add_f32 v190, v190, v191\n\t"
    "v_add_f32 v148, v148, v176\n\t"
    "v_mfma_f32_16x16x32_bf16 a[0:3], v[252:255], v[84:87], a[0:3]\n\t"
    "buffer_load_dwordx4 v[116:119], v8, %0, s41 offen offset:2048 sc1\n\t"
    "v_add_f32 v149, v149, v177\n\t"
    "v_add_f32 v150, v150, v178\n\t"
    "v_add_f32 v151, v151, v179\n\t"
    "v_mfma_f32_16x16x32_bf16 a[4:7], a[64:67], v[68:71], 0\n\t"
    "buffer_load_dwordx4 v[120:123], v8, %0, s41 offen offset:3072 sc1\n\t"
    "v_add_f32 v188, v188, v190\n\t"
    "v_cvt_pk_bf16_f32 v212, v148, v149\n\t"
    "ds_write_b128 v24, a[0:3] offset:16384\n\t"
    "v_mfma_f32_16x16x32_bf16 a[4:7], a[68:71], v[72:75], a[4:7]\n\t"
    "buffer_load_dwordx4 v[124:127], v9, %0, s41 offen offset:0 sc1\n\t"
    "v_cvt_pk_bf16_f32 v213, v150, v151\n\t"
    "v_fma_f32 v189, v145, v158, v159\n\t"
    "v_add_f32 v188, v188, v189\n\t"
    "v_mfma_f32_16x16x32_bf16 a[4:7], a[72:75], v[76:79], a[4:7]\n\t"
    "v_min_u32 v212, -2, v212\n\t"
    "buffer_store_dwordx2 v[212:213], v16, %0, s43 offen sc1\n\t"
    "v_mov_b32 v214, v188\n\t"
    "v_mfma_f32_16x16x32_bf16 a[4:7], a[76:79], v[80:83], a[4:7]\n\t"
    "v_mov_b32 v215, v188\n\t"
    "s_nop 1\n\t"
    "v_permlane16_swap_b32 v214, v215\n\t"
    "v_cvt_pk_bf16_f32 v216, v214, v215\n\t"
    "v_mfma_f32_16x16x32_bf16 a[4:7], a[80:83], v[84:87], a[4:7]\n\t"
    "buffer_load_dwordx4 a[204:207], v8, %0, s40 offen offset:2048\n\t"
    "v_mov_b32 v217, v216\n\t"
    "s_nop 1\n\t"
    "v_permlane32_swap_b32 v216, v217\n\t"
    "v_min_u32 v216, -2, v216\n\t"
    "v_mfma_f32_16x16x32_bf16 a[8:11], a[84:87], v[68:71], 0\n\t"
    "buffer_store_dwordx2 v[216:217], v17, %0, s43 offen sc1\n\t"
    "ds_write_b128 v25, a[4:7] offset:16384\n\t"
    "v_add_f32 v132, v132, v148\n\t"
    "v_mfma_f32_16x16x32_bf16 a[8:11], a[88:91], v[72:75], a[8:11]\n\t"
    "v_add_f32 v133, v133, v149\n\t"
    "v_add_f32 v134, v134, v150\n\t"
    "v_add_f32 v135, v135, v151\n\t"
    "v_mfma_f32_16x16x32_bf16 a[8:11], a[92:95], v[76:79], a[8:11]\n\t"
    "v_add_f32 v145, v145, v188\n\t"
    "v_fma_f32 v152, v136, v160, v161\n\t"
    "v_fma_f32 v153, v137, v160, v161\n\t"
    "v_mfma_f32_16x16x32_bf16 a[8:11], a[96:99], v[80:83], a[8:11]\n\t"
    "v_fma_f32 v154, v138, v160, v161\n\t"
    "v_fma_f32 v155, v139, v160, v161\n\t"
    "v_mfma_f32_16x16x32_bf16 a[8:11], a[100:103], v[84:87], a[8:11]\n\t"
    "v_mfma_f32_16x16x32_bf16 a[12:15], a[104:107], v[68:71], 0\n\t"
    "v_mfma_f32_16x16x32_bf16 a[12:15], a[108:111], v[72:75], a[12:15]\n\t"
    "v_mfma_f32_16x16x32_bf16 a[12:15], a[112:115], v[76:79], a[12:15]\n\t"
    "v_mfma_f32_16x16x32_bf16 a[12:15], a[116:119], v[80:83], a[12:15]\n\t"
    "v_mfma_f32_16x16x32_bf16 a[12:15], a[120:123], v[84:87], a[12:15]\n\t"
    "v_mfma_f32_16x16x32_bf16 v[152:155], a[124:127], v[68:71], v[152:155]\n\t"
    "v_mfma_f32_16x16x32_bf16 v[152:155], a[128:131], v[72:75], v[152:155]\n\t"
    "v_mfma_f32_16x16x32_bf16 v[152:155], a[132:135], v[76:79], v[152:155]\n\t"
    "ds_write_b128 v26, a[8:11] offset:16384\n\t"
    "v_mfma_f32_16x16x32_bf16 v[152:155], a[136:139], v[80:83], v[152:155]\n\t"
    "v_mfma_f32_16x16x32_bf16 v[152:155], a[140:143], v[84:87], v[152:155]\n\t"
    "s_nop 1\n\t"
    "ds_write_b128 v28, a[12:15] offset:16384\n\t"
    "s_waitcnt lgkmcnt(0)\n\t"
    "s_barrier\n\t"
    "ds_read_b128 v[176:179], v27 offset:16384\n\t"
    "ds_read_b128 v[180:183], v27 offset:17408\n\t"
    "ds_read_b128 v[184:187], v27 offset:18432\n\t"
    "ds_read_b32 v188, v29 offset:16384\n\t"
    "ds_read_b32 v189, v29 offset:17408\n\t"
    "ds_read_b32 v190, v29 offset:18432\n\t"
    "ds_read_b32 v191, v29 offset:19456\n\t"
    "s_waitcnt vmcnt(12)\n\t"
    "s_waitcnt vmcnt(11)\n\t"
    "s_waitcnt vmcnt(10)\n\t"
    "v_max3_u32 v227, v88, v92, v96\n\t"
    "s_waitcnt vmcnt(9)\n\t"
    "s_waitcnt vmcnt(8)\n\t"
    "v_max3_u32 v227, v227, v100, v104\n\t"
    "v_max3_u32 v227, v227, v90, v94\n\t"
    "v_max3_u32 v227, v227, v98, v102\n\t"
    "v_max_u32 v227, v227, v106\n\t"
    "v_cmp_eq_u32 vcc, -1, v227\n\t"
    "s_cbranch_vccnz H21_%=\n\t"
    "R22_%=:\n\t"
    "v_mfma_f32_16x16x32_bf16 a[0:3], v[236:239], v[88:91], 0\n\t"
    "buffer_load_dwordx4 a[208:211], v8, %0, s40 offen offset:3072\n\t"
    "s_add_u32 s43, %11, 0x194d800\n\t"
    "s_waitcnt lgkmcnt(6)\n\t"
    "s_waitcnt lgkmcnt(5)\n\t"
    "v_add_f32 v176, v176, v180\n\t"
    "v_add_f32 v177, v177, v181\n\t"
    "v_mfma_f32_16x16x32_bf16 a[0:3], v[240:243], v[92:95], a[0:3]\n\t"
    "s_add_u32 s40, s40, 4096\n\t"
    "buffer_load_dwordx4 a[212:215], v8, %0, s40 offen offset:0\n\t"
    "v_add_f32 v178, v178, v182\n\t"
    "v_add_f32 v179, v179, v183\n\t"
    "s_waitcnt lgkmcnt(4)\n\t"
    "v_add_f32 v152, v152, v184\n\t"
    "v_mfma_f32_16x16x32_bf16 a[0:3], v[244:247], v[96:99], a[0:3]\n\t"
    "buffer_load_dwordx4 a[216:219], v8, %0, s40 offen offset:1024\n\t"
    "v_add_f32 v153, v153, v185\n\t"
    "v_add_f32 v154, v154, v186\n\t"
    "v_add_f32 v155, v155, v187\n\t"
    "v_mfma_f32_16x16x32_bf16 a[0:3], v[248:251], v[100:103], a[0:3]\n\t"
    "s_waitcnt lgkmcnt(3)\n\t"
    "s_waitcnt lgkmcnt(2)\n\t"
    "v_add_f32 v188, v188, v189\n\t"
    "s_waitcnt lgkmcnt(1)\n\t"
    "s_waitcnt lgkmcnt(0)\n\t"
    "v_add_f32 v190, v190, v191\n\t"
    "v_add_f32 v152, v152, v176\n\t"
    "v_mfma_f32_16x16x32_bf16 a[0:3], v[252:255], v[104:107], a[0:3]\n\t"
    "v_add_f32 v153, v153, v177\n\t"
    "v_add_f32 v154, v154, v178\n\t"
    "v_add_f32 v155, v155, v179\n\t"
    "v_mfma_f32_16x16x32_bf16 a[4:7], a[64:67], v[88:91], 0\n\t"
    "v_add_f32 v188, v188, v190\n\t"
    "v_cvt_pk_bf16_f32 v212, v152, v153\n\t"
    "v_cvt_pk_bf16_f32 v213, v154, v155\n\t"
    "v_mfma_f32_16x16x32_bf16 a[4:7], a[68:71], v[92:95], a[4:7]\n\t"
    "buffer_load_dwordx4 a[220:223], v8, %0, s40 offen offset:2048\n\t"
    "ds_write_b128 v24, a[0:3] offset:0\n\t"
    "v_fma_f32 v189, v146, v160, v161\n\t"
    "v_add_f32 v188, v188, v189\n\t"
    "v_mfma_f32_16x16x32_bf16 a[4:7], a[72:75], v[96:99], a[4:7]\n\t"
    "v_min_u32 v212, -2, v212\n\t"
    "buffer_store_dwordx2 v[212:213], v16, %0, s43 offen sc1\n\t"
    "v_mov_b32 v214, v188\n\t"
    "v_mfma_f32_16x16x32_bf16 a[4:7], a[76:79], v[100:103], a[4:7]\n\t"
    "v_mov_b32 v215, v188\n\t"
    "s_nop 1\n\t"
    "v_permlane16_swap_b32 v214, v215\n\t"
    "v_cvt_pk_bf16_f32 v216, v214, v215\n\t"
    "v_mfma_f32_16x16x32_bf16 a[4:7], a[80:83], v[104:107], a[4:7]\n\t"
    "buffer_load_dwordx4 a[224:227], v8, %0, s40 offen offset:3072\n\t"
    "v_mov_b32 v217, v216\n\t"
    "s_nop 1\n\t"
    "v_permlane32_swap_b32 v216, v217\n\t"
    "v_min_u32 v216, -2, v216\n\t"
    "v_mfma_f32_16x16x32_bf16 a[8:11], a[84:87], v[88:91], 0\n\t"
    "buffer_store_dwordx2 v[216:217], v17, %0, s43 offen sc1\n\t"
    "ds_write_b128 v25, a[4:7] offset:0\n\t"
    "v_add_f32 v136, v136, v152\n\t"
    "v_mfma_f32_16x16x32_bf16 a[8:11], a[88:91], v[92:95], a[8:11]\n\t"
    "v_add_f32 v137, v137, v153\n\t"
    "v_add_f32 v138, v138, v154\n\t"
    "v_add_f32 v139, v139, v155\n\t"
    "v_mfma_f32_16x16x32_bf16 a[8:11], a[92:95], v[96:99], a[8:11]\n\t"
    "v_add_f32 v146, v146, v188\n\t"
    "v_fma_f32 v148, v128, v156, v157\n\t"
    "v_fma_f32 v149, v129, v156, v157\n\t"
    "v_mfma_f32_16x16x32_bf16 a[8:11], a[96:99], v[100:103], a[8:11]\n\t"
    "s_add_u32 s40, s40, 4096\n\t"
    "buffer_load_dwordx4 a[228:231], v8, %0, s40 offen offset:0\n\t"
    "v_fma_f32 v150, v130, v156, v157\n\t"
    "v_fma_f32 v151, v131, v156, v157\n\t"
    "v_mfma_f32_16x16x32_bf16 a[8:11], a[100:103], v[104:107], a[8:11]\n\t"
    "v_mfma_f32_16x16x32_bf16 a[12:15], a[104:107], v[88:91], 0\n\t"
    "v_mfma_f32_16x16x32_bf16 a[12:15], a[108:111], v[92:95], a[12:15]\n\t"
    "v_mfma_f32_16x16x32_bf16 a[12:15], a[112:115], v[96:99], a[12:15]\n\t"
    "buffer_load_dwordx4 a[232:235], v8, %0, s40 offen offset:1024\n\t"
    "v_mfma_f32_16x16x32_bf16 a[12:15], a[116:119], v[100:103], a[12:15]\n\t"
    "v_mfma_f32_16x16x32_bf16 a[12:15], a[120:123], v[104:107], a[12:15]\n\t"
    "v_mfma_f32_16x16x32_bf16 v[148:151], a[124:127], v[88:91], v[148:151]\n\t"
    "v_mfma_f32_16x16x32_bf16 v[148:151], a[128:131], v[92:95], v[148:151]\n\t"
    "ds_write_b128 v26, a[8:11] offset:0\n\t"
    "v_mfma_f32_16x16x32_bf16 v[148:151], a[132:135], v[96:99], v[148:151]\n\t"
    "buffer_load_dwordx4 a[236:239], v8, %0, s40 offen offset:2048\n\t"
    "v_mfma_f32_16x16x32_bf16 v[148:151], a[136:139], v[100:103], v[148:151]\n\t"
    "v_mfma_f32_16x16x32_bf16 v[148:151], a[140:143], v[104:107], v[148:151]\n\t"
    "s_nop 0\n\t"
    "ds_write_b128 v28, a[12:15] offset:0\n\t"
    "s_waitcnt lgkmcnt(0)\n\t"
    "s_barrier\n\t"
    "s_waitcnt vmcnt(17)\n\t"
    "s_waitcnt vmcnt(16)\n\t"
    "s_waitcnt vmcnt(15)\n\t"
    "v_max3_u32 v227, v108, v112, v116\n\t"
    "s_waitcnt vmcnt(14)\n\t"
    "s_waitcnt vmcnt(13)\n\t"
    "v_max3_u32 v227, v227, v120, v124\n\t"
    "v_max3_u32 v227, v227, v110, v114\n\t"
    "v_max3_u32 v227, v227, v118, v122\n\t"
    "v_max_u32 v227, v227, v126\n\t"
    "v_cmp_eq_u32 vcc, -1, v227\n\t"
    "s_cbranch_vccnz H23_%=\n\t"
    "R24_%=:\n\t"
    "v_mfma_f32_16x16x32_bf16 a[0:3], v[236:239], v[108:111], 0\n\t"
    "buffer_load_dwordx4 a[240:243], v8, %0, s40 offen offset:3072\n\t"
    "ds_read_b128 v[176:179], v27 offset:0\n\t"
    "ds_read_b128 v[180:183], v27 offset:1024\n\t"
    "ds_read_b128 v[184:187], v27 offset:2048\n\t"
    "v_mfma_f32_16x16x32_bf16 a[0:3], v[240:243], v[112:115], a[0:3]\n\t"
    "s_add_u32 s40, s40, 4096\n\t"
    "buffer_load_dwordx4 a[244:247], v8, %0, s40 offen offset:0\n\t"
    "ds_read_b32 v188, v29 offset:0\n\t"
    "ds_read_b32 v189, v29 offset:1024\n\t"
    "ds_read_b32 v190, v29 offset:2048\n\t"
    "v_mfma_f32_16x16x32_bf16 a[0:3], v[244:247], v[116:119], a[0:3]\n\t"
    "ds_read_b32 v191, v29 offset:3072\n\t"
    "v_fma_f32 v152, v140, v162, v163\n\t"
    "v_fma_f32 v153, v141, v162, v163\n\t"
    "v_mfma_f32_16x16x32_bf16 a[0:3], v[248:251], v[120:123], a[0:3]\n\t"
    "v_fma_f32 v154, v142, v162, v163\n\t"
    "v_fma_f32 v155, v143, v162, v163\n\t"
    "v_mfma_f32_16x16x32_bf16 a[0:3], v[252:255], v[124:127], a[0:3]\n\t"
    "v_mfma_f32_16x16x32_bf16 a[4:7], a[64:67], v[108:111], 0\n\t"
    "s_add_u32 s42, %8, 0x1948800\n\t"
    "buffer_load_dwordx4 v[48:51], v8, %0, s42 offen offset:0 sc1\n\t"
    "v_mfma_f32_16x16x32_bf16 a[4:7], a[68:71], v[112:115], a[4:7]\n\t"
    "v_mfma_f32_16x16x32_bf16 a[4:7], a[72:75], v[116:119], a[4:7]\n\t"
    "v_mfma_f32_16x16x32_bf16 a[4:7], a[76:79], v[120:123], a[4:7]\n\t"
    "v_mfma_f32_16x16x32_bf16 a[4:7], a[80:83], v[124:127], a[4:7]\n\t"
    "s_add_u32 s43, %11, 0x1943800\n\t"
    "ds_write_b128 v24, a[0:3] offset:16384\n\t"
    "s_waitcnt lgkmcnt(7)\n\t"
    "s_waitcnt lgkmcnt(6)\n\t"
    "v_add_f32 v176, v176, v180\n\t"
    "v_mfma_f32_16x16x32_bf16 a[8:11], a[84:87], v[108:111], 0\n\t"
    "buffer_load_dwordx4 v[52:55], v8, %0, s42 offen offset:1024 sc1\n\t"
    "v_add_f32 v177, v177, v181\n\t"
    "ds_write_b128 v25, a[4:7] offset:16384\n\t"
    "v_add_f32 v178, v178, v182\n\t"
    "v_mfma_f32_16x16x32_bf16 a[8:11], a[88:91], v[112:115], a[8:11]\n\t"
    "v_add_f32 v179, v179, v183\n\t"
    "s_waitcnt lgkmcnt(6)\n\t"
    "v_add_f32 v148, v148, v184\n\t"
    "v_add_f32 v149, v149, v185\n\t"
    "v_mfma_f32_16x16x32_bf16 a[8:11], a[92:95], v[116:119], a[8:11]\n\t"
    "v_add_f32 v150, v150, v186\n\t"
    "v_add_f32 v151, v151, v187\n\t"
    "s_waitcnt lgkmcnt(5)\n\t"
    "s_waitcnt lgkmcnt(4)\n\t"
    "v_add_f32 v188, v188, v189\n\t"
    "v_mfma_f32_16x16x32_bf16 a[8:11], a[96:99], v[120:123], a[8:11]\n\t"
    "s_waitcnt lgkmcnt(3)\n\t"
    "s_waitcnt lgkmcnt(2)\n\t"
    "v_add_f32 v190, v190, v191\n\t"
    "v_add_f32 v148, v148, v176\n\t"
    "v_add_f32 v149, v149, v177\n\t"
    "v_mfma_f32_16x16x32_bf16 a[8:11], a[100:103], v[124:127], a[8:11]\n\t"
    "buffer_load_dwordx4 v[56:59], v8, %0, s42 offen offset:2048 sc1\n\t"
    "v_add_f32 v150, v150, v178\n\t"
    "v_add_f32 v151, v151, v179\n\t"
    "v_add_f32 v188, v188, v190\n\t"
    "v_mfma_f32_16x16x32_bf16 a[12:15], a[104:107], v[108:111], 0\n\t"
    "v_cvt_pk_bf16_f32 v212, v148, v149\n\t"
    "v_cvt_pk_bf16_f32 v213, v150, v151\n\t"
    "v_fma_f32 v189, v144, v156, v157\n\t"
    "v_mfma_f32_16x16x32_bf16 a[12:15], a[108:111], v[112:115], a[12:15]\n\t"
    "ds_write_b128 v26, a[8:11] offset:16384\n\t"
    "v_add_f32 v188, v188, v189\n\t"
    "v_min_u32 v212, -2, v212\n\t"
    "v_mfma_f32_16x16x32_bf16 a[12:15], a[112:115], v[116:119], a[12:15]\n\t"
    "buffer_load_dwordx4 v[60:63], v8, %0, s42 offen offset:3072 sc1\n\t"
    "buffer_store_dwordx2 v[212:213], v16, %0, s43 offen sc1\n\t"
    "v_mov_b32 v214, v188\n\t"
    "v_mov_b32 v215, v188\n\t"
    "v_mfma_f32_16x16x32_bf16 a[12:15], a[116:119], v[120:123], a[12:15]\n\t"
    "s_nop 0\n\t"
    "v_permlane16_swap_b32 v214, v215\n\t"
    "v_cvt_pk_bf16_f32 v216, v214, v215\n\t"
    "v_mov_b32 v217, v216\n\t"
    "v_mfma_f32_16x16x32_bf16 a[12:15], a[120:123], v[124:127], a[12:15]\n\t"
    "s_nop 0\n\t"
    "v_permlane32_swap_b32 v216, v217\n\t"
    "v_min_u32 v216, -2, v216\n\t"
    "buffer_store_dwordx2 v[216:217], v17, %0, s43 offen sc1\n\t"
    "v_mfma_f32_16x16x32_bf16 v[152:155], a[124:127], v[108:111], v[152:155]\n\t"
    "v_add_f32 v128, v128, v148\n\t"
    "v_add_f32 v129, v129, v149\n\t"
    "v_add_f32 v130, v130, v150\n\t"
    "v_mfma_f32_16x16x32_bf16 v[152:155], a[128:131], v[112:115], v[152:155]\n\t"
    "buffer_load_dwordx4 v[64:67], v9, %0, s42 offen offset:0 sc1\n\t"
    "ds_write_b128 v28, a[12:15] offset:16384\n\t"
    "v_add_f32 v131, v131, v151\n\t"
    "v_add_f32 v144, v144, v188\n\t"
    "v_mfma_f32_16x16x32_bf16 v[152:155], a[132:135], v[116:119], v[152:155]\n\t"
    "v_mfma_f32_16x16x32_bf16 v[152:155], a[136:139], v[120:123], v[152:155]\n\t"
    "v_mfma_f32_16x16x32_bf16 v[152:155], a[140:143], v[124:127], v[152:155]\n\t"
    "s_waitcnt lgkmcnt(0)\n\t"
    "s_barrier\n\t"
    "buffer_load_dwordx4 a[248:251], v8, %0, s40 offen offset:1024\n\t"
    "ds_read_b128 v[176:179], v27 offset:16384\n\t"
    "ds_read_b128 v[180:183], v27 offset:17408\n\t"
    "ds_read_b128 v[184:187], v27 offset:18432\n\t"
    "ds_read_b32 v188, v29 offset:16384\n\t"
    "ds_read_b32 v189, v29 offset:17408\n\t"
    "ds_read_b32 v190, v29 offset:18432\n\t"
    "buffer_load_dwordx4 a[252:255], v8, %0, s40 offen offset:2048\n\t"
    "ds_read_b32 v191, v29 offset:19456\n\t"
    "s_waitcnt vmcnt(8)\n\t"
    "s_waitcnt vmcnt(7)\n\t"
    "s_waitcnt vmcnt(6)\n\t"
    "v_max3_u32 v227, v48, v52, v56\n\t"
    "s_waitcnt vmcnt(5)\n\t"
    "s_waitcnt vmcnt(2)\n\t"
    "v_max3_u32 v227, v227, v60, v64\n\t"
    "v_max3_u32 v227, v227, v50, v54\n\t"
    "v_max3_u32 v227, v227, v58, v62\n\t"
    "v_max_u32 v227, v227, v66\n\t"
    "v_cmp_eq_u32 vcc, -1, v227\n\t"
    "s_cbranch_vccnz H25_%=\n\t"
    "R26_%=:\n\t"
    "v_mfma_f32_16x16x32_bf16 v[200:203], v[48:51], v[48:51], 0\n\t"
    "s_add_u32 s41, %8, 0x194d800\n\t"
    "buffer_load_dwordx4 v[68:71], v8, %0, s41 offen offset:0 sc1\n\t"
    "buffer_load_dwordx4 v[72:75], v8, %0, s41 offen offset:1024 sc1\n\t"
    "s_add_u32 s43, %11, 0x1952800\n\t"
    "s_waitcnt lgkmcnt(6)\n\t"
    "s_waitcnt lgkmcnt(5)\n\t"
    "v_add_f32 v176, v176, v180\n\t"
    "v_add_f32 v177, v177, v181\n\t"
    "v_mfma_f32_16x16x32_bf16 v[204:207], v[208:211], v[48:51], 0\n\t"
    "buffer_load_dwordx4 v[76:79], v8, %0, s41 offen offset:2048 sc1\n\t"
    "buffer_load_dwordx4 v[80:83], v8, %0, s41 offen offset:3072 sc1\n\t"
    "v_add_f32 v178, v178, v182\n\t"
    "v_add_f32 v179, v179, v183\n\t"
    "s_waitcnt lgkmcnt(4)\n\t"
    "v_add_f32 v152, v152, v184\n\t"
    "v_mfma_f32_16x16x32_bf16 v[200:203], v[52:55], v[52:55], v[200:203]\n\t"
    "buffer_load_dwordx4 v[84:87], v9, %0, s41 offen offset:0 sc1\n\t"
    "v_add_f32 v153, v153, v185\n\t"
    "v_add_f32 v154, v154, v186\n\t"
    "v_add_f32 v155, v155, v187\n\t"
    "v_mfma_f32_16x16x32_bf16 v[204:207], v[208:211], v[52:55], v[204:207]\n\t"
    "s_waitcnt lgkmcnt(3)\n\t"
    "s_waitcnt lgkmcnt(2)\n\t"
    "v_add_f32 v188, v188, v189\n\t"
    "s_waitcnt lgkmcnt(1)\n\t"
    "s_waitcnt lgkmcnt(0)\n\t"
    "v_add_f32 v190, v190, v191\n\t"
    "v_add_f32 v152, v152, v176\n\t"
    "v_mfma_f32_16x16x32_bf16 v[200:203], v[56:59], v[56:59], v[200:203]\n\t"
    "v_add_f32 v153, v153, v177\n\t"
    "v_add_f32 v154, v154, v178\n\t"
    "v_add_f32 v155, v155, v179\n\t"
    "v_mfma_f32_16x16x32_bf16 v[204:207], v[208:211], v[56:59], v[204:207]\n\t"
    "v_add_f32 v188, v188, v190\n\t"
    "v_cvt_pk_bf16_f32 v212, v152, v153\n\t"
    "v_cvt_pk_bf16_f32 v213, v154, v155\n\t"
    "v_mfma_f32_16x16x32_bf16 v[200:203], v[60:63], v[60:63], v[200:203]\n\t"
    "v_fma_f32 v189, v147, v162, v163\n\t"
    "v_add_f32 v188, v188, v189\n\t"
    "v_min_u32 v212, -2, v212\n\t"
    "v_mfma_f32_16x16x32_bf16 v[204:207], v[208:211], v[60:63], v[204:207]\n\t"
    "buffer_store_dwordx2 v[212:213], v16, %0, s43 offen sc1\n\t"
    "v_mov_b32 v214, v188\n\t"
    "v_mov_b32 v215, v188\n\t"
    "v_mfma_f32_16x16x32_bf16 v[200:203], v[64:67], v[64:67], v[200:203]\n\t"
    "s_nop 0\n\t"
    "v_permlane16_swap_b32 v214, v215\n\t"
    "v_cvt_pk_bf16_f32 v216, v214, v215\n\t"
    "v_mov_b32 v217, v216\n\t"
    "v_mfma_f32_16x16x32_bf16 v[204:207], v[208:211], v[64:67], v[204:207]\n\t"
    "s_nop 0\n\t"
    "v_permlane32_swap_b32 v216, v217\n\t"
    "v_min_u32 v216, -2, v216\n\t"
    "buffer_store_dwordx2 v[216:217], v17, %0, s43 offen sc1\n\t"
    "v_mfma_f32_16x16x32_bf16 a[0:3], a[144:147], v[48:51], 0\n\t"
    "v_add_f32 v140, v140, v152\n\t"
    "v_add_f32 v141, v141, v153\n\t"
    "v_add_f32 v142, v142, v154\n\t"
    "v_mfma_f32_16x16x32_bf16 a[0:3], a[148:151], v[52:55], a[0:3]\n\t"
    "v_cndmask_b32_e64 v205, v200, v201, s[48:49]\n\t"
    "v_cndmask_b32_e64 v205, v205, v202, s[50:51]\n\t"
    "v_cndmask_b32_e64 v205, v205, v203, s[52:53]\n\t"
    "v_mfma_f32_16x16x32_bf16 a[0:3], a[152:155], v[56:59], a[0:3]\n\t"
    "ds_write_b64 v30, v[204:205] offset:0\n\t"
    "v_add_f32 v143, v143, v155\n\t"
    "v_add_f32 v147, v147, v188\n\t"
    "v_mfma_f32_16x16x32_bf16 a[0:3], a[156:159], v[60:63], a[0:3]\n\t"
    "s_add_u32 s42, %8, 0x1943800\n\t"
    "buffer_load_dwordx4 v[88:91], v8, %0, s42 offen offset:0 sc1\n\t"
    "ds_read_b128 v[164:167], v32 offset:320\n\t"
    "ds_read_b128 v[172:175], v32 offset:1600\n\t"
    "ds_read_b32 v168, v33 offset:320\n\t"
    "v_mfma_f32_16x16x32_bf16 a[0:3], a[160:163], v[64:67], a[0:3]\n\t"
    "buffer_load_dwordx4 v[92:95], v8, %0, s42 offen offset:1024 sc1\n\t"
    "ds_read_b32 v169, v33 offset:1600\n\t"
    "v_mfma_f32_16x16x32_bf16 a[4:7], a[164:167], v[48:51], 0\n\t"
    "buffer_load_dwordx4 v[96:99], v8, %0, s42 offen offset:2048 sc1\n\t"
    "v_mfma_f32_16x16x32_bf16 a[4:7], a[168:171], v[52:55], a[4:7]\n\t"
    "v_mfma_f32_16x16x32_bf16 a[4:7], a[172:175], v[56:59], a[4:7]\n\t"
    "v_mfma_f32_16x16x32_bf16 a[4:7], a[176:179], v[60:63], a[4:7]\n\t"
    "v_mfma_f32_16x16x32_bf16 a[4:7], a[180:183], v[64:67], a[4:7]\n\t"
    "ds_write_b128 v24, a[0:3] offset:0\n\t"
    "v_mfma_f32_16x16x32_bf16 a[8:11], a[184:187], v[48:51], 0\n\t"
    "v_mfma_f32_16x16x32_bf16 a[8:11], a[188:191], v[52:55], a[8:11]\n\t"
    "v_mfma_f32_16x16x32_bf16 a[8:11], a[192:195], v[56:59], a[8:11]\n\t"
    "s_waitcnt lgkmcnt(4)\n\t"
    "s_waitcnt lgkmcnt(3)\n\t"
    "v_add_f32 v164, v164, v172\n\t"
    "v_add_f32 v165, v165, v173\n\t"
    "ds_write_b128 v25, a[4:7] offset:0\n\t"
    "v_mfma_f32_16x16x32_bf16 a[8:11], a[196:199], v[60:63], a[8:11]\n\t"
    "v_add_f32 v166, v166, v174\n\t"
    "v_add_f32 v167, v167, v175\n\t"
    "s_waitcnt lgkmcnt(3)\n\t"
    "s_waitcnt lgkmcnt(2)\n\t"
    "v_add_f32 v168, v168, v169\n\t"
    "v_mfma_f32_16x16x32_bf16 a[8:11], a[200:203], v[64:67], a[8:11]\n\t"
    "v_mfma_f32_16x16x32_bf16 a[12:15], a[204:207], v[48:51], 0\n\t"
    "v_mfma_f32_16x16x32_bf16 a[12:15], a[208:211], v[52:55], a[12:15]\n\t"
    "v_mfma_f32_16x16x32_bf16 a[12:15], a[212:215], v[56:59], a[12:15]\n\t"
    "v_mfma_f32_16x16x32_bf16 a[12:15], a[216:219], v[60:63], a[12:15]\n\t"
    "v_mfma_f32_16x16x32_bf16 a[12:15], a[220:223], v[64:67], a[12:15]\n\t"
    "v_mfma_f32_16x16x32_bf16 v[148:151], a[224:227], v[48:51], 0\n\t"
    "v_mfma_f32_16x16x32_bf16 v[148:151], a[228:231], v[52:55], v[148:151]\n\t"
    "v_mfma_f32_16x16x32_bf16 v[148:151], a[232:235], v[56:59], v[148:151]\n\t"
    "ds_write_b128 v26, a[8:11] offset:0\n\t"
    "v_mfma_f32_16x16x32_bf16 v[148:151], a[236:239], v[60:63], v[148:151]\n\t"
    "v_mfma_f32_16x16x32_bf16 v[148:151], a[240:243], v[64:67], v[148:151]\n\t"
    "s_nop 1\n\t"
    "ds_write_b128 v28, a[12:15] offset:0\n\t"
    "s_waitcnt lgkmcnt(0)\n\t"
    "s_barrier\n\t"
    "buffer_load_dwordx4 v[100:103], v8, %0, s42 offen offset:3072 sc1\n\t"
    "buffer_load_dwordx4 v[104:107], v9, %0, s42 offen offset:0 sc1\n\t"
    "ds_read_b128 v[176:179], v27 offset:0\n\t"
    "ds_read_b128 v[180:183], v27 offset:1024\n\t"
    "ds_read_b128 v[184:187], v27 offset:2048\n\t"
    "ds_read_b32 v188, v29 offset:0\n\t"
    "ds_read_b32 v189, v29 offset:1024\n\t"
    "ds_read_b32 v190, v29 offset:2048\n\t"
    "ds_read_b32 v191, v29 offset:3072\n\t"
    "ds_read_b128 v[192:195], v31 offset:0\n\t"
    "ds_read_b128 v[196:199], v31 offset:16\n\t"
    "s_waitcnt vmcnt(11)\n\t"
    "s_waitcnt vmcnt(10)\n\t"
    "s_waitcnt vmcnt(9)\n\t"
    "v_max3_u32 v227, v68, v72, v76\n\t"
    "s_waitcnt vmcnt(8)\n\t"
    "s_waitcnt vmcnt(7)\n\t"
    "v_max3_u32 v227, v227, v80, v84\n\t"
    "v_max3_u32 v227, v227, v70, v74\n\t"
    "v_max3_u32 v227, v227, v78, v82\n\t"
    "v_max_u32 v227, v227, v86\n\t"
    "v_cmp_eq_u32 vcc, -1, v227\n\t"
    "s_cbranch_vccnz H27_%=\n\t"
    "R28_%=:\n\t"
    "v_mfma_f32_16x16x32_bf16 v[200:203], v[68:71], v[68:71], 0\n\t"
    "buffer_load_dwordx4 v[228:231], v8, %0, s40 offen offset:3072\n\t"
    "s_waitcnt lgkmcnt(1)\n\t"
    "v_add_f32 v192, v192, v194\n\t"
    "v_add_f32 v193, v193, v195\n\t"
    "s_waitcnt lgkmcnt(0)\n\t"
    "v_add_f32 v196, v196, v198\n\t"
    "v_mfma_f32_16x16x32_bf16 v[204:207], v[208:211], v[68:71], 0\n\t"
    "s_add_u32 s40, s40, 4096\n\t"
    "buffer_load_dwordx4 v[232:235], v8, %0, s40 offen offset:0\n\t"
    "v_add_f32 v197, v197, v199\n\t"
    "v_add_f32 v192, v192, v196\n\t"
    "v_add_f32 v193, v193, v197\n\t"
    "v_mfma_f32_16x16x32_bf16 v[200:203], v[72:75], v[72:75], v[200:203]\n\t"
    "buffer_load_dwordx4 v[236:239], v8, %0, s40 offen offset:1024\n\t"
    "v_mul_f32 v222, 0xbacccccd, v192\n\t"
    "v_mul_f32 v223, 0x3acccccd, v193\n\t"
    "v_fma_f32 v223, -v222, v222, v223\n\t"
    "v_mfma_f32_16x16x32_bf16 v[204:207], v[208:211], v[72:75], v[204:207]\n\t"
    "buffer_load_dwordx4 v[240:243], v8, %0, s40 offen offset:2048\n\t"
    "v_max_f32 v223, 0, v223\n\t"
    "v_add_f32 v226, 0x3727c5ac, v223\n\t"
    "v_rsq_f32 v158, v226\n\t"
    "v_mfma_f32_16x16x32_bf16 v[200:203], v[76:79], v[76:79], v[200:203]\n\t"
    "v_add_f32 v176, v176, v180\n\t"
    "v_add_f32 v177, v177, v181\n\t"
    "v_add_f32 v178, v178, v182\n\t"
    "v_mfma_f32_16x16x32_bf16 v[204:207], v[208:211], v[76:79], v[204:207]\n\t"
    "v_add_f32 v179, v179, v183\n\t"
    "v_mul_f32 v159, v222, v158\n\t"
    "v_add_f32 v148, v148, v184\n\t"
    "v_mfma_f32_16x16x32_bf16 v[200:203], v[80:83], v[80:83], v[200:203]\n\t"
    "buffer_load_dwordx4 v[244:247], v8, %0, s40 offen offset:3072\n\t"
    "v_add_f32 v149, v149, v185\n\t"
    "v_add_f32 v150, v150, v186\n\t"
    "v_add_f32 v151, v151, v187\n\t"
    "v_mfma_f32_16x16x32_bf16 v[204:207], v[208:211], v[80:83], v[204:207]\n\t"
    "v_add_f32 v188, v188, v189\n\t"
    "v_add_f32 v190, v190, v191\n\t"
    "v_add_f32 v148, v148, v176\n\t"
    "v_mfma_f32_16x16x32_bf16 v[200:203], v[84:87], v[84:87], v[200:203]\n\t"
    "v_add_f32 v149, v149, v177\n\t"
    "v_add_f32 v150, v150, v178\n\t"
    "v_add_f32 v151, v151, v179\n\t"
    "v_mfma_f32_16x16x32_bf16 v[204:207], v[208:211], v[84:87], v[204:207]\n\t"
    "s_add_u32 s40, s40, 4096\n\t"
    "buffer_load_dwordx4 v[248:251], v8, %0, s40 offen offset:0\n\t"
    "v_add_f32 v188, v188, v190\n\t"
    "v_mul_f32 v212, v164, v159\n\t"
    "v_mul_f32 v213, v165, v159\n\t"
    "v_mfma_f32_16x16x32_bf16 a[0:3], a[144:147], v[68:71], 0\n\t"
    "v_mul_f32 v214, v166, v159\n\t"
    "v_mul_f32 v215, v167, v159\n\t"
    "v_cndmask_b32_e64 v205, v200, v201, s[48:49]\n\t"
    "v_mfma_f32_16x16x32_bf16 a[0:3], a[148:151], v[72:75], a[0:3]\n\t"
    "v_cndmask_b32_e64 v205, v205, v202, s[50:51]\n\t"
    "v_cndmask_b32_e64 v205, v205, v203, s[52:53]\n\t"
    "v_mul_f32 v216, v168, v159\n\t"
    "v_mfma_f32_16x16x32_bf16 a[0:3], a[152:155], v[76:79], a[0:3]\n\t"
    "ds_write_b64 v30, v[204:205] offset:512\n\t"
    "v_fma_f32 v148, v148, v158, v212\n\t"
    "v_fma_f32 v149, v149, v158, v213\n\t"
    "v_mfma_f32_16x16x32_bf16 a[0:3], a[156:159], v[80:83], a[0:3]\n\t"
    "v_fma_f32 v150, v150, v158, v214\n\t"
    "v_fma_f32 v151, v151, v158, v215\n\t"
    "v_fma_f32 v188, v188, v158, v216\n\t"
    "v_mfma_f32_16x16x32_bf16 a[0:3], a[160:163], v[84:87], a[0:3]\n\t"
    "v_mul_f32 v212, v148, v148\n\t"
    "v_mul_f32 v213, v149, v149\n\t"
    "v_mul_f32 v214, v150, v150\n\t"
    "v_mfma_f32_16x16x32_bf16 a[4:7], a[164:167], v[68:71], 0\n\t"
    "v_mul_f32 v215, v151, v151\n\t"
    "v_mul_f32 v216, v188, v188\n\t"
    "v_fma_f32 v212, v212, v11, v10\n\t"
    "v_mfma_f32_16x16x32_bf16 a[4:7], a[168:171], v[72:75], a[4:7]\n\t"
    "ds_write_b128 v24, a[0:3] offset:16384\n\t"
    "v_fma_f32 v213, v213, v11, v10\n\t"
    "v_fma_f32 v214, v214, v11, v10\n\t"
    "v_mfma_f32_16x16x32_bf16 a[4:7], a[172:175], v[76:79], a[4:7]\n\t"
    "v_fma_f32 v215, v215, v11, v10\n\t"
    "v_fma_f32 v216, v216, v11, v10\n\t"
    "v_mul_f32 v212, v212, v148\n\t"
    "v_mfma_f32_16x16x32_bf16 a[4:7], a[176:179], v[80:83], a[4:7]\n\t"
    "v_mul_f32 v213, v213, v149\n\t"
    "v_mul_f32 v214, v214, v150\n\t"
    "v_mul_f32 v215, v215, v151\n\t"
    "v_mfma_f32_16x16x32_bf16 a[4:7], a[180:183], v[84:87], a[4:7]\n\t"
    "v_mul_f32 v216, v216, v188\n\t"
    "v_exp_f32 v212, v212\n\t"
    "v_exp_f32 v213, v213\n\t"
    "v_mfma_f32_16x16x32_bf16 a[8:11], a[184:187], v[68:71], 0\n\t"
    "buffer_load_dwordx4 v[252:255], v8, %0, s40 offen offset:1024\n\t"
    "v_exp_f32 v214, v214\n\t"
    "v_exp_f32 v215, v215\n\t"
    "v_exp_f32 v216, v216\n\t"
    "v_mfma_f32_16x16x32_bf16 a[8:11], a[188:191], v[72:75], a[8:11]\n\t"
    "ds_write_b128 v25, a[4:7] offset:16384\n\t"
    "v_add_f32 v212, 1.0, v212\n\t"
    "v_add_f32 v213, 1.0, v213\n\t"
    "v_mfma_f32_16x16x32_bf16 a[8:11], a[192:195], v[76:79], a[8:11]\n\t"
    "v_add_f32 v214, 1.0, v214\n\t"
    "v_add_f32 v215, 1.0, v215\n\t"
    "v_add_f32 v216, 1.0, v216\n\t"
    "v_mfma_f32_16x16x32_bf16 a[8:11], a[196:199], v[80:83], a[8:11]\n\t"
    "s_add_u32 s41, %8, 0x1952800\n\t"
    "buffer_load_dwordx4 v[108:111], v8, %0, s41 offen offset:0 sc1\n\t"
    "v_rcp_f32 v212, v212\n\t"
    "v_rcp_f32 v213, v213\n\t"
    "v_rcp_f32 v214, v214\n\t"
    "v_mfma_f32_16x16x32_bf16 a[8:11], a[200:203], v[84:87], a[8:11]\n\t"
    "v_rcp_f32 v215, v215\n\t"
    "v_rcp_f32 v216, v216\n\t"
    "v_mul_f32 v148, v148, v212\n\t"
    "v_mfma_f32_16x16x32_bf16 a[12:15], a[204:207], v[68:71], 0\n\t"
    "v_mul_f32 v149, v149, v213\n\t"
    "v_mul_f32 v150, v150, v214\n\t"
    "v_mul_f32 v151, v151, v215\n\t"
    "v_mfma_f32_16x16x32_bf16 a[12:15], a[208:211], v[72:75], a[12:15]\n\t"
    "buffer_load_dwordx4 v[112:115], v8, %0, s41 offen offset:1024 sc1\n\t"
    "ds_write_b128 v26, a[8:11] offset:16384\n\t"
    "v_mul_f32 v188, v188, v216\n\t"
    "v_cvt_pk_bf16_f32 v212, v148, v149\n\t"
    "v_mfma_f32_16x16x32_bf16 a[12:15], a[212:215], v[76:79], a[12:15]\n\t"
    "v_cvt_pk_bf16_f32 v213, v150, v151\n\t"
    "s_add_u32 s43, %12, 0xac7800\n\t"
    "buffer_store_dwordx2 v[212:213], v16, %0, s43 offen sc1\n\t"
    "v_mfma_f32_16x16x32_bf16 a[12:15], a[216:219], v[80:83], a[12:15]\n\t"
    "v_cvt_pk_bf16_f32 v214, v188, v188\n\t"
    "buffer_store_short v214, v18, %0, s43 offen sc1\n\t"
    "v_mfma_f32_16x16x32_bf16 a[12:15], a[220:223], v[84:87], a[12:15]\n\t"
    "v_mfma_f32_16x16x32_bf16 v[152:155], a[224:227], v[68:71], 0\n\t"
    "buffer_load_dwordx4 v[116:119], v8, %0, s41 offen offset:2048 sc1\n\t"
    "v_mfma_f32_16x16x32_bf16 v[152:155], a[228:231], v[72:75], v[152:155]\n\t"
    "v_mfma_f32_16x16x32_bf16 v[152:155], a[232:235], v[76:79], v[152:155]\n\t"
    "v_mfma_f32_16x16x32_bf16 v[152:155], a[236:239], v[80:83], v[152:155]\n\t"
    "v_mfma_f32_16x16x32_bf16 v[152:155], a[240:243], v[84:87], v[152:155]\n\t"
    "s_nop 1\n\t"
    "ds_write_b128 v28, a[12:15] offset:16384\n\t"
    "s_waitcnt lgkmcnt(0)\n\t"
    "s_barrier\n\t"
    "s_waitcnt vmcnt(16)\n\t"
    "s_waitcnt vmcnt(15)\n\t"
    "s_waitcnt vmcnt(14)\n\t"
    "v_max3_u32 v227, v88, v92, v96\n\t"
    "s_waitcnt vmcnt(13)\n\t"
    "s_waitcnt vmcnt(12)\n\t"
    "v_max3_u32 v227, v227, v100, v104\n\t"
    "v_max3_u32 v227, v227, v90, v94\n\t"
    "v_max3_u32 v227, v227, v98, v102\n\t"
    "v_max_u32 v227, v227, v106\n\t"
    "v_cmp_eq_u32 vcc, -1, v227\n\t"
    "s_cbranch_vccnz H29_%=\n\t"
    "R30_%=:\n\t"
    "v_mfma_f32_16x16x32_bf16 v[200:203], v[88:91], v[88:91], 0\n\t"
    "buffer_load_dwordx4 v[120:123], v8, %0, s41 offen offset:3072 sc1\n\t"
    "ds_read_b128 v[176:179], v27 offset:16384\n\t"
    "ds_read_b128 v[180:183], v27 offset:17408\n\t"
    "ds_read_b128 v[184:187], v27 offset:18432\n\t"
    "v_mfma_f32_16x16x32_bf16 v[204:207], v[208:211], v[88:91], 0\n\t"
    "buffer_load_dwordx4 v[124:127], v9, %0, s41 offen offset:0 sc1\n\t"
    "ds_read_b32 v188, v29 offset:16384\n\t"
    "ds_read_b32 v189, v29 offset:17408\n\t"
    "ds_read_b32 v190, v29 offset:18432\n\t"
    "v_mfma_f32_16x16x32_bf16 v[200:203], v[92:95], v[92:95], v[200:203]\n\t"
    "buffer_load_dwordx4 a[64:67], v8, %0, s40 offen offset:2048\n\t"
    "ds_read_b32 v191, v29 offset:19456\n\t"
    "ds_read_b128 v[192:195], v31 offset:512\n\t"
    "ds_read_b128 v[196:199], v31 offset:528\n\t"
    "v_mfma_f32_16x16x32_bf16 v[204:207], v[208:211], v[92:95], v[204:207]\n\t"
    "buffer_load_dwordx4 a[68:71], v8, %0, s40 offen offset:3072\n\t"
    "v_mfma_f32_16x16x32_bf16 v[200:203], v[96:99], v[96:99], v[200:203]\n\t"
    "v_mfma_f32_16x16x32_bf16 v[204:207], v[208:211], v[96:99], v[204:207]\n\t"
    "v_mfma_f32_16x16x32_bf16 v[200:203], v[100:103], v[100:103], v[200:203]\n\t"
    "v_mfma_f32_16x16x32_bf16 v[204:207], v[208:211], v[100:103], v[204:207]\n\t"
    "v_mfma_f32_16x16x32_bf16 v[200:203], v[104:107], v[104:107], v[200:203]\n\t"
    "s_add_u32 s40, s40, 4096\n\t"
    "buffer_load_dwordx4 a[72:75], v8, %0, s40 offen offset:0\n\t"
    "v_mfma_f32_16x16x32_bf16 v[204:207], v[208:211], v[104:107], v[204:207]\n\t"
    "v_mfma_f32_16x16x32_bf16 a[0:3], a[144:147], v[88:91], 0\n\t"
    "v_mfma_f32_16x16x32_bf16 a[0:3], a[148:151], v[92:95], a[0:3]\n\t"
    "s_waitcnt lgkmcnt(1)\n\t"
    "v_add_f32 v192, v192, v194\n\t"
    "v_add_f32 v193, v193, v195\n\t"
    "s_waitcnt lgkmcnt(0)\n\t"
    "v_add_f32 v196, v196, v198\n\t"
    "v_mfma_f32_16x16x32_bf16 a[0:3], a[152:155], v[96:99], a[0:3]\n\t"
    "v_cndmask_b32_e64 v205, v200, v201, s[48:49]\n\t"
    "v_cndmask_b32_e64 v205, v205, v202, s[50:51]\n\t"
    "v_cndmask_b32_e64 v205, v205, v203, s[52:53]\n\t"
    "v_mfma_f32_16x16x32_bf16 a[0:3], a[156:159], v[100:103], a[0:3]\n\t"
    "ds_write_b64 v30, v[204:205] offset:0\n\t"
    "v_add_f32 v197, v197, v199\n\t"
    "v_add_f32 v192, v192, v196\n\t"
    "v_mfma_f32_16x16x32_bf16 a[0:3], a[160:163], v[104:107], a[0:3]\n\t"
    "v_add_f32 v193, v193, v197\n\t"
    "v_mul_f32 v222, 0xbacccccd, v192\n\t"
    "v_mul_f32 v223, 0x3acccccd, v193\n\t"
    "v_mfma_f32_16x16x32_bf16 a[4:7], a[164:167], v[88:91], 0\n\t"
    "v_fma_f32 v223, -v222, v222, v223\n\t"
    "v_max_f32 v223, 0, v223\n\t"
    "v_add_f32 v226, 0x3727c5ac, v223\n\t"
    "v_mfma_f32_16x16x32_bf16 a[4:7], a[168:171], v[92:95], a[4:7]\n\t"
    "buffer_load_dwordx4 a[76:79], v8, %0, s40 offen offset:1024\n\t"
    "ds_write_b128 v24, a[0:3] offset:0\n\t"
    "v_rsq_f32 v160, v226\n\t"
    "v_add_f32 v176, v176, v180\n\t"
    "v_mfma_f32_16x16x32_bf16 a[4:7], a[172:175], v[96:99], a[4:7]\n\t"
    "v_add_f32 v177, v177, v181\n\t"
    "v_add_f32 v178, v178, v182\n\t"
    "v_add_f32 v179, v179, v183\n\t"
    "v_mfma_f32_16x16x32_bf16 a[4:7], a[176:179], v[100:103], a[4:7]\n\t"
    "v_mul_f32 v161, v222, v160\n\t"
    "v_add_f32 v152, v152, v184\n\t"
    "v_add_f32 v153, v153, v185\n\t"
    "v_mfma_f32_16x16x32_bf16 a[4:7], a[180:183], v[104:107], a[4:7]\n\t"
    "v_add_f32 v154, v154, v186\n\t"
    "v_add_f32 v155, v155, v187\n\t"
    "v_add_f32 v188, v188, v189\n\t"
    "v_mfma_f32_16x16x32_bf16 a[8:11], a[184:187], v[88:91], 0\n\t"
    "buffer_load_dwordx4 a[80:83], v8, %0, s40 offen offset:2048\n\t"
    "v_add_f32 v190, v190, v191\n\t"
    "v_add_f32 v152, v152, v176\n\t"
    "v_add_f32 v153, v153, v177\n\t"
    "v_mfma_f32_16x16x32_bf16 a[8:11], a[188:191], v[92:95], a[8:11]\n\t"
    "ds_write_b128 v25, a[4:7] offset:0\n\t"
    "v_add_f32 v154, v154, v178\n\t"
    "v_add_f32 v155, v155, v179\n\t"
    "v_mfma_f32_16x16x32_bf16 a[8:11], a[192:195], v[96:99], a[8:11]\n\t"
    "v_add_f32 v188, v188, v190\n\t"
    "v_mul_f32 v212, v164, v161\n\t"
    "v_mul_f32 v213, v165, v161\n\t"
    "v_mfma_f32_16x16x32_bf16 a[8:11], a[196:199], v[100:103], a[8:11]\n\t"
    "v_mul_f32 v214, v166, v161\n\t"
    "v_mul_f32 v215, v167, v161\n\t"
    "v_mul_f32 v216, v168, v161\n\t"
    "v_mfma_f32_16x16x32_bf16 a[8:11], a[200:203], v[104:107], a[8:11]\n\t"
    "buffer_load_dwordx4 a[84:87], v8, %0, s40 offen offset:3072\n\t"
    "v_fma_f32 v152, v152, v160, v212\n\t"
    "v_fma_f32 v153, v153, v160, v213\n\t"
    "v_fma_f32 v154, v154, v160, v214\n\t"
    "v_mfma_f32_16x16x32_bf16 a[12:15], a[204:207], v[88:91], 0\n\t"
    "v_fma_f32 v155, v155, v160, v215\n\t"
    "v_fma_f32 v188, v188, v160, v216\n\t"
    "v_mul_f32 v212, v152, v152\n\t"
    "v_mfma_f32_16x16x32_bf16 a[12:15], a[208:211], v[92:95], a[12:15]\n\t"
    "ds_write_b128 v26, a[8:11] offset:0\n\t"
    "v_mul_f32 v213, v153, v153\n\t"
    "v_mul_f32 v214, v154, v154\n\t"
    "v_mfma_f32_16x16x32_bf16 a[12:15], a[212:215], v[96:99], a[12:15]\n\t"
    "v_mul_f32 v215, v155, v155\n\t"
    "v_mul_f32 v216, v188, v188\n\t"
    "v_fma_f32 v212, v212, v11, v10\n\t"
    "v_mfma_f32_16x16x32_bf16 a[12:15], a[216:219], v[100:103], a[12:15]\n\t"
    "v_fma_f32 v213, v213, v11, v10\n\t"
    "v_fma_f32 v214, v214, v11, v10\n\t"
    "v_fma_f32 v215, v215, v11, v10\n\t"
    "v_mfma_f32_16x16x32_bf16 a[12:15], a[220:223], v[104:107], a[12:15]\n\t"
    "v_fma_f32 v216, v216, v11, v10\n\t"
    "v_mul_f32 v212, v212, v152\n\t"
    "v_mul_f32 v213, v213, v153\n\t"
    "v_mfma_f32_16x16x32_bf16 v[148:151], a[224:227], v[88:91], 0\n\t"
    "v_mul_f32 v214, v214, v154\n\t"
    "v_mul_f32 v215, v215, v155\n\t"
    "v_mul_f32 v216, v216, v188\n\t"
    "v_mfma_f32_16x16x32_bf16 v[148:151], a[228:231], v[92:95], v[148:151]\n\t"
    "ds_write_b128 v28, a[12:15] offset:0\n\t"
    "v_exp_f32 v212, v212\n\t"
    "v_exp_f32 v213, v213\n\t"
    "v_mfma_f32_16x16x32_bf16 v[148:151], a[232:235], v[96:99], v[148:151]\n\t"
    "v_exp_f32 v214, v214\n\t"
    "v_exp_f32 v215, v215\n\t"
    "v_exp_f32 v216, v216\n\t"
    "v_mfma_f32_16x16x32_bf16 v[148:151], a[236:239], v[100:103], v[148:151]\n\t"
    "v_add_f32 v212, 1.0, v212\n\t"
    "v_add_f32 v213, 1.0, v213\n\t"
    "v_add_f32 v214, 1.0, v214\n\t"
    "v_mfma_f32_16x16x32_bf16 v[148:151], a[240:243], v[104:107], v[148:151]\n\t"
    "v_add_f32 v215, 1.0, v215\n\t"
    "v_add_f32 v216, 1.0, v216\n\t"
    "v_rcp_f32 v212, v212\n\t"
    "v_rcp_f32 v213, v213\n\t"
    "v_rcp_f32 v214, v214\n\t"
    "v_rcp_f32 v215, v215\n\t"
    "v_rcp_f32 v216, v216\n\t"
    "v_mul_f32 v152, v152, v212\n\t"
    "v_mul_f32 v153, v153, v213\n\t"
    "v_mul_f32 v154, v154, v214\n\t"
    "v_mul_f32 v155, v155, v215\n\t"
    "v_mul_f32 v188, v188, v216\n\t"
    "v_cvt_pk_bf16_f32 v212, v152, v153\n\t"
    "v_cvt_pk_bf16_f32 v213, v154, v155\n\t"
    "s_add_u32 s43, %12, 0xad5800\n\t"
    "buffer_store_dwordx2 v[212:213], v16, %0, s43 offen sc1\n\t"
    "v_cvt_pk_bf16_f32 v214, v188, v188\n\t"
    "buffer_store_short v214, v18, %0, s43 offen sc1\n\t"
    "s_waitcnt lgkmcnt(0)\n\t"
    "s_barrier\n\t"
    "s_waitcnt vmcnt(14)\n\t"
    "s_waitcnt vmcnt(13)\n\t"
    "s_waitcnt vmcnt(10)\n\t"
    "v_max3_u32 v227, v108, v112, v116\n\t"
    "s_waitcnt vmcnt(9)\n\t"
    "s_waitcnt vmcnt(8)\n\t"
    "v_max3_u32 v227, v227, v120, v124\n\t"
    "v_max3_u32 v227, v227, v110, v114\n\t"
    "v_max3_u32 v227, v227, v118, v122\n\t"
    "v_max_u32 v227, v227, v126\n\t"
    "v_cmp_eq_u32 vcc, -1, v227\n\t"
    "s_cbranch_vccnz H31_%=\n\t"
    "R32_%=:\n\t"
    "v_mfma_f32_16x16x32_bf16 v[200:203], v[108:111], v[108:111], 0\n\t"
    "s_add_u32 s40, s40, 4096\n\t"
    "buffer_load_dwordx4 a[88:91], v8, %0, s40 offen offset:0\n\t"
    "ds_read_b128 v[176:179], v27 offset:0\n\t"
    "ds_read_b128 v[180:183], v27 offset:1024\n\t"
    "ds_read_b128 v[184:187], v27 offset:2048\n\t"
    "v_mfma_f32_16x16x32_bf16 v[204:207], v[208:211], v[108:111], 0\n\t"
    "buffer_load_dwordx4 a[92:95], v8, %0, s40 offen offset:1024\n\t"
    "ds_read_b32 v188, v29 offset:0\n\t"
    "ds_read_b32 v189, v29 offset:1024\n\t"
    "ds_read_b32 v190, v29 offset:2048\n\t"
    "v_mfma_f32_16x16x32_bf16 v[200:203], v[112:115], v[112:115], v[200:203]\n\t"
    "buffer_load_dwordx4 a[96:99], v8, %0, s40 offen offset:2048\n\t"
    "ds_read_b32 v191, v29 offset:3072\n\t"
    "ds_read_b128 v[192:195], v31 offset:0\n\t"
    "ds_read_b128 v[196:199], v31 offset:16\n\t"
    "v_mfma_f32_16x16x32_bf16 v[204:207], v[208:211], v[112:115], v[204:207]\n\t"
    "buffer_load_dwordx4 a[100:103], v8, %0, s40 offen offset:3072\n\t"
    "v_mfma_f32_16x16x32_bf16 v[200:203], v[116:119], v[116:119], v[200:203]\n\t"
    "v_mfma_f32_16x16x32_bf16 v[204:207], v[208:211], v[116:119], v[204:207]\n\t"
    "v_mfma_f32_16x16x32_bf16 v[200:203], v[120:123], v[120:123], v[200:203]\n\t"
    "v_mfma_f32_16x16x32_bf16 v[204:207], v[208:211], v[120:123], v[204:207]\n\t"
    "v_mfma_f32_16x16x32_bf16 v[200:203], v[124:127], v[124:127], v[200:203]\n\t"
    "s_add_u32 s40, s40, 4096\n\t"
    "buffer_load_dwordx4 a[104:107], v8, %0, s40 offen offset:0\n\t"
    "v_mfma_f32_16x16x32_bf16 v[204:207], v[208:211], v[124:127], v[204:207]\n\t"
    "v_mfma_f32_16x16x32_bf16 a[0:3], a[144:147], v[108:111], 0\n\t"
    "v_mfma_f32_16x16x32_bf16 a[0:3], a[148:151], v[112:115], a[0:3]\n\t"
    "s_waitcnt lgkmcnt(1)\n\t"
    "v_add_f32 v192, v192, v194\n\t"
    "v_add_f32 v193, v193, v195\n\t"
    "s_waitcnt lgkmcnt(0)\n\t"
    "v_add_f32 v196, v196, v198\n\t"
    "v_mfma_f32_16x16x32_bf16 a[0:3], a[152:155], v[116:119], a[0:3]\n\t"
    "v_cndmask_b32_e64 v205, v200, v201, s[48:49]\n\t"
    "v_cndmask_b32_e64 v205, v205, v202, s[50:51]\n\t"
    "v_cndmask_b32_e64 v205, v205, v203, s[52:53]\n\t"
    "v_mfma_f32_16x16x32_bf16 a[0:3], a[156:159], v[120:123], a[0:3]\n\t"
    "buffer_load_dwordx4 a[108:111], v8, %0, s40 offen offset:1024\n\t"
    "ds_write_b64 v30, v[204:205] offset:512\n\t"
    "v_add_f32 v197, v197, v199\n\t"
    "v_add_f32 v192, v192, v196\n\t"
    "v_mfma_f32_16x16x32_bf16 a[0:3], a[160:163], v[124:127], a[0:3]\n\t"
    "v_add_f32 v193, v193, v197\n\t"
    "v_mul_f32 v222, 0xbacccccd, v192\n\t"
    "v_mul_f32 v223, 0x3acccccd, v193\n\t"
    "v_mfma_f32_16x16x32_bf16 a[4:7], a[164:167], v[108:111], 0\n\t"
    "v_fma_f32 v223, -v222, v222, v223\n\t"
    "v_max_f32 v223, 0, v223\n\t"
    "v_add_f32 v226, 0x3727c5ac, v223\n\t"
    "v_mfma_f32_16x16x32_bf16 a[4:7], a[168:171], v[112:115], a[4:7]\n\t"
    "ds_write_b128 v24, a[0:3] offset:16384\n\t"
    "v_rsq_f32 v156, v226\n\t"
    "v_add_f32 v176, v176, v180\n\t"
    "v_mfma_f32_16x16x32_bf16 a[4:7], a[172:175], v[116:119], a[4:7]\n\t"
    "buffer_load_dwordx4 a[112:115], v8, %0, s40 offen offset:2048\n\t"
    "v_add_f32 v177, v177, v181\n\t"
    "v_add_f32 v178, v178, v182\n\t"
    "v_add_f32 v179, v179, v183\n\t"
    "v_mfma_f32_16x16x32_bf16 a[4:7], a[176:179], v[120:123], a[4:7]\n\t"
    "v_mul_f32 v157, v222, v156\n\t"
    "v_add_f32 v148, v148, v184\n\t"
    "v_add_f32 v149, v149, v185\n\t"
    "v_mfma_f32_16x16x32_bf16 a[4:7], a[180:183], v[124:127], a[4:7]\n\t"
    "v_add_f32 v150, v150, v186\n\t"
    "v_add_f32 v151, v151, v187\n\t"
    "v_add_f32 v188, v188, v189\n\t"
    "v_mfma_f32_16x16x32_bf16 a[8:11], a[184:187], v[108:111], 0\n\t"
    "buffer_load_dwordx4 a[116:119], v8, %0, s40 offen offset:3072\n\t"
    "v_add_f32 v190, v190, v191\n\t"
    "v_add_f32 v148, v148, v176\n\t"
    "v_add_f32 v149, v149, v177\n\t"
    "v_mfma_f32_16x16x32_bf16 a[8:11], a[188:191], v[112:115], a[8:11]\n\t"
    "ds_write_b128 v25, a[4:7] offset:16384\n\t"
    "v_add_f32 v150, v150, v178\n\t"
    "v_add_f32 v151, v151, v179\n\t"
    "v_mfma_f32_16x16x32_bf16 a[8:11], a[192:195], v[116:119], a[8:11]\n\t"
    "v_add_f32 v188, v188, v190\n\t"
    "v_mul_f32 v212, v164, v157\n\t"
    "v_mul_f32 v213, v165, v157\n\t"
    "v_mfma_f32_16x16x32_bf16 a[8:11], a[196:199], v[120:123], a[8:11]\n\t"
    "v_mul_f32 v214, v166, v157\n\t"
    "v_mul_f32 v215, v167, v157\n\t"
    "v_mul_f32 v216, v168, v157\n\t"
    "v_mfma_f32_16x16x32_bf16 a[8:11], a[200:203], v[124:127], a[8:11]\n\t"
    "v_fma_f32 v148, v148, v156, v212\n\t"
    "v_fma_f32 v149, v149, v156, v213\n\t"
    "v_fma_f32 v150, v150, v156, v214\n\t"
    "v_mfma_f32_16x16x32_bf16 a[12:15], a[204:207], v[108:111], 0\n\t"
    "v_fma_f32 v151, v151, v156, v215\n\t"
    "v_fma_f32 v188, v188, v156, v216\n\t"
    "v_mul_f32 v212, v148, v148\n\t"
    "v_mfma_f32_16x16x32_bf16 a[12:15], a[208:211], v[112:115], a[12:15]\n\t"
    "ds_write_b128 v26, a[8:11] offset:16384\n\t"
    "v_mul_f32 v213, v149, v149\n\t"
    "v_mul_f32 v214, v150, v150\n\t"
    "v_mfma_f32_16x16x32_bf16 a[12:15], a[212:215], v[116:119], a[12:15]\n\t"
    "v_mul_f32 v215, v151, v151\n\t"
    "v_mul_f32 v216, v188, v188\n\t"
    "v_fma_f32 v212, v212, v11, v10\n\t"
    "v_mfma_f32_16x16x32_bf16 a[12:15], a[216:219], v[120:123], a[12:15]\n\t"
    "v_fma_f32 v213, v213, v11, v10\n\t"
    "v_fma_f32 v214, v214, v11, v10\n\t"
    "v_fma_f32 v215, v215, v11, v10\n\t"
    "v_mfma_f32_16x16x32_bf16 a[12:15], a[220:223], v[124:127], a[12:15]\n\t"
    "v_fma_f32 v216, v216, v11, v10\n\t"
    "v_mul_f32 v212, v212, v148\n\t"
    "v_mul_f32 v213, v213, v149\n\t"
    "v_mfma_f32_16x16x32_bf16 v[152:155], a[224:227], v[108:111], 0\n\t"
    "v_mul_f32 v214, v214, v150\n\t"
    "v_mul_f32 v215, v215, v151\n\t"
    "v_mul_f32 v216, v216, v188\n\t"
    "v_mfma_f32_16x16x32_bf16 v[152:155], a[228:231], v[112:115], v[152:155]\n\t"
    "s_add_u32 s40, s40, 4096\n\t"
    "buffer_load_dwordx4 a[120:123], v8, %0, s40 offen offset:0\n\t"
    "ds_write_b128 v28, a[12:15] offset:16384\n\t"
    "v_exp_f32 v212, v212\n\t"
    "v_exp_f32 v213, v213\n\t"
    "v_mfma_f32_16x16x32_bf16 v[152:155], a[232:235], v[116:119], v[152:155]\n\t"
    "v_exp_f32 v214, v214\n\t"
    "v_exp_f32 v215, v215\n\t"
    "v_exp_f32 v216, v216\n\t"
    "v_mfma_f32_16x16x32_bf16 v[152:155], a[236:239], v[120:123], v[152:155]\n\t"
    "buffer_load_dwordx4 a[124:127], v8, %0, s40 offen offset:1024\n\t"
    "v_add_f32 v212, 1.0, v212\n\t"
    "v_add_f32 v213, 1.0, v213\n\t"
    "v_add_f32 v214, 1.0, v214\n\t"
    "v_mfma_f32_16x16x32_bf16 v[152:155], a[240:243], v[124:127], v[152:155]\n\t"
    "v_add_f32 v215, 1.0, v215\n\t"
    "v_add_f32 v216, 1.0, v216\n\t"
    "v_rcp_f32 v212, v212\n\t"
    "v_rcp_f32 v213, v213\n\t"
    "v_rcp_f32 v214, v214\n\t"
    "v_rcp_f32 v215, v215\n\t"
    "v_rcp_f32 v216, v216\n\t"
    "v_mul_f32 v148, v148, v212\n\t"
    "v_mul_f32 v149, v149, v213\n\t"
    "v_mul_f32 v150, v150, v214\n\t"
    "v_mul_f32 v151, v151, v215\n\t"
    "v_mul_f32 v188, v188, v216\n\t"
    "v_cvt_pk_bf16_f32 v212, v148, v149\n\t"
    "v_cvt_pk_bf16_f32 v213, v150, v151\n\t"
    "s_add_u32 s43, %12, 0xab9800\n\t"
    "buffer_store_dwordx2 v[212:213], v16, %0, s43 offen sc1\n\t"
    "v_cvt_pk_bf16_f32 v214, v188, v188\n\t"
    "buffer_store_short v214, v18, %0, s43 offen sc1\n\t"
    "s_waitcnt lgkmcnt(0)\n\t"
    "s_barrier\n\t"
    "v_mfma_f32_16x16x32_bf16 a[0:3], a[244:247], v[48:51], 0\n\t"
    "buffer_load_dwordx4 a[128:131], v8, %0, s40 offen offset:2048\n\t"
    "ds_read_b128 v[176:179], v27 offset:16384\n\t"
    "ds_read_b128 v[180:183], v27 offset:17408\n\t"
    "ds_read_b128 v[184:187], v27 offset:18432\n\t"
    "v_mfma_f32_16x16x32_bf16 a[0:3], a[248:251], v[52:55], a[0:3]\n\t"
    "buffer_load_dwordx4 a[132:135], v8, %0, s40 offen offset:3072\n\t"
    "ds_read_b32 v188, v29 offset:16384\n\t"
    "ds_read_b32 v189, v29 offset:17408\n\t"
    "ds_read_b32 v190, v29 offset:18432\n\t"
    "v_mfma_f32_16x16x32_bf16 a[0:3], a[252:255], v[56:59], a[0:3]\n\t"
    "ds_read_b32 v191, v29 offset:19456\n\t"
    "ds_read_b128 v[192:195], v31 offset:512\n\t"
    "ds_read_b128 v[196:199], v31 offset:528\n\t"
    "v_mfma_f32_16x16x32_bf16 a[0:3], v[228:231], v[60:63], a[0:3]\n\t"
    "v_mfma_f32_16x16x32_bf16 a[0:3], v[232:235], v[64:67], a[0:3]\n\t"
    "s_add_u32 s40, s40, 4096\n\t"
    "buffer_load_dwordx4 a[136:139], v8, %0, s40 offen offset:0\n\t"
    "v_mfma_f32_16x16x32_bf16 a[4:7], v[236:239], v[48:51], 0\n\t"
    "v_mfma_f32_16x16x32_bf16 a[4:7], v[240:243], v[52:55], a[4:7]\n\t"
    "v_mfma_f32_16x16x32_bf16 a[4:7], v[244:247], v[56:59], a[4:7]\n\t"
    "v_mfma_f32_16x16x32_bf16 a[4:7], v[248:251], v[60:63], a[4:7]\n\t"
    "v_mfma_f32_16x16x32_bf16 a[4:7], v[252:255], v[64:67], a[4:7]\n\t"
    "buffer_load_dwordx4 a[140:143], v8, %0, s40 offen offset:1024\n\t"
    "ds_write_b128 v24, a[0:3] offset:0\n\t"
    "s_waitcnt vmcnt(23)\n\t"
    "v_mfma_f32_16x16x32_bf16 a[8:11], a[64:67], v[48:51], 0\n\t"
    "s_waitcnt lgkmcnt(2)\n\t"
    "v_add_f32 v192, v192, v194\n\t"
    "v_add_f32 v193, v193, v195\n\t"
    "s_waitcnt lgkmcnt(1)\n\t"
    "v_add_f32 v196, v196, v198\n\t"
    "s_waitcnt vmcnt(22)\n\t"
    "v_mfma_f32_16x16x32_bf16 a[8:11], a[68:71], v[52:55], a[8:11]\n\t"
    "ds_write_b128 v25, a[4:7] offset:0\n\t"
    "v_add_f32 v197, v197, v199\n\t"
    "v_add_f32 v192, v192, v196\n\t"
    "s_waitcnt vmcnt(21)\n\t"
    "v_mfma_f32_16x16x32_bf16 a[8:11], a[72:75], v[56:59], a[8:11]\n\t"
    "buffer_load_dwordx4 a[144:147], v8, %0, s40 offen offset:2048\n\t"
    "v_add_f32 v193, v193, v197\n\t"
    "v_mul_f32 v222, 0xbacccccd, v192\n\t"
    "v_mul_f32 v223, 0x3acccccd, v193\n\t"
    "s_waitcnt vmcnt(21)\n\t"
    "v_mfma_f32_16x16x32_bf16 a[8:11], a[76:79], v[60:63], a[8:11]\n\t"
    "v_fma_f32 v223, -v222, v222, v223\n\t"
    "v_max_f32 v223, 0, v223\n\t"
    "v_add_f32 v226, 0x3727c5ac, v223\n\t"
    "s_waitcnt vmcnt(20)\n\t"
    "v_mfma_f32_16x16x32_bf16 a[8:11], a[80:83], v[64:67], a[8:11]\n\t"
    "v_rsq_f32 v162, v226\n\t"
    "v_add_f32 v176, v176, v180\n\t"
    "v_add_f32 v177, v177, v181\n\t"
    "s_waitcnt vmcnt(19)\n\t"
    "v_mfma_f32_16x16x32_bf16 a[12:15], a[84:87], v[48:51], 0\n\t"
    "v_add_f32 v178, v178, v182\n\t"
    "v_add_f32 v179, v179, v183\n\t"
    "v_mul_f32 v163, v222, v162\n\t"
    "s_waitcnt vmcnt(16)\n\t"
    "v_mfma_f32_16x16x32_bf16 a[12:15], a[88:91], v[52:55], a[12:15]\n\t"
    "buffer_load_dwordx4 a[148:151], v8, %0, s40 offen offset:3072\n\t"
    "ds_write_b128 v26, a[8:11] offset:0\n\t"
    "v_add_f32 v152, v152, v184\n\t"
    "v_add_f32 v153, v153, v185\n\t"
    "s_waitcnt vmcnt(16)\n\t"
    "v_mfma_f32_16x16x32_bf16 a[12:15], a[92:95], v[56:59], a[12:15]\n\t"
    "v_add_f32 v154, v154, v186\n\t"
    "v_add_f32 v155, v155, v187\n\t"
    "v_add_f32 v188, v188, v189\n\t"
    "s_waitcnt vmcnt(15)\n\t"
    "v_mfma_f32_16x16x32_bf16 a[12:15], a[96:99], v[60:63], a[12:15]\n\t"
    "v_add_f32 v190, v190, v191\n\t"
    "v_add_f32 v152, v152, v176\n\t"
    "v_add_f32 v153, v153, v177\n\t"
    "s_waitcnt vmcnt(14)\n\t"
    "v_mfma_f32_16x16x32_bf16 a[12:15], a[100:103], v[64:67], a[12:15]\n\t"
    "v_add_f32 v154, v154, v178\n\t"
    "v_add_f32 v155, v155, v179\n\t"
    "v_add_f32 v188, v188, v190\n\t"
    "s_waitcnt vmcnt(13)\n\t"
    "v_mfma_f32_16x16x32_bf16 v[148:151], a[104:107], v[48:51], 0\n\t"
    "s_add_u32 s40, s40, 4096\n\t"
    "buffer_load_dwordx4 a[152:155], v8, %0, s40 offen offset:0\n\t"
    "v_mul_f32 v212, v164, v163\n\t"
    "ds_write_b128 v28, a[12:15] offset:0\n\t"
    "v_mul_f32 v213, v165, v163\n\t"
    "s_waitcnt vmcnt(13)\n\t"
    "v_mfma_f32_16x16x32_bf16 v[148:151], a[108:111], v[52:55], v[148:151]\n\t"
    "v_mul_f32 v214, v166, v163\n\t"
    "v_mul_f32 v215, v167, v163\n\t"
    "v_mul_f32 v216, v168, v163\n\t"
    "s_waitcnt vmcnt(12)\n\t"
    "v_mfma_f32_16x16x32_bf16 v[148:151], a[112:115], v[56:59], v[148:151]\n\t"
    "v_fma_f32 v152, v152, v162, v212\n\t"
    "v_fma_f32 v153, v153, v162, v213\n\t"
    "v_fma_f32 v154, v154, v162, v214\n\t"
    "s_waitcnt vmcnt(11)\n\t"
    "v_mfma_f32_16x16x32_bf16 v[148:151], a[116:119], v[60:63], v[148:151]\n\t"
    "buffer_load_dwordx4 a[156:159], v8, %0, s40 offen offset:1024\n\t"
    "v_fma_f32 v155, v155, v162, v215\n\t"
    "v_fma_f32 v188, v188, v162, v216\n\t"
    "v_mul_f32 v212, v152, v152\n\t"
    "s_waitcnt vmcnt(11)\n\t"
    "v_mfma_f32_16x16x32_bf16 v[148:151], a[120:123], v[64:67], v[148:151]\n\t"
    "v_mul_f32 v213, v153, v153\n\t"
    "v_mul_f32 v214, v154, v154\n\t"
    "v_mul_f32 v215, v155, v155\n\t"
    "v_mul_f32 v216, v188, v188\n\t"
    "v_fma_f32 v212, v212, v11, v10\n\t"
    "v_fma_f32 v213, v213, v11, v10\n\t"
    "v_fma_f32 v214, v214, v11, v10\n\t"
    "v_fma_f32 v215, v215, v11, v10\n\t"
    "v_fma_f32 v216, v216, v11, v10\n\t"
    "v_mul_f32 v212, v212, v152\n\t"
    "v_mul_f32 v213, v213, v153\n\t"
    "v_mul_f32 v214, v214, v154\n\t"
    "v_mul_f32 v215, v215, v155\n\t"
    "v_mul_f32 v216, v216, v188\n\t"
    "v_exp_f32 v212, v212\n\t"
    "v_exp_f32 v213, v213\n\t"
    "v_exp_f32 v214, v214\n\t"
    "v_exp_f32 v215, v215\n\t"
    "v_exp_f32 v216, v216\n\t"
    "v_add_f32 v212, 1.0, v212\n\t"
    "v_add_f32 v213, 1.0, v213\n\t"
    "v_add_f32 v214, 1.0, v214\n\t"
    "v_add_f32 v215, 1.0, v215\n\t"
    "v_add_f32 v216, 1.0, v216\n\t"
    "v_rcp_f32 v212, v212\n\t"
    "v_rcp_f32 v213, v213\n\t"
    "v_rcp_f32 v214, v214\n\t"
    "v_rcp_f32 v215, v215\n\t"
    "v_rcp_f32 v216, v216\n\t"
    "v_mul_f32 v152, v152, v212\n\t"
    "v_mul_f32 v153, v153, v213\n\t"
    "v_mul_f32 v154, v154, v214\n\t"
    "v_mul_f32 v155, v155, v215\n\t"
    "v_mul_f32 v188, v188, v216\n\t"
    "v_cvt_pk_bf16_f32 v212, v152, v153\n\t"
    "v_cvt_pk_bf16_f32 v213, v154, v155\n\t"
    "s_add_u32 s43, %12, 0xae3800\n\t"
    "buffer_store_dwordx2 v[212:213], v16, %0, s43 offen sc1\n\t"
    "v_cvt_pk_bf16_f32 v214, v188, v188\n\t"
    "buffer_store_short v214, v18, %0, s43 offen sc1\n\t"
    "ds_read_b128 v[164:167], v32 offset:640\n\t"
    "ds_read_b128 v[172:175], v32 offset:1920\n\t"
    "ds_read_b32 v168, v33 offset:640\n\t"
    "ds_read_b32 v169, v33 offset:1920\n\t"
    "s_waitcnt lgkmcnt(3)\n\t"
    "s_waitcnt lgkmcnt(2)\n\t"
    "v_add_f32 v164, v164, v172\n\t"
    "v_add_f32 v165, v165, v173\n\t"
    "v_add_f32 v166, v166, v174\n\t"
    "v_add_f32 v167, v167, v175\n\t"
    "s_waitcnt lgkmcnt(1)\n\t"
    "s_waitcnt lgkmcnt(0)\n\t"
    "v_add_f32 v168, v168, v169\n\t"
    "s_waitcnt lgkmcnt(0)\n\t"
    "s_barrier\n\t"
    "v_mfma_f32_16x16x32_bf16 a[0:3], a[244:247], v[68:71], 0\n\t"
    "buffer_load_dwordx4 a[160:163], v8, %0, s40 offen offset:2048\n\t"
    "ds_read_b128 v[176:179], v27 offset:0\n\t"
    "ds_read_b128 v[180:183], v27 offset:1024\n\t"
    "ds_read_b128 v[184:187], v27 offset:2048\n\t"
    "v_mfma_f32_16x16x32_bf16 a[0:3], a[248:251], v[72:75], a[0:3]\n\t"
    "buffer_load_dwordx4 a[164:167], v8, %0, s40 offen offset:3072\n\t"
    "ds_read_b32 v188, v29 offset:0\n\t"
    "ds_read_b32 v189, v29 offset:1024\n\t"
    "ds_read_b32 v190, v29 offset:2048\n\t"
    "v_mfma_f32_16x16x32_bf16 a[0:3], a[252:255], v[76:79], a[0:3]\n\t"
    "s_add_u32 s40, s40, 4096\n\t"
    "buffer_load_dwordx4 a[168:171], v8, %0, s40 offen offset:0\n\t"
    "ds_read_b32 v191, v29 offset:3072\n\t"
    "v_mfma_f32_16x16x32_bf16 a[0:3], v[228:231], v[80:83], a[0:3]\n\t"
    "buffer_load_dwordx4 a[172:175], v8, %0, s40 offen offset:1024\n\t"
    "v_mfma_f32_16x16x32_bf16 a[0:3], v[232:235], v[84:87], a[0:3]\n\t"
    "buffer_load_dwordx4 a[176:179], v8, %0, s40 offen offset:2048\n\t"
    "v_mfma_f32_16x16x32_bf16 a[4:7], v[236:239], v[68:71], 0\n\t"
    "v_mfma_f32_16x16x32_bf16 a[4:7], v[240:243], v[72:75], a[4:7]\n\t"
    "v_mfma_f32_16x16x32_bf16 a[4:7], v[244:247], v[76:79], a[4:7]\n\t"
    "buffer_load_dwordx4 a[180:183], v8, %0, s40 offen offset:3072\n\t"
    "v_mfma_f32_16x16x32_bf16 a[4:7], v[248:251], v[80:83], a[4:7]\n\t"
    "v_mfma_f32_16x16x32_bf16 a[4:7], v[252:255], v[84:87], a[4:7]\n\t"
    "v_mfma_f32_16x16x32_bf16 a[8:11], a[64:67], v[68:71], 0\n\t"
    "ds_write_b128 v24, a[0:3] offset:16384\n\t"
    "s_waitcnt lgkmcnt(7)\n\t"
    "s_waitcnt lgkmcnt(6)\n\t"
    "v_add_f32 v176, v176, v180\n\t"
    "v_add_f32 v177, v177, v181\n\t"
    "v_mfma_f32_16x16x32_bf16 a[8:11], a[68:71], v[72:75], a[8:11]\n\t"
    "s_add_u32 s40, s40, 4096\n\t"
    "buffer_load_dwordx4 a[184:187], v8, %0, s40 offen offset:0\n\t"
    "ds_write_b128 v25, a[4:7] offset:16384\n\t"
    "v_add_f32 v178, v178, v182\n\t"
    "v_add_f32 v179, v179, v183\n\t"
    "v_mfma_f32_16x16x32_bf16 a[8:11], a[72:75], v[76:79], a[8:11]\n\t"
    "s_waitcnt lgkmcnt(6)\n\t"
    "v_add_f32 v148, v148, v184\n\t"
    "v_add_f32 v149, v149, v185\n\t"
    "v_add_f32 v150, v150, v186\n\t"
    "v_mfma_f32_16x16x32_bf16 a[8:11], a[76:79], v[80:83], a[8:11]\n\t"
    "v_add_f32 v151, v151, v187\n\t"
    "s_waitcnt lgkmcnt(5)\n\t"
    "s_waitcnt lgkmcnt(4)\n\t"
    "v_add_f32 v188, v188, v189\n\t"
    "s_waitcnt lgkmcnt(3)\n\t"
    "s_waitcnt lgkmcnt(2)\n\t"
    "v_add_f32 v190, v190, v191\n\t"
    "v_mfma_f32_16x16x32_bf16 a[8:11], a[80:83], v[84:87], a[8:11]\n\t"
    "v_add_f32 v148, v148, v176\n\t"
    "v_add_f32 v149, v149, v177\n\t"
    "v_add_f32 v150, v150, v178\n\t"
    "v_mfma_f32_16x16x32_bf16 a[12:15], a[84:87], v[68:71], 0\n\t"
    "buffer_load_dwordx4 a[188:191], v8, %0, s40 offen offset:1024\n\t"
    "v_add_f32 v151, v151, v179\n\t"
    "v_add_f32 v188, v188, v190\n\t"
    "v_mul_f32 v212, v164, v159\n\t"
    "v_mfma_f32_16x16x32_bf16 a[12:15], a[88:91], v[72:75], a[12:15]\n\t"
    "ds_write_b128 v26, a[8:11] offset:16384\n\t"
    "v_mul_f32 v213, v165, v159\n\t"
    "v_mul_f32 v214, v166, v159\n\t"
    "v_mfma_f32_16x16x32_bf16 a[12:15], a[92:95], v[76:79], a[12:15]\n\t"
    "v_mul_f32 v215, v167, v159\n\t"
    "v_mul_f32 v216, v168, v159\n\t"
    "v_fma_f32 v148, v148, v158, v212\n\t"
    "v_mfma_f32_16x16x32_bf16 a[12:15], a[96:99], v[80:83], a[12:15]\n\t"
    "v_fma_f32 v149, v149, v158, v213\n\t"
    "v_fma_f32 v150, v150, v158, v214\n\t"
    "v_fma_f32 v151, v151, v158, v215\n\t"
    "v_mfma_f32_16x16x32_bf16 a[12:15], a[100:103], v[84:87], a[12:15]\n\t"
    "v_fma_f32 v188, v188, v158, v216\n\t"
    "v_mul_f32 v212, v148, v148\n\t"
    "v_mul_f32 v213, v149, v149\n\t"
    "v_mfma_f32_16x16x32_bf16 v[152:155], a[104:107], v[68:71], 0\n\t"
    "v_mul_f32 v214, v150, v150\n\t"
    "v_mul_f32 v215, v151, v151\n\t"
    "v_mul_f32 v216, v188, v188\n\t"
    "v_mfma_f32_16x16x32_bf16 v[152:155], a[108:111], v[72:75], v[152:155]\n\t"
    "ds_write_b128 v28, a[12:15] offset:16384\n\t"
    "v_fma_f32 v212, v212, v11, v10\n\t"
    "v_fma_f32 v213, v213, v11, v10\n\t"
    "v_mfma_f32_16x16x32_bf16 v[152:155], a[112:115], v[76:79], v[152:155]\n\t"
    "v_fma_f32 v214, v214, v11, v10\n\t"
    "v_fma_f32 v215, v215, v11, v10\n\t"
    "v_fma_f32 v216, v216, v11, v10\n\t"
    "v_mfma_f32_16x16x32_bf16 v[152:155], a[116:119], v[80:83], v[152:155]\n\t"
    "v_mul_f32 v212, v212, v148\n\t"
    "v_mul_f32 v213, v213, v149\n\t"
    "v_mul_f32 v214, v214, v150\n\t"
    "v_mfma_f32_16x16x32_bf16 v[152:155], a[120:123], v[84:87], v[152:155]\n\t"
    "v_mul_f32 v215, v215, v151\n\t"
    "v_mul_f32 v216, v216, v188\n\t"
    "v_exp_f32 v212, v212\n\t"
    "v_exp_f32 v213, v213\n\t"
    "v_exp_f32 v214, v214\n\t"
    "v_exp_f32 v215, v215\n\t"
    "v_exp_f32 v216, v216\n\t"
    "v_add_f32 v212, 1.0, v212\n\t"
    "v_add_f32 v213, 1.0, v213\n\t"
    "v_add_f32 v214, 1.0, v214\n\t"
    "v_add_f32 v215, 1.0, v215\n\t"
    "v_add_f32 v216, 1.0, v216\n\t"
    "v_rcp_f32 v212, v212\n\t"
    "v_rcp_f32 v213, v213\n\t"
    "v_rcp_f32 v214, v214\n\t"
    "v_rcp_f32 v215, v215\n\t"
    "v_rcp_f32 v216, v216\n\t"
    "v_mul_f32 v148, v148, v212\n\t"
    "v_mul_f32 v149, v149, v213\n\t"
    "v_mul_f32 v150, v150, v214\n\t"
    "v_mul_f32 v151, v151, v215\n\t"
    "v_mul_f32 v188, v188, v216\n\t"
    "v_cvt_pk_bf16_f32 v212, v148, v149\n\t"
    "v_cvt_pk_bf16_f32 v213, v150, v151\n\t"
    "s_add_u32 s43, %12, 0xacc800\n\t"
    "buffer_store_dwordx2 v[212:213], v16, %0, s43 offen sc1\n\t"
    "v_cvt_pk_bf16_f32 v214, v188, v188\n\t"
    "buffer_store_short v214, v18, %0, s43 offen sc1\n\t"
    "s_waitcnt lgkmcnt(0)\n\t"
    "s_barrier\n\t"
    "v_mfma_f32_16x16x32_bf16 a[0:3], a[244:247], v[88:91], 0\n\t"
    "buffer_load_dwordx4 a[192:195], v8, %0, s40 offen offset:2048\n\t"
    "ds_read_b128 v[176:179], v27 offset:16384\n\t"
    "ds_read_b128 v[180:183], v27 offset:17408\n\t"
    "ds_read_b128 v[184:187], v27 offset:18432\n\t"
    "v_mfma_f32_16x16x32_bf16 a[0:3], a[248:251], v[92:95], a[0:3]\n\t"
    "buffer_load_dwordx4 a[196:199], v8, %0, s40 offen offset:3072\n\t"
    "ds_read_b32 v188, v29 offset:16384\n\t"
    "ds_read_b32 v189, v29 offset:17408\n\t"
    "ds_read_b32 v190, v29 offset:18432\n\t"
    "v_mfma_f32_16x16x32_bf16 a[0:3], a[252:255], v[96:99], a[0:3]\n\t"
    "ds_read_b32 v191, v29 offset:19456\n\t"
    "v_mfma_f32_16x16x32_bf16 a[0:3], v[228:231], v[100:103], a[0:3]\n\t"
    "v_mfma_f32_16x16x32_bf16 a[0:3], v[232:235], v[104:107], a[0:3]\n\t"
    "v_mfma_f32_16x16x32_bf16 a[4:7], v[236:239], v[88:91], 0\n\t"
    "s_add_u32 s40, s40, 4096\n\t"
    "buffer_load_dwordx4 a[200:203], v8, %0, s40 offen offset:0\n\t"
    "v_mfma_f32_16x16x32_bf16 a[4:7], v[240:243], v[92:95], a[4:7]\n\t"
    "v_mfma_f32_16x16x32_bf16 a[4:7], v[244:247], v[96:99], a[4:7]\n\t"
    "v_mfma_f32_16x16x32_bf16 a[4:7], v[248:251], v[100:103], a[4:7]\n\t"
    "s_waitcnt vmcnt(13)\n\t"
    "s_add_u32 s55, %13, 0x1bc3800\n\t"
    "buffer_store_dword v12, v36, %0, s55 offen sc1\n\t"
    "ds_write_b128 v24, a[0:3] offset:0\n\t"
    "v_mfma_f32_16x16x32_bf16 a[4:7], v[252:255], v[104:107], a[4:7]\n\t"
    "s_waitcnt lgkmcnt(7)\n\t"
    "s_waitcnt lgkmcnt(6)\n\t"
    "v_add_f32 v176, v176, v180\n\t"
    "v_add_f32 v177, v177, v181\n\t"
    "v_add_f32 v178, v178, v182\n\t"
    "v_mfma_f32_16x16x32_bf16 a[8:11], a[64:67], v[88:91], 0\n\t"
    "buffer_load_dwordx4 a[204:207], v8, %0, s40 offen offset:1024\n\t"
    "v_add_f32 v179, v179, v183\n\t"
    "ds_write_b128 v25, a[4:7] offset:0\n\t"
    "s_waitcnt lgkmcnt(6)\n\t"
    "v_add_f32 v152, v152, v184\n\t"
    "v_mfma_f32_16x16x32_bf16 a[8:11], a[68:71], v[92:95], a[8:11]\n\t"
    "v_add_f32 v153, v153, v185\n\t"
    "v_add_f32 v154, v154, v186\n\t"
    "v_add_f32 v155, v155, v187\n\t"
    "v_mfma_f32_16x16x32_bf16 a[8:11], a[72:75], v[96:99], a[8:11]\n\t"
    "s_waitcnt lgkmcnt(5)\n\t"
    "s_waitcnt lgkmcnt(4)\n\t"
    "v_add_f32 v188, v188, v189\n\t"
    "s_waitcnt lgkmcnt(3)\n\t"
    "s_waitcnt lgkmcnt(2)\n\t"
    "v_add_f32 v190, v190, v191\n\t"
    "v_add_f32 v152, v152, v176\n\t"
    "v_mfma_f32_16x16x32_bf16 a[8:11], a[76:79], v[100:103], a[8:11]\n\t"
    "buffer_load_dwordx4 a[208:211], v8, %0, s40 offen offset:2048\n\t"
    "v_add_f32 v153, v153, v177\n\t"
    "v_add_f32 v154, v154, v178\n\t"
    "v_add_f32 v155, v155, v179\n\t"
    "v_mfma_f32_16x16x32_bf16 a[8:11], a[80:83], v[104:107], a[8:11]\n\t"
    "v_add_f32 v188, v188, v190\n\t"
    "v_mul_f32 v212, v164, v161\n\t"
    "v_mul_f32 v213, v165, v161\n\t"
    "v_mfma_f32_16x16x32_bf16 a[12:15], a[84:87], v[88:91], 0\n\t"
    "v_mul_f32 v214, v166, v161\n\t"
    "v_mul_f32 v215, v167, v161\n\t"
    "v_mul_f32 v216, v168, v161\n\t"
    "v_mfma_f32_16x16x32_bf16 a[12:15], a[88:91], v[92:95], a[12:15]\n\t"
    "ds_write_b128 v26, a[8:11] offset:0\n\t"
    "v_fma_f32 v152, v152, v160, v212\n\t"
    "v_fma_f32 v153, v153, v160, v213\n\t"
    "v_mfma_f32_16x16x32_bf16 a[12:15], a[92:95], v[96:99], a[12:15]\n\t"
    "buffer_load_dwordx4 a[212:215], v8, %0, s40 offen offset:3072\n\t"
    "v_fma_f32 v154, v154, v160, v214\n\t"
    "v_fma_f32 v155, v155, v160, v215\n\t"
    "v_fma_f32 v188, v188, v160, v216\n\t"
    "v_mfma_f32_16x16x32_bf16 a[12:15], a[96:99], v[100:103], a[12:15]\n\t"
    "v_mul_f32 v212, v152, v152\n\t"
    "v_mul_f32 v213, v153, v153\n\t"
    "v_mul_f32 v214, v154, v154\n\t"
    "v_mfma_f32_16x16x32_bf16 a[12:15], a[100:103], v[104:107], a[12:15]\n\t"
    "v_mul_f32 v215, v155, v155\n\t"
    "v_mul_f32 v216, v188, v188\n\t"
    "v_fma_f32 v212, v212, v11, v10\n\t"
    "v_mfma_f32_16x16x32_bf16 v[148:151], a[104:107], v[88:91], 0\n\t"
    "v_fma_f32 v213, v213, v11, v10\n\t"
    "v_fma_f32 v214, v214, v11, v10\n\t"
    "v_fma_f32 v215, v215, v11, v10\n\t"
    "v_mfma_f32_16x16x32_bf16 v[148:151], a[108:111], v[92:95], v[148:151]\n\t"
    "s_add_u32 s40, s40, 4096\n\t"
    "buffer_load_dwordx4 a[216:219], v8, %0, s40 offen offset:0\n\t"
    "ds_write_b128 v28, a[12:15] offset:0\n\t"
    "v_fma_f32 v216, v216, v11, v10\n\t"
    "v_mul_f32 v212, v212, v152\n\t"
    "v_mfma_f32_16x16x32_bf16 v[148:151], a[112:115], v[96:99], v[148:151]\n\t"
    "v_mul_f32 v213, v213, v153\n\t"
    "v_mul_f32 v214, v214, v154\n\t"
    "v_mul_f32 v215, v215, v155\n\t"
    "v_mfma_f32_16x16x32_bf16 v[148:151], a[116:119], v[100:103], v[148:151]\n\t"
    "v_mul_f32 v216, v216, v188\n\t"
    "v_exp_f32 v212, v212\n\t"
    "v_exp_f32 v213, v213\n\t"
    "v_mfma_f32_16x16x32_bf16 v[148:151], a[120:123], v[104:107], v[148:151]\n\t"
    "buffer_load_dwordx4 a[220:223], v8, %0, s40 offen offset:1024\n\t"
    "v_exp_f32 v214, v214\n\t"
    "v_exp_f32 v215, v215\n\t"
    "v_exp_f32 v216, v216\n\t"
    "v_add_f32 v212, 1.0, v212\n\t"
    "v_add_f32 v213, 1.0, v213\n\t"
    "v_add_f32 v214, 1.0, v214\n\t"
    "v_add_f32 v215, 1.0, v215\n\t"
    "v_add_f32 v216, 1.0, v216\n\t"
    "v_rcp_f32 v212, v212\n\t"
    "v_rcp_f32 v213, v213\n\t"
    "v_rcp_f32 v214, v214\n\t"
    "v_rcp_f32 v215, v215\n\t"
    "v_rcp_f32 v216, v216\n\t"
    "v_mul_f32 v152, v152, v212\n\t"
    "v_mul_f32 v153, v153, v213\n\t"
    "v_mul_f32 v154, v154, v214\n\t"
    "v_mul_f32 v155, v155, v215\n\t"
    "v_mul_f32 v188, v188, v216\n\t"
    "v_cvt_pk_bf16_f32 v212, v152, v153\n\t"
    "v_cvt_pk_bf16_f32 v213, v154, v155\n\t"
    "s_add_u32 s43, %12, 0xada800\n\t"
    "buffer_store_dwordx2 v[212:213], v16, %0, s43 offen sc1\n\t"
    "v_cvt_pk_bf16_f32 v214, v188, v188\n\t"
    "buffer_store_short v214, v18, %0, s43 offen sc1\n\t"
    "s_waitcnt lgkmcnt(0)\n\t"
    "s_barrier\n\t"
    "v_mfma_f32_16x16x32_bf16 a[0:3], a[244:247], v[108:111], 0\n\t"
    "buffer_load_dwordx4 a[224:227], v8, %0, s40 offen offset:2048\n\t"
    "ds_read_b128 v[176:179], v27 offset:0\n\t"
    "ds_read_b128 v[180:183], v27 offset:1024\n\t"
    "ds_read_b128 v[184:187], v27 offset:2048\n\t"
    "v_mfma_f32_16x16x32_bf16 a[0:3], a[248:251], v[112:115], a[0:3]\n\t"
    "buffer_load_dwordx4 a[228:231], v8, %0, s40 offen offset:3072\n\t"
    "ds_read_b32 v188, v29 offset:0\n\t"
    "ds_read_b32 v189, v29 offset:1024\n\t"
    "ds_read_b32 v190, v29 offset:2048\n\t"
    "v_mfma_f32_16x16x32_bf16 a[0:3], a[252:255], v[116:119], a[0:3]\n\t"
    "ds_read_b32 v191, v29 offset:3072\n\t"
    "v_mfma_f32_16x16x32_bf16 a[0:3], v[228:231], v[120:123], a[0:3]\n\t"
    "v_mfma_f32_16x16x32_bf16 a[0:3], v[232:235], v[124:127], a[0:3]\n\t"
    "v_mfma_f32_16x16x32_bf16 a[4:7], v[236:239], v[108:111], 0\n\t"
    "s_add_u32 s40, s40, 4096\n\t"
    "buffer_load_dwordx4 a[232:235], v8, %0, s40 offen offset:0\n\t"
    "v_mfma_f32_16x16x32_bf16 a[4:7], v[240:243], v[112:115], a[4:7]\n\t"
    "v_mfma_f32_16x16x32_bf16 a[4:7], v[244:247], v[116:119], a[4:7]\n\t"
    "v_mfma_f32_16x16x32_bf16 a[4:7], v[248:251], v[120:123], a[4:7]\n\t"
    "v_mfma_f32_16x16x32_bf16 a[4:7], v[252:255], v[124:127], a[4:7]\n\t"
    "v_mfma_f32_16x16x32_bf16 a[8:11], a[64:67], v[108:111], 0\n\t"
    "buffer_load_dwordx4 a[236:239], v8, %0, s40 offen offset:1024\n\t"
    "ds_write_b128 v24, a[0:3] offset:16384\n\t"
    "s_waitcnt lgkmcnt(7)\n\t"
    "s_waitcnt lgkmcnt(6)\n\t"
    "v_add_f32 v176, v176, v180\n\t"
    "v_add_f32 v177, v177, v181\n\t"
    "v_mfma_f32_16x16x32_bf16 a[8:11], a[68:71], v[112:115], a[8:11]\n\t"
    "ds_write_b128 v25, a[4:7] offset:16384\n\t"
    "v_add_f32 v178, v178, v182\n\t"
    "v_add_f32 v179, v179, v183\n\t"
    "v_mfma_f32_16x16x32_bf16 a[8:11], a[72:75], v[116:119], a[8:11]\n\t"
    "s_waitcnt lgkmcnt(6)\n\t"
    "v_add_f32 v148, v148, v184\n\t"
    "v_add_f32 v149, v149, v185\n\t"
    "v_add_f32 v150, v150, v186\n\t"
    "v_mfma_f32_16x16x32_bf16 a[8:11], a[76:79], v[120:123], a[8:11]\n\t"
    "v_add_f32 v151, v151, v187\n\t"
    "s_waitcnt lgkmcnt(5)\n\t"
    "s_waitcnt lgkmcnt(4)\n\t"
    "v_add_f32 v188, v188, v189\n\t"
    "s_waitcnt lgkmcnt(3)\n\t"
    "s_waitcnt lgkmcnt(2)\n\t"
    "v_add_f32 v190, v190, v191\n\t"
    "v_mfma_f32_16x16x32_bf16 a[8:11], a[80:83], v[124:127], a[8:11]\n\t"
    "buffer_load_dwordx4 a[240:243], v8, %0, s40 offen offset:2048\n\t"
    "v_add_f32 v148, v148, v176\n\t"
    "v_add_f32 v149, v149, v177\n\t"
    "v_add_f32 v150, v150, v178\n\t"
    "v_mfma_f32_16x16x32_bf16 a[12:15], a[84:87], v[108:111], 0\n\t"
    "v_add_f32 v151, v151, v179\n\t"
    "v_add_f32 v188, v188, v190\n\t"
    "v_mul_f32 v212, v164, v157\n\t"
    "v_mfma_f32_16x16x32_bf16 a[12:15], a[88:91], v[112:115], a[12:15]\n\t"
    "ds_write_b128 v26, a[8:11] offset:16384\n\t"
    "v_mul_f32 v213, v165, v157\n\t"
    "v_mul_f32 v214, v166, v157\n\t"
    "v_mfma_f32_16x16x32_bf16 a[12:15], a[92:95], v[116:119], a[12:15]\n\t"
    "v_mul_f32 v215, v167, v157\n\t"
    "v_mul_f32 v216, v168, v157\n\t"
    "v_fma_f32 v148, v148, v156, v212\n\t"
    "v_mfma_f32_16x16x32_bf16 a[12:15], a[96:99], v[120:123], a[12:15]\n\t"
    "buffer_load_dwordx4 a[244:247], v8, %0, s40 offen offset:3072\n\t"
    "v_fma_f32 v149, v149, v156, v213\n\t"
    "v_fma_f32 v150, v150, v156, v214\n\t"
    "v_fma_f32 v151, v151, v156, v215\n\t"
    "v_mfma_f32_16x16x32_bf16 a[12:15], a[100:103], v[124:127], a[12:15]\n\t"
    "v_fma_f32 v188, v188, v156, v216\n\t"
    "v_mul_f32 v212, v148, v148\n\t"
    "v_mul_f32 v213, v149, v149\n\t"
    "v_mfma_f32_16x16x32_bf16 v[152:155], a[104:107], v[108:111], 0\n\t"
    "v_mul_f32 v214, v150, v150\n\t"
    "v_mul_f32 v215, v151, v151\n\t"
    "v_mul_f32 v216, v188, v188\n\t"
    "v_mfma_f32_16x16x32_bf16 v[152:155], a[108:111], v[112:115], v[152:155]\n\t"
    "s_add_u32 s40, s40, 4096\n\t"
    "buffer_load_dwordx4 a[248:251], v8, %0, s40 offen offset:0\n\t"
    "ds_write_b128 v28, a[12:15] offset:16384\n\t"
    "v_fma_f32 v212, v212, v11, v10\n\t"
    "v_fma_f32 v213, v213, v11, v10\n\t"
    "v_mfma_f32_16x16x32_bf16 v[152:155], a[112:115], v[116:119], v[152:155]\n\t"
    "v_fma_f32 v214, v214, v11, v10\n\t"
    "v_fma_f32 v215, v215, v11, v10\n\t"
    "v_fma_f32 v216, v216, v11, v10\n\t"
    "v_mfma_f32_16x16x32_bf16 v[152:155], a[116:119], v[120:123], v[152:155]\n\t"
    "v_mul_f32 v212, v212, v148\n\t"
    "v_mul_f32 v213, v213, v149\n\t"
    "v_mul_f32 v214, v214, v150\n\t"
    "v_mfma_f32_16x16x32_bf16 v[152:155], a[120:123], v[124:127], v[152:155]\n\t"
    "v_mul_f32 v215, v215, v151\n\t"
    "v_mul_f32 v216, v216, v188\n\t"
    "v_exp_f32 v212, v212\n\t"
    "v_exp_f32 v213, v213\n\t"
    "v_exp_f32 v214, v214\n\t"
    "v_exp_f32 v215, v215\n\t"
    "v_exp_f32 v216, v216\n\t"
    "v_add_f32 v212, 1.0, v212\n\t"
    "v_add_f32 v213, 1.0, v213\n\t"
    "v_add_f32 v214, 1.0, v214\n\t"
    "v_add_f32 v215, 1.0, v215\n\t"
    "v_add_f32 v216, 1.0, v216\n\t"
    "v_rcp_f32 v212, v212\n\t"
    "v_rcp_f32 v213, v213\n\t"
    "v_rcp_f32 v214, v214\n\t"
    "v_rcp_f32 v215, v215\n\t"
    "v_rcp_f32 v216, v216\n\t"
    "v_mul_f32 v148, v148, v212\n\t"
    "v_mul_f32 v149, v149, v213\n\t"
    "v_mul_f32 v150, v150, v214\n\t"
    "v_mul_f32 v151, v151, v215\n\t"
    "v_mul_f32 v188, v188, v216\n\t"
    "v_cvt_pk_bf16_f32 v212, v148, v149\n\t"
    "v_cvt_pk_bf16_f32 v213, v150, v151\n\t"
    "s_add_u32 s43, %12, 0xabe800\n\t"
    "buffer_store_dwordx2 v[212:213], v16, %0, s43 offen sc1\n\t"
    "v_cvt_pk_bf16_f32 v214, v188, v188\n\t"
    "buffer_store_short v214, v18, %0, s43 offen sc1\n\t"
    "s_waitcnt lgkmcnt(0)\n\t"
    "s_barrier\n\t"
    "v_mfma_f32_16x16x32_bf16 a[0:3], a[124:127], v[48:51], 0\n\t"
    "buffer_load_dwordx4 a[252:255], v8, %0, s40 offen offset:1024\n\t"
    "ds_read_b128 v[176:179], v27 offset:16384\n\t"
    "ds_read_b128 v[180:183], v27 offset:17408\n\t"
    "ds_read_b128 v[184:187], v27 offset:18432\n\t"
    "v_mfma_f32_16x16x32_bf16 a[0:3], a[128:131], v[52:55], a[0:3]\n\t"
    "buffer_load_dwordx4 v[228:231], v8, %0, s40 offen offset:2048\n\t"
    "ds_read_b32 v188, v29 offset:16384\n\t"
    "ds_read_b32 v189, v29 offset:17408\n\t"
    "ds_read_b32 v190, v29 offset:18432\n\t"
    "v_mfma_f32_16x16x32_bf16 a[0:3], a[132:135], v[56:59], a[0:3]\n\t"
    "buffer_load_dwordx4 v[232:235], v8, %0, s40 offen offset:3072\n\t"
    "ds_read_b32 v191, v29 offset:19456\n\t"
    "v_mfma_f32_16x16x32_bf16 a[0:3], a[136:139], v[60:63], a[0:3]\n\t"
    "v_mfma_f32_16x16x32_bf16 a[0:3], a[140:143], v[64:67], a[0:3]\n\t"
    "v_mfma_f32_16x16x32_bf16 a[4:7], a[144:147], v[48:51], 0\n\t"
    "s_add_u32 s40, s40, 4096\n\t"
    "buffer_load_dwordx4 v[236:239], v8, %0, s40 offen offset:0\n\t"
    "v_mfma_f32_16x16x32_bf16 a[4:7], a[148:151], v[52:55], a[4:7]\n\t"
    "v_mfma_f32_16x16x32_bf16 a[4:7], a[152:155], v[56:59], a[4:7]\n\t"
    "v_mfma_f32_16x16x32_bf16 a[4:7], a[156:159], v[60:63], a[4:7]\n\t"
    "s_waitcnt vmcnt(33)\n\t"
    "v_mfma_f32_16x16x32_bf16 a[4:7], a[160:163], v[64:67], a[4:7]\n\t"
    "ds_write_b128 v24, a[0:3] offset:0\n\t"
    "s_waitcnt vmcnt(32)\n\t"
    "v_mfma_f32_16x16x32_bf16 a[8:11], a[164:167], v[48:51], 0\n\t"
    "s_add_u32 s47, %13, 0x1bc3800\n\t"
    "buffer_load_dword v13, v35, %0, s47 offen sc1\n\t"
    "s_waitcnt lgkmcnt(7)\n\t"
    "s_waitcnt lgkmcnt(6)\n\t"
    "v_add_f32 v176, v176, v180\n\t"
    "ds_write_b128 v25, a[4:7] offset:0\n\t"
    "v_add_f32 v177, v177, v181\n\t"
    "s_waitcnt vmcnt(32)\n\t"
    "v_mfma_f32_16x16x32_bf16 a[8:11], a[168:171], v[52:55], a[8:11]\n\t"
    "v_add_f32 v178, v178, v182\n\t"
    "v_add_f32 v179, v179, v183\n\t"
    "s_waitcnt lgkmcnt(6)\n\t"
    "v_add_f32 v152, v152, v184\n\t"
    "s_waitcnt vmcnt(31)\n\t"
    "v_mfma_f32_16x16x32_bf16 a[8:11], a[172:175], v[56:59], a[8:11]\n\t"
    "v_add_f32 v153, v153, v185\n\t"
    "v_add_f32 v154, v154, v186\n\t"
    "v_add_f32 v155, v155, v187\n\t"
    "s_waitcnt vmcnt(30)\n\t"
    "v_mfma_f32_16x16x32_bf16 a[8:11], a[176:179], v[60:63], a[8:11]\n\t"
    "buffer_load_dwordx4 v[240:243], v8, %0, s40 offen offset:1024\n\t"
    "s_waitcnt lgkmcnt(5)\n\t"
    "s_waitcnt lgkmcnt(4)\n\t"
    "v_add_f32 v188, v188, v189\n\t"
    "s_waitcnt lgkmcnt(3)\n\t"
    "s_waitcnt lgkmcnt(2)\n\t"
    "v_add_f32 v190, v190, v191\n\t"
    "v_add_f32 v152, v152, v176\n\t"
    "s_waitcnt vmcnt(30)\n\t"
    "v_mfma_f32_16x16x32_bf16 a[8:11], a[180:183], v[64:67], a[8:11]\n\t"
    "v_add_f32 v153, v153, v177\n\t"
    "v_add_f32 v154, v154, v178\n\t"
    "v_add_f32 v155, v155, v179\n\t"
    "s_waitcnt vmcnt(29)\n\t"
    "v_mfma_f32_16x16x32_bf16 v[148:151], a[184:187], v[48:51], 0\n\t"
    "v_add_f32 v188, v188, v190\n\t"
    "v_mul_f32 v212, v164, v163\n\t"
    "v_mul_f32 v213, v165, v163\n\t"
    "s_waitcnt vmcnt(28)\n\t"
    "v_mfma_f32_16x16x32_bf16 v[148:151], a[188:191], v[52:55], v[148:151]\n\t"
    "ds_write_b128 v26, a[8:11] offset:0\n\t"
    "v_mul_f32 v214, v166, v163\n\t"
    "v_mul_f32 v215, v167, v163\n\t"
    "s_waitcnt vmcnt(25)\n\t"
    "v_mfma_f32_16x16x32_bf16 v[148:151], a[192:195], v[56:59], v[148:151]\n\t"
    "buffer_load_dwordx4 v[244:247], v8, %0, s40 offen offset:2048\n\t"
    "v_mul_f32 v216, v168, v163\n\t"
    "v_fma_f32 v152, v152, v162, v212\n\t"
    "v_fma_f32 v153, v153, v162, v213\n\t"
    "s_waitcnt vmcnt(25)\n\t"
    "v_mfma_f32_16x16x32_bf16 v[148:151], a[196:199], v[60:63], v[148:151]\n\t"
    "v_fma_f32 v154, v154, v162, v214\n\t"
    "v_fma_f32 v155, v155, v162, v215\n\t"
    "v_fma_f32 v188, v188, v162, v216\n\t"
    "s_waitcnt vmcnt(24)\n\t"
    "v_mfma_f32_16x16x32_bf16 v[148:151], a[200:203], v[64:67], v[148:151]\n\t"
    "v_mul_f32 v212, v152, v152\n\t"
    "v_mul_f32 v213, v153, v153\n\t"
    "v_mul_f32 v214, v154, v154\n\t"
    "v_mul_f32 v215, v155, v155\n\t"
    "v_mul_f32 v216, v188, v188\n\t"
    "v_fma_f32 v212, v212, v11, v10\n\t"
    "v_fma_f32 v213, v213, v11, v10\n\t"
    "v_fma_f32 v214, v214, v11, v10\n\t"
    "v_fma_f32 v215, v215, v11, v10\n\t"
    "v_fma_f32 v216, v216, v11, v10\n\t"
    "v_mul_f32 v212, v212, v152\n\t"
    "v_mul_f32 v213, v213, v153\n\t"
    "v_mul_f32 v214, v214, v154\n\t"
    "v_mul_f32 v215, v215, v155\n\t"
    "v_mul_f32 v216, v216, v188\n\t"
    "v_exp_f32 v212, v212\n\t"
    "v_exp_f32 v213, v213\n\t"
    "v_exp_f32 v214, v214\n\t"
    "v_exp_f32 v215, v215\n\t"
    "v_exp_f32 v216, v216\n\t"
    "v_add_f32 v212, 1.0, v212\n\t"
    "v_add_f32 v213, 1.0, v213\n\t"
    "v_add_f32 v214, 1.0, v214\n\t"
    "v_add_f32 v215, 1.0, v215\n\t"
    "v_add_f32 v216, 1.0, v216\n\t"
    "v_rcp_f32 v212, v212\n\t"
    "v_rcp_f32 v213, v213\n\t"
    "v_rcp_f32 v214, v214\n\t"
    "v_rcp_f32 v215, v215\n\t"
    "v_rcp_f32 v216, v216\n\t"
    "v_mul_f32 v152, v152, v212\n\t"
    "v_mul_f32 v153, v153, v213\n\t"
    "v_mul_f32 v154, v154, v214\n\t"
    "v_mul_f32 v155, v155, v215\n\t"
    "v_mul_f32 v188, v188, v216\n\t"
    "v_cvt_pk_bf16_f32 v212, v152, v153\n\t"
    "v_cvt_pk_bf16_f32 v213, v154, v155\n\t"
    "s_add_u32 s43, %12, 0xae8800\n\t"
    "buffer_store_dwordx2 v[212:213], v16, %0, s43 offen sc1\n\t"
    "v_cvt_pk_bf16_f32 v214, v188, v188\n\t"
    "buffer_store_short v214, v18, %0, s43 offen sc1\n\t"
    "ds_read_b128 v[164:167], v32 offset:960\n\t"
    "ds_read_b128 v[172:175], v32 offset:2240\n\t"
    "s_waitcnt lgkmcnt(1)\n\t"
    "s_waitcnt lgkmcnt(0)\n\t"
    "v_add_f32 v164, v164, v172\n\t"
    "v_add_f32 v165, v165, v173\n\t"
    "v_add_f32 v166, v166, v174\n\t"
    "v_add_f32 v167, v167, v175\n\t"
    "s_waitcnt lgkmcnt(0)\n\t"
    "s_barrier\n\t"
    "v_mfma_f32_16x16x32_bf16 a[0:3], a[124:127], v[68:71], 0\n\t"
    "buffer_load_dwordx4 v[248:251], v8, %0, s40 offen offset:3072\n\t"
    "ds_read_b128 v[176:179], v27 offset:0\n\t"
    "ds_read_b128 v[180:183], v27 offset:1024\n\t"
    "ds_read_b128 v[184:187], v27 offset:2048\n\t"
    "v_mfma_f32_16x16x32_bf16 a[0:3], a[128:131], v[72:75], a[0:3]\n\t"
    "s_add_u32 s40, s40, 4096\n\t"
    "buffer_load_dwordx4 v[252:255], v8, %0, s40 offen offset:0\n\t"
    "v_mfma_f32_16x16x32_bf16 a[0:3], a[132:135], v[76:79], a[0:3]\n\t"
    "buffer_load_dwordx4 a[64:67], v8, %0, s40 offen offset:1024\n\t"
    "v_mfma_f32_16x16x32_bf16 a[0:3], a[136:139], v[80:83], a[0:3]\n\t"
    "buffer_load_dwordx4 a[68:71], v8, %0, s40 offen offset:2048\n\t"
    "v_mfma_f32_16x16x32_bf16 a[0:3], a[140:143], v[84:87], a[0:3]\n\t"
    "v_mfma_f32_16x16x32_bf16 a[4:7], a[144:147], v[68:71], 0\n\t"
    "v_mfma_f32_16x16x32_bf16 a[4:7], a[148:151], v[72:75], a[4:7]\n\t"
    "v_mfma_f32_16x16x32_bf16 a[4:7], a[152:155], v[76:79], a[4:7]\n\t"
    "v_mfma_f32_16x16x32_bf16 a[4:7], a[156:159], v[80:83], a[4:7]\n\t"
    "v_mfma_f32_16x16x32_bf16 a[4:7], a[160:163], v[84:87], a[4:7]\n\t"
    "s_waitcnt lgkmcnt(2)\n\t"
    "s_waitcnt lgkmcnt(1)\n\t"
    "v_add_f32 v176, v176, v180\n\t"
    "ds_write_b128 v24, a[0:3] offset:16384\n\t"
    "v_add_f32 v177, v177, v181\n\t"
    "v_mfma_f32_16x16x32_bf16 a[8:11], a[164:167], v[68:71], 0\n\t"
    "v_add_f32 v178, v178, v182\n\t"
    "v_add_f32 v179, v179, v183\n\t"
    "ds_write_b128 v25, a[4:7] offset:16384\n\t"
    "v_mfma_f32_16x16x32_bf16 a[8:11], a[168:171], v[72:75], a[8:11]\n\t"
    "s_waitcnt vmcnt(8)\n\t"
    "v_cmp_eq_u32 vcc, -1, v13\n\t"
    "s_cbranch_vccnz H33_%=\n\t"
    "R34_%=:\n\t"
    "s_add_u32 s42, %9, 0xac7800\n\t"
    "buffer_load_dwordx4 v[48:51], v8, %0, s42 offen offset:0 sc1\n\t"
    "s_waitcnt lgkmcnt(2)\n\t"
    "v_add_f32 v148, v148, v184\n\t"
    "v_add_f32 v149, v149, v185\n\t"
    "v_add_f32 v150, v150, v186\n\t"
    "v_mfma_f32_16x16x32_bf16 a[8:11], a[172:175], v[76:79], a[8:11]\n\t"
    "buffer_load_dwordx4 v[52:55], v8, %0, s42 offen offset:1024 sc1\n\t"
    "v_add_f32 v151, v151, v187\n\t"
    "v_add_f32 v148, v148, v176\n\t"
    "v_add_f32 v149, v149, v177\n\t"
    "v_mfma_f32_16x16x32_bf16 a[8:11], a[176:179], v[80:83], a[8:11]\n\t"
    "buffer_load_dwordx4 v[56:59], v8, %0, s42 offen offset:2048 sc1\n\t"
    "v_add_f32 v150, v150, v178\n\t"
    "v_add_f32 v151, v151, v179\n\t"
    "v_mul_f32 v212, v164, v159\n\t"
    "v_mfma_f32_16x16x32_bf16 a[8:11], a[180:183], v[84:87], a[8:11]\n\t"
    "v_mul_f32 v213, v165, v159\n\t"
    "v_mul_f32 v214, v166, v159\n\t"
    "v_mul_f32 v215, v167, v159\n\t"
    "v_mfma_f32_16x16x32_bf16 v[152:155], a[184:187], v[68:71], 0\n\t"
    "buffer_load_dwordx4 v[60:63], v8, %0, s42 offen offset:3072 sc1\n\t"
    "v_fma_f32 v148, v148, v158, v212\n\t"
    "v_fma_f32 v149, v149, v158, v213\n\t"
    "v_fma_f32 v150, v150, v158, v214\n\t"
    "v_mfma_f32_16x16x32_bf16 v[152:155], a[188:191], v[72:75], v[152:155]\n\t"
    "ds_write_b128 v26, a[8:11] offset:16384\n\t"
    "v_fma_f32 v151, v151, v158, v215\n\t"
    "v_mul_f32 v212, v148, v148\n\t"
    "v_mfma_f32_16x16x32_bf16 v[152:155], a[192:195], v[76:79], v[152:155]\n\t"
    "v_mul_f32 v213, v149, v149\n\t"
    "v_mul_f32 v214, v150, v150\n\t"
    "v_mul_f32 v215, v151, v151\n\t"
    "v_mfma_f32_16x16x32_bf16 v[152:155], a[196:199], v[80:83], v[152:155]\n\t"
    "v_fma_f32 v212, v212, v11, v10\n\t"
    "v_fma_f32 v213, v213, v11, v10\n\t"
    "v_fma_f32 v214, v214, v11, v10\n\t"
    "v_mfma_f32_16x16x32_bf16 v[152:155], a[200:203], v[84:87], v[152:155]\n\t"
    "v_fma_f32 v215, v215, v11, v10\n\t"
    "v_mul_f32 v212, v212, v148\n\t"
    "v_mul_f32 v213, v213, v149\n\t"
    "v_mul_f32 v214, v214, v150\n\t"
    "v_mul_f32 v215, v215, v151\n\t"
    "v_exp_f32 v212, v212\n\t"
    "v_exp_f32 v213, v213\n\t"
    "v_exp_f32 v214, v214\n\t"
    "v_exp_f32 v215, v215\n\t"
    "v_add_f32 v212, 1.0, v212\n\t"
    "v_add_f32 v213, 1.0, v213\n\t"
    "v_add_f32 v214, 1.0, v214\n\t"
    "v_add_f32 v215, 1.0, v215\n\t"
    "v_rcp_f32 v212, v212\n\t"
    "v_rcp_f32 v213, v213\n\t"
    "v_rcp_f32 v214, v214\n\t"
    "v_rcp_f32 v215, v215\n\t"
    "v_mul_f32 v148, v148, v212\n\t"
    "v_mul_f32 v149, v149, v213\n\t"
    "v_mul_f32 v150, v150, v214\n\t"
    "v_mul_f32 v151, v151, v215\n\t"
    "v_cvt_pk_bf16_f32 v212, v148, v149\n\t"
    "v_cvt_pk_bf16_f32 v213, v150, v151\n\t"
    "s_add_u32 s43, %12, 0xad1800\n\t"
    "v_and_b32 v212, v212, v37\n\t"
    "v_and_b32 v213, v213, v37\n\t"
    "buffer_store_dwordx2 v[212:213], v19, %0, s43 offen sc1\n\t"
    "s_waitcnt lgkmcnt(0)\n\t"
    "s_barrier\n\t"
    "v_mfma_f32_16x16x32_bf16 a[0:3], a[124:127], v[88:91], 0\n\t"
    "ds_read_b128 v[176:179], v27 offset:16384\n\t"
    "ds_read_b128 v[180:183], v27 offset:17408\n\t"
    "ds_read_b128 v[184:187], v27 offset:18432\n\t"
    "v_mfma_f32_16x16x32_bf16 a[0:3], a[128:131], v[92:95], a[0:3]\n\t"
    "v_mfma_f32_16x16x32_bf16 a[0:3], a[132:135], v[96:99], a[0:3]\n\t"
    "buffer_load_dwordx4 v[64:67], v9, %0, s42 offen offset:0 sc1\n\t"
    "v_mfma_f32_16x16x32_bf16 a[0:3], a[136:139], v[100:103], a[0:3]\n\t"
    "v_mfma_f32_16x16x32_bf16 a[0:3], a[140:143], v[104:107], a[0:3]\n\t"
    "v_mfma_f32_16x16x32_bf16 a[4:7], a[144:147], v[88:91], 0\n\t"
    "v_mfma_f32_16x16x32_bf16 a[4:7], a[148:151], v[92:95], a[4:7]\n\t"
    "v_mfma_f32_16x16x32_bf16 a[4:7], a[152:155], v[96:99], a[4:7]\n\t"
    "buffer_load_dwordx4 a[72:75], v8, %0, s40 offen offset:3072\n\t"
    "v_mfma_f32_16x16x32_bf16 a[4:7], a[156:159], v[100:103], a[4:7]\n\t"
    "s_waitcnt lgkmcnt(2)\n\t"
    "s_waitcnt lgkmcnt(1)\n\t"
    "v_add_f32 v176, v176, v180\n\t"
    "ds_write_b128 v24, a[0:3] offset:0\n\t"
    "v_add_f32 v177, v177, v181\n\t"
    "v_mfma_f32_16x16x32_bf16 a[4:7], a[160:163], v[104:107], a[4:7]\n\t"
    "v_add_f32 v178, v178, v182\n\t"
    "v_add_f32 v179, v179, v183\n\t"
    "s_waitcnt lgkmcnt(1)\n\t"
    "v_add_f32 v152, v152, v184\n\t"
    "v_mfma_f32_16x16x32_bf16 a[8:11], a[164:167], v[88:91], 0\n\t"
    "v_add_f32 v153, v153, v185\n\t"
    "v_add_f32 v154, v154, v186\n\t"
    "v_add_f32 v155, v155, v187\n\t"
    "v_mfma_f32_16x16x32_bf16 a[8:11], a[168:171], v[92:95], a[8:11]\n\t"
    "s_add_u32 s40, s40, 4096\n\t"
    "buffer_load_dwordx4 a[76:79], v8, %0, s40 offen offset:0\n\t"
    "ds_write_b128 v25, a[4:7] offset:0\n\t"
    "v_add_f32 v152, v152, v176\n\t"
    "v_add_f32 v153, v153, v177\n\t"
    "v_mfma_f32_16x16x32_bf16 a[8:11], a[172:175], v[96:99], a[8:11]\n\t"
    "v_add_f32 v154, v154, v178\n\t"
    "v_add_f32 v155, v155, v179\n\t"
    "v_mul_f32 v212, v164, v161\n\t"
    "v_mfma_f32_16x16x32_bf16 a[8:11], a[176:179], v[100:103], a[8:11]\n\t"
    "v_mul_f32 v213, v165, v161\n\t"
    "v_mul_f32 v214, v166, v161\n\t"
    "v_mul_f32 v215, v167, v161\n\t"
    "v_mfma_f32_16x16x32_bf16 a[8:11], a[180:183], v[104:107], a[8:11]\n\t"
    "s_waitcnt vmcnt(12)\n\t"
    "s_add_u32 s55, %13, 0x1bc3880\n\t"
    "buffer_store_dword v12, v36, %0, s55 offen sc1\n\t"
    "v_fma_f32 v152, v152, v160, v212\n\t"
    "v_fma_f32 v153, v153, v160, v213\n\t"
    "v_fma_f32 v154, v154, v160, v214\n\t"
    "v_mfma_f32_16x16x32_bf16 v[148:151], a[184:187], v[88:91], 0\n\t"
    "buffer_load_dwordx4 a[80:83], v8, %0, s40 offen offset:1024\n\t"
    "ds_write_b128 v26, a[8:11] offset:0\n\t"
    "v_fma_f32 v155, v155, v160, v215\n\t"
    "v_mul_f32 v212, v152, v152\n\t"
    "v_mfma_f32_16x16x32_bf16 v[148:151], a[188:191], v[92:95], v[148:151]\n\t"
    "v_mul_f32 v213, v153, v153\n\t"
    "v_mul_f32 v214, v154, v154\n\t"
    "v_mul_f32 v215, v155, v155\n\t"
    "v_mfma_f32_16x16x32_bf16 v[148:151], a[192:195], v[96:99], v[148:151]\n\t"
    "v_fma_f32 v212, v212, v11, v10\n\t"
    "v_fma_f32 v213, v213, v11, v10\n\t"
    "v_fma_f32 v214, v214, v11, v10\n\t"
    "v_mfma_f32_16x16x32_bf16 v[148:151], a[196:199], v[100:103], v[148:151]\n\t"
    "s_add_u32 s41, %9, 0xad5800\n\t"
    "buffer_load_dwordx4 v[68:71], v8, %0, s41 offen offset:0 sc1\n\t"
    "v_fma_f32 v215, v215, v11, v10\n\t"
    "v_mul_f32 v212, v212, v152\n\t"
    "v_mul_f32 v213, v213, v153\n\t"
    "v_mfma_f32_16x16x32_bf16 v[148:151], a[200:203], v[104:107], v[148:151]\n\t"
    "v_mul_f32 v214, v214, v154\n\t"
    "v_mul_f32 v215, v215, v155\n\t"
    "v_exp_f32 v212, v212\n\t"
    "v_exp_f32 v213, v213\n\t"
    "v_exp_f32 v214, v214\n\t"
    "v_exp_f32 v215, v215\n\t"
    "v_add_f32 v212, 1.0, v212\n\t"
    "v_add_f32 v213, 1.0, v213\n\t"
    "v_add_f32 v214, 1.0, v214\n\t"
    "v_add_f32 v215, 1.0, v215\n\t"
    "v_rcp_f32 v212, v212\n\t"
    "v_rcp_f32 v213, v213\n\t"
    "v_rcp_f32 v214, v214\n\t"
    "v_rcp_f32 v215, v215\n\t"
    "v_mul_f32 v152, v152, v212\n\t"
    "v_mul_f32 v153, v153, v213\n\t"
    "v_mul_f32 v154, v154, v214\n\t"
    "v_mul_f32 v155, v155, v215\n\t"
    "v_cvt_pk_bf16_f32 v212, v152, v153\n\t"
    "v_cvt_pk_bf16_f32 v213, v154, v155\n\t"
    "s_add_u32 s43, %12, 0xadf800\n\t"
    "v_and_b32 v212, v212, v37\n\t"
    "v_and_b32 v213, v213, v37\n\t"
    "buffer_store_dwordx2 v[212:213], v19, %0, s43 offen sc1\n\t"
    "s_waitcnt lgkmcnt(0)\n\t"
    "s_barrier\n\t"
    "v_mfma_f32_16x16x32_bf16 a[0:3], a[124:127], v[108:111], 0\n\t"
    "buffer_load_dwordx4 v[72:75], v8, %0, s41 offen offset:1024 sc1\n\t"
    "ds_read_b128 v[176:179], v27 offset:0\n\t"
    "ds_read_b128 v[180:183], v27 offset:1024\n\t"
    "ds_read_b128 v[184:187], v27 offset:2048\n\t"
    "v_mfma_f32_16x16x32_bf16 a[0:3], a[128:131], v[112:115], a[0:3]\n\t"
    "buffer_load_dwordx4 v[76:79], v8, %0, s41 offen offset:2048 sc1\n\t"
    "v_mfma_f32_16x16x32_bf16 a[0:3], a[132:135], v[116:119], a[0:3]\n\t"
    "buffer_load_dwordx4 v[80:83], v8, %0, s41 offen offset:3072 sc1\n\t"
    "v_mfma_f32_16x16x32_bf16 a[0:3], a[136:139], v[120:123], a[0:3]\n\t"
    "v_mfma_f32_16x16x32_bf16 a[0:3], a[140:143], v[124:127], a[0:3]\n\t"
    "v_mfma_f32_16x16x32_bf16 a[4:7], a[144:147], v[108:111], 0\n\t"
    "v_mfma_f32_16x16x32_bf16 a[4:7], a[148:151], v[112:115], a[4:7]\n\t"
    "v_mfma_f32_16x16x32_bf16 a[4:7], a[152:155], v[116:119], a[4:7]\n\t"
    "v_mfma_f32_16x16x32_bf16 a[4:7], a[156:159], v[120:123], a[4:7]\n\t"
    "v_mfma_f32_16x16x32_bf16 a[4:7], a[160:163], v[124:127], a[4:7]\n\t"
    "s_waitcnt lgkmcnt(2)\n\t"
    "s_waitcnt lgkmcnt(1)\n\t"
    "v_add_f32 v176, v176, v180\n\t"
    "ds_write_b128 v24, a[0:3] offset:16384\n\t"
    "v_add_f32 v177, v177, v181\n\t"
    "v_mfma_f32_16x16x32_bf16 a[8:11], a[164:167], v[108:111], 0\n\t"
    "v_add_f32 v178, v178, v182\n\t"
    "v_add_f32 v179, v179, v183\n\t"
    "ds_write_b128 v25, a[4:7] offset:16384\n\t"
    "v_mfma_f32_16x16x32_bf16 a[8:11], a[168:171], v[112:115], a[8:11]\n\t"
    "buffer_load_dwordx4 v[84:87], v9, %0, s41 offen offset:0 sc1\n\t"
    "s_waitcnt lgkmcnt(2)\n\t"
    "v_add_f32 v148, v148, v184\n\t"
    "v_add_f32 v149, v149, v185\n\t"
    "v_add_f32 v150, v150, v186\n\t"
    "v_mfma_f32_16x16x32_bf16 a[8:11], a[172:175], v[116:119], a[8:11]\n\t"
    "v_add_f32 v151, v151, v187\n\t"
    "v_add_f32 v148, v148, v176\n\t"
    "v_add_f32 v149, v149, v177\n\t"
    "v_mfma_f32_16x16x32_bf16 a[8:11], a[176:179], v[120:123], a[8:11]\n\t"
    "v_add_f32 v150, v150, v178\n\t"
    "v_add_f32 v151, v151, v179\n\t"
    "v_mul_f32 v212, v164, v157\n\t"
    "v_mfma_f32_16x16x32_bf16 a[8:11], a[180:183], v[124:127], a[8:11]\n\t"
    "v_mul_f32 v213, v165, v157\n\t"
    "v_mul_f32 v214, v166, v157\n\t"
    "v_mul_f32 v215, v167, v157\n\t"
    "v_mfma_f32_16x16x32_bf16 v[152:155], a[184:187], v[108:111], 0\n\t"
    "s_add_u32 s42, %9, 0xab9800\n\t"
    "buffer_load_dwordx4 v[88:91], v8, %0, s42 offen offset:0 sc1\n\t"
    "v_fma_f32 v148, v148, v156, v212\n\t"
    "v_fma_f32 v149, v149, v156, v213\n\t"
    "ds_write_b128 v26, a[8:11] offset:16384\n\t"
    "v_mfma_f32_16x16x32_bf16 v[152:155], a[188:191], v[112:115], v[152:155]\n\t"
    "v_fma_f32 v150, v150, v156, v214\n\t"
    "v_fma_f32 v151, v151, v156, v215\n\t"
    "v_mul_f32 v212, v148, v148\n\t"
    "v_mfma_f32_16x16x32_bf16 v[152:155], a[192:195], v[116:119], v[152:155]\n\t"
    "v_mul_f32 v213, v149, v149\n\t"
    "v_mul_f32 v214, v150, v150\n\t"
    "v_mul_f32 v215, v151, v151\n\t"
    "v_mfma_f32_16x16x32_bf16 v[152:155], a[196:199], v[120:123], v[152:155]\n\t"
    "buffer_load_dwordx4 v[92:95], v8, %0, s42 offen offset:1024 sc1\n\t"
    "v_fma_f32 v212, v212, v11, v10\n\t"
    "v_fma_f32 v213, v213, v11, v10\n\t"
    "v_fma_f32 v214, v214, v11, v10\n\t"
    "v_mfma_f32_16x16x32_bf16 v[152:155], a[200:203], v[124:127], v[152:155]\n\t"
    "v_fma_f32 v215, v215, v11, v10\n\t"
    "v_mul_f32 v212, v212, v148\n\t"
    "v_mul_f32 v213, v213, v149\n\t"
    "v_mul_f32 v214, v214, v150\n\t"
    "v_mul_f32 v215, v215, v151\n\t"
    "v_exp_f32 v212, v212\n\t"
    "v_exp_f32 v213, v213\n\t"
    "v_exp_f32 v214, v214\n\t"
    "v_exp_f32 v215, v215\n\t"
    "v_add_f32 v212, 1.0, v212\n\t"
    "v_add_f32 v213, 1.0, v213\n\t"
    "v_add_f32 v214, 1.0, v214\n\t"
    "v_add_f32 v215, 1.0, v215\n\t"
    "v_rcp_f32 v212, v212\n\t"
    "v_rcp_f32 v213, v213\n\t"
    "v_rcp_f32 v214, v214\n\t"
    "v_rcp_f32 v215, v215\n\t"
    "v_mul_f32 v148, v148, v212\n\t"
    "v_mul_f32 v149, v149, v213\n\t"
    "v_mul_f32 v150, v150, v214\n\t"
    "v_mul_f32 v151, v151, v215\n\t"
    "v_cvt_pk_bf16_f32 v212, v148, v149\n\t"
    "v_cvt_pk_bf16_f32 v213, v150, v151\n\t"
    "s_add_u32 s43, %12, 0xac3800\n\t"
    "v_and_b32 v212, v212, v37\n\t"
    "v_and_b32 v213, v213, v37\n\t"
    "buffer_store_dwordx2 v[212:213], v19, %0, s43 offen sc1\n\t"
    "s_waitcnt lgkmcnt(0)\n\t"
    "s_barrier\n\t"
    "s_waitcnt vmcnt(18)\n\t"
    "v_mfma_f32_16x16x32_bf16 a[16:19], a[204:207], v[48:51], 0\n\t"
    "buffer_load_dwordx4 v[96:99], v8, %0, s42 offen offset:2048 sc1\n\t"
    "ds_read_b128 v[176:179], v27 offset:16384\n\t"
    "ds_read_b128 v[180:183], v27 offset:17408\n\t"
    "ds_read_b128 v[184:187], v27 offset:18432\n\t"
    "s_waitcnt vmcnt(18)\n\t"
    "v_mfma_f32_16x16x32_bf16 a[16:19], a[208:211], v[52:55], a[16:19]\n\t"
    "buffer_load_dwordx4 v[100:103], v8, %0, s42 offen offset:3072 sc1\n\t"
    "s_waitcnt vmcnt(18)\n\t"
    "v_mfma_f32_16x16x32_bf16 a[16:19], a[212:215], v[56:59], a[16:19]\n\t"
    "s_waitcnt vmcnt(17)\n\t"
    "v_mfma_f32_16x16x32_bf16 a[16:19], a[216:219], v[60:63], a[16:19]\n\t"
    "s_waitcnt vmcnt(15)\n\t"
    "v_mfma_f32_16x16x32_bf16 a[16:19], a[220:223], v[64:67], a[16:19]\n\t"
    "v_mfma_f32_16x16x32_bf16 a[20:23], a[224:227], v[48:51], 0\n\t"
    "v_mfma_f32_16x16x32_bf16 a[20:23], a[228:231], v[52:55], a[20:23]\n\t"
    "v_mfma_f32_16x16x32_bf16 a[20:23], a[232:235], v[56:59], a[20:23]\n\t"
    "v_mfma_f32_16x16x32_bf16 a[20:23], a[236:239], v[60:63], a[20:23]\n\t"
    "v_mfma_f32_16x16x32_bf16 a[20:23], a[240:243], v[64:67], a[20:23]\n\t"
    "buffer_load_dwordx4 v[104:107], v9, %0, s42 offen offset:0 sc1\n\t"
    "s_waitcnt lgkmcnt(2)\n\t"
    "s_waitcnt lgkmcnt(1)\n\t"
    "v_add_f32 v176, v176, v180\n\t"
    "v_add_f32 v177, v177, v181\n\t"
    "v_add_f32 v178, v178, v182\n\t"
    "v_mfma_f32_16x16x32_bf16 a[24:27], a[244:247], v[48:51], 0\n\t"
    "v_add_f32 v179, v179, v183\n\t"
    "s_waitcnt lgkmcnt(0)\n\t"
    "v_add_f32 v152, v152, v184\n\t"
    "v_add_f32 v153, v153, v185\n\t"
    "v_mfma_f32_16x16x32_bf16 a[24:27], a[248:251], v[52:55], a[24:27]\n\t"
    "v_add_f32 v154, v154, v186\n\t"
    "v_add_f32 v155, v155, v187\n\t"
    "v_add_f32 v152, v152, v176\n\t"
    "v_mfma_f32_16x16x32_bf16 a[24:27], a[252:255], v[56:59], a[24:27]\n\t"
    "v_add_f32 v153, v153, v177\n\t"
    "v_add_f32 v154, v154, v178\n\t"
    "v_add_f32 v155, v155, v179\n\t"
    "v_mfma_f32_16x16x32_bf16 a[24:27], v[228:231], v[60:63], a[24:27]\n\t"
    "s_add_u32 s41, %9, 0xae3800\n\t"
    "buffer_load_dwordx4 v[108:111], v8, %0, s41 offen offset:0 sc1\n\t"
    "v_mul_f32 v212, v164, v163\n\t"
    "v_mul_f32 v213, v165, v163\n\t"
    "v_mul_f32 v214, v166, v163\n\t"
    "v_mfma_f32_16x16x32_bf16 a[24:27], v[232:235], v[64:67], a[24:27]\n\t"
    "v_mul_f32 v215, v167, v163\n\t"
    "v_fma_f32 v152, v152, v162, v212\n\t"
    "v_fma_f32 v153, v153, v162, v213\n\t"
    "v_mfma_f32_16x16x32_bf16 a[28:31], v[236:239], v[48:51], 0\n\t"
    "v_fma_f32 v154, v154, v162, v214\n\t"
    "v_fma_f32 v155, v155, v162, v215\n\t"
    "v_mul_f32 v212, v152, v152\n\t"
    "v_mfma_f32_16x16x32_bf16 a[28:31], v[240:243], v[52:55], a[28:31]\n\t"
    "buffer_load_dwordx4 v[112:115], v8, %0, s41 offen offset:1024 sc1\n\t"
    "v_mul_f32 v213, v153, v153\n\t"
    "v_mul_f32 v214, v154, v154\n\t"
    "v_mul_f32 v215, v155, v155\n\t"
    "v_mfma_f32_16x16x32_bf16 a[28:31], v[244:247], v[56:59], a[28:31]\n\t"
    "v_fma_f32 v212, v212, v11, v10\n\t"
    "v_fma_f32 v213, v213, v11, v10\n\t"
    "v_fma_f32 v214, v214, v11, v10\n\t"
    "v_mfma_f32_16x16x32_bf16 a[28:31], v[248:251], v[60:63], a[28:31]\n\t"
    "v_fma_f32 v215, v215, v11, v10\n\t"
    "v_mul_f32 v212, v212, v152\n\t"
    "v_mul_f32 v213, v213, v153\n\t"
    "v_mfma_f32_16x16x32_bf16 a[28:31], v[252:255], v[64:67], a[28:31]\n\t"
    "v_mul_f32 v214, v214, v154\n\t"
    "v_mul_f32 v215, v215, v155\n\t"
    "v_exp_f32 v212, v212\n\t"
    "v_mfma_f32_16x16x32_bf16 v[132:135], a[64:67], v[48:51], v[132:135]\n\t"
    "buffer_load_dwordx4 v[116:119], v8, %0, s41 offen offset:2048 sc1\n\t"
    "v_exp_f32 v213, v213\n\t"
    "v_exp_f32 v214, v214\n\t"
    "v_exp_f32 v215, v215\n\t"
    "v_mfma_f32_16x16x32_bf16 v[132:135], a[68:71], v[52:55], v[132:135]\n\t"
    "v_add_f32 v212, 1.0, v212\n\t"
    "v_add_f32 v213, 1.0, v213\n\t"
    "v_add_f32 v214, 1.0, v214\n\t"
    "s_waitcnt vmcnt(18)\n\t"
    "v_mfma_f32_16x16x32_bf16 v[132:135], a[72:75], v[56:59], v[132:135]\n\t"
    "v_add_f32 v215, 1.0, v215\n\t"
    "v_rcp_f32 v212, v212\n\t"
    "v_rcp_f32 v213, v213\n\t"
    "s_waitcnt vmcnt(17)\n\t"
    "v_mfma_f32_16x16x32_bf16 v[132:135], a[76:79], v[60:63], v[132:135]\n\t"
    "s_add_u32 s47, %13, 0x1bc3880\n\t"
    "buffer_load_dword v13, v35, %0, s47 offen sc1\n\t"
    "v_rcp_f32 v214, v214\n\t"
    "v_rcp_f32 v215, v215\n\t"
    "v_mul_f32 v152, v152, v212\n\t"
    "s_waitcnt vmcnt(16)\n\t"
    "v_mfma_f32_16x16x32_bf16 v[132:135], a[80:83], v[64:67], v[132:135]\n\t"
    "v_mul_f32 v153, v153, v213\n\t"
    "v_mul_f32 v154, v154, v214\n\t"
    "v_mul_f32 v155, v155, v215\n\t"
    "s_waitcnt vmcnt(15)\n\t"
    "v_mfma_f32_16x16x32_bf16 a[32:35], a[204:207], v[68:71], 0\n\t"
    "v_cvt_pk_bf16_f32 v212, v152, v153\n\t"
    "v_cvt_pk_bf16_f32 v213, v154, v155\n\t"
    "s_add_u32 s43, %12, 0xaed800\n\t"
    "s_waitcnt vmcnt(13)\n\t"
    "v_mfma_f32_16x16x32_bf16 a[32:35], a[208:211], v[72:75], a[32:35]\n\t"
    "buffer_load_dwordx4 v[120:123], v8, %0, s41 offen offset:3072 sc1\n\t"
    "v_and_b32 v212, v212, v37\n\t"
    "v_and_b32 v213, v213, v37\n\t"
    "buffer_store_dwordx2 v[212:213], v19, %0, s43 offen sc1\n\t"
    "s_waitcnt vmcnt(14)\n\t"
    "v_mfma_f32_16x16x32_bf16 a[32:35], a[212:215], v[76:79], a[32:35]\n\t"
    "s_waitcnt vmcnt(13)\n\t"
    "v_mfma_f32_16x16x32_bf16 a[32:35], a[216:219], v[80:83], a[32:35]\n\t"
    "s_waitcnt vmcnt(12)\n\t"
    "v_mfma_f32_16x16x32_bf16 a[32:35], a[220:223], v[84:87], a[32:35]\n\t"
    "v_mfma_f32_16x16x32_bf16 a[36:39], a[224:227], v[68:71], 0\n\t"
    "v_mfma_f32_16x16x32_bf16 a[36:39], a[228:231], v[72:75], a[36:39]\n\t"
    "v_mfma_f32_16x16x32_bf16 a[36:39], a[232:235], v[76:79], a[36:39]\n\t"
    "v_mfma_f32_16x16x32_bf16 a[36:39], a[236:239], v[80:83], a[36:39]\n\t"
    "v_mfma_f32_16x16x32_bf16 a[36:39], a[240:243], v[84:87], a[36:39]\n\t"
    "v_mfma_f32_16x16x32_bf16 a[40:43], a[244:247], v[68:71], 0\n\t"
    "buffer_load_dwordx4 v[124:127], v9, %0, s41 offen offset:0 sc1\n\t"
    "v_mfma_f32_16x16x32_bf16 a[40:43], a[248:251], v[72:75], a[40:43]\n\t"
    "v_mfma_f32_16x16x32_bf16 a[40:43], a[252:255], v[76:79], a[40:43]\n\t"
    "v_mfma_f32_16x16x32_bf16 a[40:43], v[228:231], v[80:83], a[40:43]\n\t"
    "v_mfma_f32_16x16x32_bf16 a[40:43], v[232:235], v[84:87], a[40:43]\n\t"
    "v_mfma_f32_16x16x32_bf16 a[44:47], v[236:239], v[68:71], 0\n\t"
    "buffer_load_dwordx4 a[84:87], v8, %0, s40 offen offset:2048\n\t"
    "v_mfma_f32_16x16x32_bf16 a[44:47], v[240:243], v[72:75], a[44:47]\n\t"
    "v_mfma_f32_16x16x32_bf16 a[44:47], v[244:247], v[76:79], a[44:47]\n\t"
    "v_mfma_f32_16x16x32_bf16 a[44:47], v[248:251], v[80:83], a[44:47]\n\t"
    "v_mfma_f32_16x16x32_bf16 a[44:47], v[252:255], v[84:87], a[44:47]\n\t"
    "v_mfma_f32_16x16x32_bf16 v[136:139], a[64:67], v[68:71], v[136:139]\n\t"
    "buffer_load_dwordx4 a[88:91], v8, %0, s40 offen offset:3072\n\t"
    "v_mfma_f32_16x16x32_bf16 v[136:139], a[68:71], v[72:75], v[136:139]\n\t"
    "v_mfma_f32_16x16x32_bf16 v[136:139], a[72:75], v[76:79], v[136:139]\n\t"
    "v_mfma_f32_16x16x32_bf16 v[136:139], a[76:79], v[80:83], v[136:139]\n\t"
    "v_mfma_f32_16x16x32_bf16 v[136:139], a[80:83], v[84:87], v[136:139]\n\t"
    "s_waitcnt vmcnt(14)\n\t"
    "v_mfma_f32_16x16x32_bf16 a[0:3], a[204:207], v[88:91], 0\n\t"
    "s_add_u32 s40, s40, 4096\n\t"
    "buffer_load_dwordx4 a[92:95], v8, %0, s40 offen offset:0\n\t"
    "s_waitcnt vmcnt(14)\n\t"
    "v_mfma_f32_16x16x32_bf16 a[0:3], a[208:211], v[92:95], a[0:3]\n\t"
    "s_waitcnt vmcnt(12)\n\t"
    "v_mfma_f32_16x16x32_bf16 a[0:3], a[212:215], v[96:99], a[0:3]\n\t"
    "s_waitcnt vmcnt(11)\n\t"
    "v_mfma_f32_16x16x32_bf16 a[0:3], a[216:219], v[100:103], a[0:3]\n\t"
    "s_waitcnt vmcnt(10)\n\t"
    "v_mfma_f32_16x16x32_bf16 a[0:3], a[220:223], v[104:107], a[0:3]\n\t"
    "v_mfma_f32_16x16x32_bf16 a[4:7], a[224:227], v[88:91], 0\n\t"
    "buffer_load_dwordx4 a[96:99], v8, %0, s40 offen offset:1024\n\t"
    "v_mfma_f32_16x16x32_bf16 a[4:7], a[228:231], v[92:95], a[4:7]\n\t"
    "v_mfma_f32_16x16x32_bf16 a[4:7], a[232:235], v[96:99], a[4:7]\n\t"
    "v_mfma_f32_16x16x32_bf16 a[4:7], a[236:239], v[100:103], a[4:7]\n\t"
    "v_mfma_f32_16x16x32_bf16 a[4:7], a[240:243], v[104:107], a[4:7]\n\t"
    "v_mfma_f32_16x16x32_bf16 a[8:11], a[244:247], v[88:91], 0\n\t"
    "buffer_load_dwordx4 a[100:103], v8, %0, s40 offen offset:2048\n\t"
    "v_mfma_f32_16x16x32_bf16 a[8:11], a[248:251], v[92:95], a[8:11]\n\t"
    "v_mfma_f32_16x16x32_bf16 a[8:11], a[252:255], v[96:99], a[8:11]\n\t"
    "v_mfma_f32_16x16x32_bf16 a[8:11], v[228:231], v[100:103], a[8:11]\n\t"
    "v_mfma_f32_16x16x32_bf16 a[8:11], v[232:235], v[104:107], a[8:11]\n\t"
    "v_mfma_f32_16x16x32_bf16 a[12:15], v[236:239], v[88:91], 0\n\t"
    "s_waitcnt vmcnt(8)\n\t"
    "v_cmp_eq_u32 vcc, -1, v13\n\t"
    "s_cbranch_vccnz H35_%=\n\t"
    "R36_%=:\n\t"
    "s_add_u32 s42, %9, 0xacc800\n\t"
    "buffer_load_dwordx4 v[48:51], v8, %0, s42 offen offset:0 sc1\n\t"
    "v_mfma_f32_16x16x32_bf16 a[12:15], v[240:243], v[92:95], a[12:15]\n\t"
    "v_mfma_f32_16x16x32_bf16 a[12:15], v[244:247], v[96:99], a[12:15]\n\t"
    "v_mfma_f32_16x16x32_bf16 a[12:15], v[248:251], v[100:103], a[12:15]\n\t"
    "v_mfma_f32_16x16x32_bf16 a[12:15], v[252:255], v[104:107], a[12:15]\n\t"
    "v_mfma_f32_16x16x32_bf16 v[128:131], a[64:67], v[88:91], v[128:131]\n\t"
    "buffer_load_dwordx4 v[52:55], v8, %0, s42 offen offset:1024 sc1\n\t"
    "v_mfma_f32_16x16x32_bf16 v[128:131], a[68:71], v[92:95], v[128:131]\n\t"
    "v_mfma_f32_16x16x32_bf16 v[128:131], a[72:75], v[96:99], v[128:131]\n\t"
    "v_mfma_f32_16x16x32_bf16 v[128:131], a[76:79], v[100:103], v[128:131]\n\t"
    "v_mfma_f32_16x16x32_bf16 v[128:131], a[80:83], v[104:107], v[128:131]\n\t"
    "v_mfma_f32_16x16x32_bf16 a[48:51], a[204:207], v[108:111], 0\n\t"
    "v_mfma_f32_16x16x32_bf16 a[48:51], a[208:211], v[112:115], a[48:51]\n\t"
    "v_mfma_f32_16x16x32_bf16 a[48:51], a[212:215], v[116:119], a[48:51]\n\t"
    "s_waitcnt vmcnt(9)\n\t"
    "v_mfma_f32_16x16x32_bf16 a[48:51], a[216:219], v[120:123], a[48:51]\n\t"
    "s_waitcnt vmcnt(7)\n\t"
    "v_mfma_f32_16x16x32_bf16 a[48:51], a[220:223], v[124:127], a[48:51]\n\t"
    "s_add_u32 s55, %13, 0x1bc3900\n\t"
    "buffer_store_dword v12, v36, %0, s55 offen sc1\n\t"
    "v_mfma_f32_16x16x32_bf16 a[52:55], a[224:227], v[108:111], 0\n\t"
    "buffer_load_dwordx4 v[56:59], v8, %0, s42 offen offset:2048 sc1\n\t"
    "v_mfma_f32_16x16x32_bf16 a[52:55], a[228:231], v[112:115], a[52:55]\n\t"
    "buffer_load_dwordx4 v[60:63], v8, %0, s42 offen offset:3072 sc1\n\t"
    "v_mfma_f32_16x16x32_bf16 a[52:55], a[232:235], v[116:119], a[52:55]\n\t"
    "buffer_load_dwordx4 v[64:67], v9, %0, s42 offen offset:0 sc1\n\t"
    "v_mfma_f32_16x16x32_bf16 a[52:55], a[236:239], v[120:123], a[52:55]\n\t"
    "v_mfma_f32_16x16x32_bf16 a[52:55], a[240:243], v[124:127], a[52:55]\n\t"
    "buffer_load_dwordx4 a[104:107], v8, %0, s40 offen offset:3072\n\t"
    "v_mfma_f32_16x16x32_bf16 a[56:59], a[244:247], v[108:111], 0\n\t"
    "v_mfma_f32_16x16x32_bf16 a[56:59], a[248:251], v[112:115], a[56:59]\n\t"
    "v_mfma_f32_16x16x32_bf16 a[56:59], a[252:255], v[116:119], a[56:59]\n\t"
    "v_mfma_f32_16x16x32_bf16 a[56:59], v[228:231], v[120:123], a[56:59]\n\t"
    "v_mfma_f32_16x16x32_bf16 a[56:59], v[232:235], v[124:127], a[56:59]\n\t"
    "s_add_u32 s40, s40, 4096\n\t"
    "buffer_load_dwordx4 a[108:111], v8, %0, s40 offen offset:0\n\t"
    "v_mfma_f32_16x16x32_bf16 a[60:63], v[236:239], v[108:111], 0\n\t"
    "v_mfma_f32_16x16x32_bf16 a[60:63], v[240:243], v[112:115], a[60:63]\n\t"
    "v_mfma_f32_16x16x32_bf16 a[60:63], v[244:247], v[116:119], a[60:63]\n\t"
    "v_mfma_f32_16x16x32_bf16 a[60:63], v[248:251], v[120:123], a[60:63]\n\t"
    "v_mfma_f32_16x16x32_bf16 a[60:63], v[252:255], v[124:127], a[60:63]\n\t"
    "buffer_load_dwordx4 a[112:115], v8, %0, s40 offen offset:1024\n\t"
    "v_mfma_f32_16x16x32_bf16 v[140:143], a[64:67], v[108:111], v[140:143]\n\t"
    "v_mfma_f32_16x16x32_bf16 v[140:143], a[68:71], v[112:115], v[140:143]\n\t"
    "v_mfma_f32_16x16x32_bf16 v[140:143], a[72:75], v[116:119], v[140:143]\n\t"
    "v_mfma_f32_16x16x32_bf16 v[140:143], a[76:79], v[120:123], v[140:143]\n\t"
    "v_mfma_f32_16x16x32_bf16 v[140:143], a[80:83], v[124:127], v[140:143]\n\t"
    "s_waitcnt vmcnt(13)\n\t"
    "s_waitcnt vmcnt(8)\n\t"
    "v_mfma_f32_16x16x32_bf16 a[16:19], a[84:87], v[48:51], a[16:19]\n\t"
    "buffer_load_dwordx4 a[116:119], v8, %0, s40 offen offset:2048\n\t"
    "s_waitcnt vmcnt(8)\n\t"
    "v_mfma_f32_16x16x32_bf16 a[16:19], a[88:91], v[52:55], a[16:19]\n\t"
    "buffer_load_dwordx4 a[120:123], v8, %0, s40 offen offset:3072\n\t"
    "s_waitcnt vmcnt(7)\n\t"
    "v_mfma_f32_16x16x32_bf16 a[16:19], a[92:95], v[56:59], a[16:19]\n\t"
    "s_add_u32 s40, s40, 4096\n\t"
    "buffer_load_dwordx4 a[124:127], v8, %0, s40 offen offset:0\n\t"
    "s_waitcnt vmcnt(7)\n\t"
    "v_mfma_f32_16x16x32_bf16 a[16:19], a[96:99], v[60:63], a[16:19]\n\t"
    "buffer_load_dwordx4 a[128:131], v8, %0, s40 offen offset:1024\n\t"
    "s_waitcnt vmcnt(7)\n\t"
    "v_mfma_f32_16x16x32_bf16 a[16:19], a[100:103], v[64:67], a[16:19]\n\t"
    "buffer_load_dwordx4 a[132:135], v8, %0, s40 offen offset:2048\n\t"
    "s_waitcnt vmcnt(7)\n\t"
    "v_mfma_f32_16x16x32_bf16 a[20:23], a[104:107], v[48:51], a[20:23]\n\t"
    "buffer_load_dwordx4 a[136:139], v8, %0, s40 offen offset:3072\n\t"
    "s_waitcnt vmcnt(7)\n\t"
    "v_mfma_f32_16x16x32_bf16 a[20:23], a[108:111], v[52:55], a[20:23]\n\t"
    "s_add_u32 s40, s40, 4096\n\t"
    "buffer_load_dwordx4 a[140:143], v8, %0, s40 offen offset:0\n\t"
    "s_waitcnt vmcnt(7)\n\t"
    "v_mfma_f32_16x16x32_bf16 a[20:23], a[112:115], v[56:59], a[20:23]\n\t"
    "buffer_load_dwordx4 a[144:147], v8, %0, s40 offen offset:1024\n\t"
    "s_waitcnt vmcnt(7)\n\t"
    "v_mfma_f32_16x16x32_bf16 a[20:23], a[116:119], v[60:63], a[20:23]\n\t"
    "buffer_load_dwordx4 a[148:151], v8, %0, s40 offen offset:2048\n\t"
    "s_waitcnt vmcnt(7)\n\t"
    "v_mfma_f32_16x16x32_bf16 a[20:23], a[120:123], v[64:67], a[20:23]\n\t"
    "buffer_load_dwordx4 a[152:155], v8, %0, s40 offen offset:3072\n\t"
    "s_waitcnt vmcnt(7)\n\t"
    "v_mfma_f32_16x16x32_bf16 a[24:27], a[124:127], v[48:51], a[24:27]\n\t"
    "s_add_u32 s47, %13, 0x1bc3900\n\t"
    "buffer_load_dword v13, v35, %0, s47 offen sc1\n\t"
    "s_waitcnt vmcnt(7)\n\t"
    "v_mfma_f32_16x16x32_bf16 a[24:27], a[128:131], v[52:55], a[24:27]\n\t"
    "s_add_u32 s40, s40, 4096\n\t"
    "buffer_load_dwordx4 a[156:159], v8, %0, s40 offen offset:0\n\t"
    "s_waitcnt vmcnt(7)\n\t"
    "v_mfma_f32_16x16x32_bf16 a[24:27], a[132:135], v[56:59], a[24:27]\n\t"
    "buffer_load_dwordx4 a[160:163], v8, %0, s40 offen offset:1024\n\t"
    "s_waitcnt vmcnt(7)\n\t"
    "v_mfma_f32_16x16x32_bf16 a[24:27], a[136:139], v[60:63], a[24:27]\n\t"
    "buffer_load_dwordx4 a[164:167], v8, %0, s40 offen offset:2048\n\t"
    "s_waitcnt vmcnt(7)\n\t"
    "v_mfma_f32_16x16x32_bf16 a[24:27], a[140:143], v[64:67], a[24:27]\n\t"
    "buffer_load_dwordx4 a[168:171], v8, %0, s40 offen offset:3072\n\t"
    "s_waitcnt vmcnt(7)\n\t"
    "v_mfma_f32_16x16x32_bf16 a[28:31], a[144:147], v[48:51], a[28:31]\n\t"
    "s_add_u32 s40, s40, 4096\n\t"
    "buffer_load_dwordx4 a[172:175], v8, %0, s40 offen offset:0\n\t"
    "s_waitcnt vmcnt(7)\n\t"
    "v_mfma_f32_16x16x32_bf16 a[28:31], a[148:151], v[52:55], a[28:31]\n\t"
    "buffer_load_dwordx4 a[176:179], v8, %0, s40 offen offset:1024\n\t"
    "s_waitcnt vmcnt(7)\n\t"
    "v_mfma_f32_16x16x32_bf16 a[28:31], a[152:155], v[56:59], a[28:31]\n\t"
    "buffer_load_dwordx4 a[180:183], v8, %0, s40 offen offset:2048\n\t"
    "s_waitcnt vmcnt(6)\n\t"
    "v_mfma_f32_16x16x32_bf16 a[28:31], a[156:159], v[60:63], a[28:31]\n\t"
    "s_add_u32 s41, %9, 0xada800\n\t"
    "buffer_load_dwordx4 v[68:71], v8, %0, s41 offen offset:0 sc1\n\t"
    "s_waitcnt vmcnt(6)\n\t"
    "v_mfma_f32_16x16x32_bf16 a[28:31], a[160:163], v[64:67], a[28:31]\n\t"
    "buffer_load_dwordx4 v[72:75], v8, %0, s41 offen offset:1024 sc1\n\t"
    "s_waitcnt vmcnt(6)\n\t"
    "v_mfma_f32_16x16x32_bf16 v[132:135], a[164:167], v[48:51], v[132:135]\n\t"
    "buffer_load_dwordx4 v[76:79], v8, %0, s41 offen offset:2048 sc1\n\t"
    "s_waitcnt vmcnt(6)\n\t"
    "v_mfma_f32_16x16x32_bf16 v[132:135], a[168:171], v[52:55], v[132:135]\n\t"
    "buffer_load_dwordx4 v[80:83], v8, %0, s41 offen offset:3072 sc1\n\t"
    "s_waitcnt vmcnt(6)\n\t"
    "v_mfma_f32_16x16x32_bf16 v[132:135], a[172:175], v[56:59], v[132:135]\n\t"
    "buffer_load_dwordx4 v[84:87], v9, %0, s41 offen offset:0 sc1\n\t"
    "s_waitcnt vmcnt(6)\n\t"
    "v_mfma_f32_16x16x32_bf16 v[132:135], a[176:179], v[60:63], v[132:135]\n\t"
    "s_add_u32 s42, %9, 0xabe800\n\t"
    "buffer_load_dwordx4 v[88:91], v8, %0, s42 offen offset:0 sc1\n\t"
    "s_waitcnt vmcnt(6)\n\t"
    "v_mfma_f32_16x16x32_bf16 v[132:135], a[180:183], v[64:67], v[132:135]\n\t"
    "buffer_load_dwordx4 v[92:95], v8, %0, s42 offen offset:1024 sc1\n\t"
    "buffer_load_dwordx4 v[96:99], v8, %0, s42 offen offset:2048 sc1\n\t"
    "s_waitcnt vmcnt(7)\n\t"
    "v_mfma_f32_16x16x32_bf16 a[32:35], a[84:87], v[68:71], a[32:35]\n\t"
    "buffer_load_dwordx4 v[100:103], v8, %0, s42 offen offset:3072 sc1\n\t"
    "s_waitcnt vmcnt(7)\n\t"
    "v_mfma_f32_16x16x32_bf16 a[32:35], a[88:91], v[72:75], a[32:35]\n\t"
    "buffer_load_dwordx4 v[104:107], v9, %0, s42 offen offset:0 sc1\n\t"
    "s_waitcnt vmcnt(7)\n\t"
    "v_mfma_f32_16x16x32_bf16 a[32:35], a[92:95], v[76:79], a[32:35]\n\t"
    "s_add_u32 s41, %9, 0xae8800\n\t"
    "buffer_load_dwordx4 v[108:111], v8, %0, s41 offen offset:0 sc1\n\t"
    "s_waitcnt vmcnt(7)\n\t"
    "v_mfma_f32_16x16x32_bf16 a[32:35], a[96:99], v[80:83], a[32:35]\n\t"
    "buffer_load_dwordx4 v[112:115], v8, %0, s41 offen offset:1024 sc1\n\t"
    "s_waitcnt vmcnt(7)\n\t"
    "v_mfma_f32_16x16x32_bf16 a[32:35], a[100:103], v[84:87], a[32:35]\n\t"
    "buffer_load_dwordx4 v[116:119], v8, %0, s41 offen offset:2048 sc1\n\t"
    "v_mfma_f32_16x16x32_bf16 a[36:39], a[104:107], v[68:71], a[36:39]\n\t"
    "v_mfma_f32_16x16x32_bf16 a[36:39], a[108:111], v[72:75], a[36:39]\n\t"
    "v_mfma_f32_16x16x32_bf16 a[36:39], a[112:115], v[76:79], a[36:39]\n\t"
    "v_mfma_f32_16x16x32_bf16 a[36:39], a[116:119], v[80:83], a[36:39]\n\t"
    "v_mfma_f32_16x16x32_bf16 a[36:39], a[120:123], v[84:87], a[36:39]\n\t"
    "buffer_load_dwordx4 v[120:123], v8, %0, s41 offen offset:3072 sc1\n\t"
    "v_mfma_f32_16x16x32_bf16 a[40:43], a[124:127], v[68:71], a[40:43]\n\t"
    "v_mfma_f32_16x16x32_bf16 a[40:43], a[128:131], v[72:75], a[40:43]\n\t"
    "v_mfma_f32_16x16x32_bf16 a[40:43], a[132:135], v[76:79], a[40:43]\n\t"
    "v_mfma_f32_16x16x32_bf16 a[40:43], a[136:139], v[80:83], a[40:43]\n\t"
    "v_mfma_f32_16x16x32_bf16 a[40:43], a[140:143], v[84:87], a[40:43]\n\t"
    "buffer_load_dwordx4 v[124:127], v9, %0, s41 offen offset:0 sc1\n\t"
    "v_mfma_f32_16x16x32_bf16 a[44:47], a[144:147], v[68:71], a[44:47]\n\t"
    "v_mfma_f32_16x16x32_bf16 a[44:47], a[148:151], v[72:75], a[44:47]\n\t"
    "v_mfma_f32_16x16x32_bf16 a[44:47], a[152:155], v[76:79], a[44:47]\n\t"
    "v_mfma_f32_16x16x32_bf16 a[44:47], a[156:159], v[80:83], a[44:47]\n\t"
    "v_mfma_f32_16x16x32_bf16 a[44:47], a[160:163], v[84:87], a[44:47]\n\t"
    "v_cmp_eq_u32 vcc, -1, v13\n\t"
    "s_cbranch_vccnz H37_%=\n\t"
    "R38_%=:\n\t"
    "s_add_u32 s42, %10, 0xac7800\n\t"
    "buffer_load_dwordx4 v[48:51], v8, %0, s42 offen offset:0 sc1\n\t"
    "v_mfma_f32_16x16x32_bf16 v[136:139], a[164:167], v[68:71], v[136:139]\n\t"
    "v_mfma_f32_16x16x32_bf16 v[136:139], a[168:171], v[72:75], v[136:139]\n\t"
    "v_mfma_f32_16x16x32_bf16 v[136:139], a[172:175], v[76:79], v[136:139]\n\t"
    "v_mfma_f32_16x16x32_bf16 v[136:139], a[176:179], v[80:83], v[136:139]\n\t"
    "v_mfma_f32_16x16x32_bf16 v[136:139], a[180:183], v[84:87], v[136:139]\n\t"
    "s_waitcnt vmcnt(10)\n\t"
    "v_mfma_f32_16x16x32_bf16 a[0:3], a[84:87], v[88:91], a[0:3]\n\t"
    "s_waitcnt vmcnt(9)\n\t"
    "v_mfma_f32_16x16x32_bf16 a[0:3], a[88:91], v[92:95], a[0:3]\n\t"
    "s_waitcnt vmcnt(8)\n\t"
    "v_mfma_f32_16x16x32_bf16 a[0:3], a[92:95], v[96:99], a[0:3]\n\t"
    "s_waitcnt vmcnt(7)\n\t"
    "v_mfma_f32_16x16x32_bf16 a[0:3], a[96:99], v[100:103], a[0:3]\n\t"
    "buffer_load_dwordx4 v[52:55], v8, %0, s42 offen offset:1024 sc1\n\t"
    "s_waitcnt vmcnt(7)\n\t"
    "v_mfma_f32_16x16x32_bf16 a[0:3], a[100:103], v[104:107], a[0:3]\n\t"
    "buffer_load_dwordx4 v[56:59], v8, %0, s42 offen offset:2048 sc1\n\t"
    "v_mfma_f32_16x16x32_bf16 a[4:7], a[104:107], v[88:91], a[4:7]\n\t"
    "v_mfma_f32_16x16x32_bf16 a[4:7], a[108:111], v[92:95], a[4:7]\n\t"
    "v_mfma_f32_16x16x32_bf16 a[4:7], a[112:115], v[96:99], a[4:7]\n\t"
    "v_mfma_f32_16x16x32_bf16 a[4:7], a[116:119], v[100:103], a[4:7]\n\t"
    "v_mfma_f32_16x16x32_bf16 a[4:7], a[120:123], v[104:107], a[4:7]\n\t"
    "buffer_load_dwordx4 v[60:63], v8, %0, s42 offen offset:3072 sc1\n\t"
    "v_mfma_f32_16x16x32_bf16 a[8:11], a[124:127], v[88:91], a[8:11]\n\t"
    "v_mfma_f32_16x16x32_bf16 a[8:11], a[128:131], v[92:95], a[8:11]\n\t"
    "v_mfma_f32_16x16x32_bf16 a[8:11], a[132:135], v[96:99], a[8:11]\n\t"
    "v_mfma_f32_16x16x32_bf16 a[8:11], a[136:139], v[100:103], a[8:11]\n\t"
    "v_mfma_f32_16x16x32_bf16 a[8:11], a[140:143], v[104:107], a[8:11]\n\t"
    "buffer_load_dwordx4 a[184:187], v8, %0, s40 offen offset:3072\n\t"
    "v_mfma_f32_16x16x32_bf16 a[12:15], a[144:147], v[88:91], a[12:15]\n\t"
    "v_mfma_f32_16x16x32_bf16 a[12:15], a[148:151], v[92:95], a[12:15]\n\t"
    "v_mfma_f32_16x16x32_bf16 a[12:15], a[152:155], v[96:99], a[12:15]\n\t"
    "v_mfma_f32_16x16x32_bf16 a[12:15], a[156:159], v[100:103], a[12:15]\n\t"
    "v_mfma_f32_16x16x32_bf16 a[12:15], a[160:163], v[104:107], a[12:15]\n\t"
    "s_add_u32 s40, s40, 4096\n\t"
    "buffer_load_dwordx4 a[188:191], v8, %0, s40 offen offset:0\n\t"
    "v_mfma_f32_16x16x32_bf16 v[128:131], a[164:167], v[88:91], v[128:131]\n\t"
    "v_mfma_f32_16x16x32_bf16 v[128:131], a[168:171], v[92:95], v[128:131]\n\t"
    "v_mfma_f32_16x16x32_bf16 v[128:131], a[172:175], v[96:99], v[128:131]\n\t"
    "v_mfma_f32_16x16x32_bf16 v[128:131], a[176:179], v[100:103], v[128:131]\n\t"
    "v_mfma_f32_16x16x32_bf16 v[128:131], a[180:183], v[104:107], v[128:131]\n\t"
    "buffer_load_dwordx4 a[192:195], v8, %0, s40 offen offset:1024\n\t"
    "s_waitcnt vmcnt(11)\n\t"
    "v_mfma_f32_16x16x32_bf16 a[48:51], a[84:87], v[108:111], a[48:51]\n\t"
    "s_waitcnt vmcnt(10)\n\t"
    "v_mfma_f32_16x16x32_bf16 a[48:51], a[88:91], v[112:115], a[48:51]\n\t"
    "s_waitcnt vmcnt(9)\n\t"
    "v_mfma_f32_16x16x32_bf16 a[48:51], a[92:95], v[116:119], a[48:51]\n\t"
    "s_waitcnt vmcnt(8)\n\t"
    "v_mfma_f32_16x16x32_bf16 a[48:51], a[96:99], v[120:123], a[48:51]\n\t"
    "s_waitcnt vmcnt(7)\n\t"
    "v_mfma_f32_16x16x32_bf16 a[48:51], a[100:103], v[124:127], a[48:51]\n\t"
    "buffer_load_dwordx4 a[196:199], v8, %0, s40 offen offset:2048\n\t"
    "v_mfma_f32_16x16x32_bf16 a[52:55], a[104:107], v[108:111], a[52:55]\n\t"
    "v_mfma_f32_16x16x32_bf16 a[52:55], a[108:111], v[112:115], a[52:55]\n\t"
    "v_mfma_f32_16x16x32_bf16 a[52:55], a[112:115], v[116:119], a[52:55]\n\t"
    "v_mfma_f32_16x16x32_bf16 a[52:55], a[116:119], v[120:123], a[52:55]\n\t"
    "v_mfma_f32_16x16x32_bf16 a[52:55], a[120:123], v[124:127], a[52:55]\n\t"
    "buffer_load_dwordx4 a[200:203], v8, %0, s40 offen offset:3072\n\t"
    "v_mfma_f32_16x16x32_bf16 a[56:59], a[124:127], v[108:111], a[56:59]\n\t"
    "v_mfma_f32_16x16x32_bf16 a[56:59], a[128:131], v[112:115], a[56:59]\n\t"
    "v_mfma_f32_16x16x32_bf16 a[56:59], a[132:135], v[116:119], a[56:59]\n\t"
    "v_mfma_f32_16x16x32_bf16 a[56:59], a[136:139], v[120:123], a[56:59]\n\t"
    "v_mfma_f32_16x16x32_bf16 a[56:59], a[140:143], v[124:127], a[56:59]\n\t"
    "v_mfma_f32_16x16x32_bf16 a[60:63], a[144:147], v[108:111], a[60:63]\n\t"
    "v_mfma_f32_16x16x32_bf16 a[60:63], a[148:151], v[112:115], a[60:63]\n\t"
    "v_mfma_f32_16x16x32_bf16 a[60:63], a[152:155], v[116:119], a[60:63]\n\t"
    "v_mfma_f32_16x16x32_bf16 a[60:63], a[156:159], v[120:123], a[60:63]\n\t"
    "s_add_u32 s40, s40, 4096\n\t"
    "buffer_load_dwordx4 a[204:207], v8, %0, s40 offen offset:0\n\t"
    "v_mfma_f32_16x16x32_bf16 a[60:63], a[160:163], v[124:127], a[60:63]\n\t"
    "v_mfma_f32_16x16x32_bf16 v[140:143], a[164:167], v[108:111], v[140:143]\n\t"
    "v_mfma_f32_16x16x32_bf16 v[140:143], a[168:171], v[112:115], v[140:143]\n\t"
    "v_mfma_f32_16x16x32_bf16 v[140:143], a[172:175], v[116:119], v[140:143]\n\t"
    "v_mfma_f32_16x16x32_bf16 v[140:143], a[176:179], v[120:123], v[140:143]\n\t"
    "buffer_load_dwordx4 a[208:211], v8, %0, s40 offen offset:1024\n\t"
    "v_mfma_f32_16x16x32_bf16 v[140:143], a[180:183], v[124:127], v[140:143]\n\t"
    "s_waitcnt vmcnt(6)\n\t"
    "v_mfma_f32_16x16x32_bf16 a[16:19], a[184:187], v[48:51], a[16:19]\n\t"
    "buffer_load_dwordx4 a[212:215], v8, %0, s40 offen offset:2048\n\t"
    "s_waitcnt vmcnt(6)\n\t"
    "v_mfma_f32_16x16x32_bf16 a[16:19], a[188:191], v[52:55], a[16:19]\n\t"
    "buffer_load_dwordx4 a[216:219], v8, %0, s40 offen offset:3072\n\t"
    "s_waitcnt vmcnt(6)\n\t"
    "v_mfma_f32_16x16x32_bf16 a[16:19], a[192:195], v[56:59], a[16:19]\n\t"
    "s_add_u32 s40, s40, 4096\n\t"
    "buffer_load_dwordx4 a[220:223], v8, %0, s40 offen offset:0\n\t"
    "s_waitcnt vmcnt(6)\n\t"
    "v_mfma_f32_16x16x32_bf16 a[16:19], a[196:199], v[60:63], a[16:19]\n\t"
    "buffer_load_dwordx4 a[224:227], v8, %0, s40 offen offset:1024\n\t"
    "s_waitcnt vmcnt(6)\n\t"
    "v_mfma_f32_16x16x32_bf16 a[20:23], a[200:203], v[48:51], a[20:23]\n\t"
    "buffer_load_dwordx4 a[228:231], v8, %0, s40 offen offset:2048\n\t"
    "s_waitcnt vmcnt(6)\n\t"
    "v_mfma_f32_16x16x32_bf16 a[20:23], a[204:207], v[52:55], a[20:23]\n\t"
    "buffer_load_dwordx4 a[232:235], v8, %0, s40 offen offset:3072\n\t"
    "s_waitcnt vmcnt(6)\n\t"
    "v_mfma_f32_16x16x32_bf16 a[20:23], a[208:211], v[56:59], a[20:23]\n\t"
    "s_add_u32 s40, s40, 4096\n\t"
    "buffer_load_dwordx4 a[236:239], v8, %0, s40 offen offset:0\n\t"
    "ds_write_b128 v24, a[16:19] offset:0\n\t"
    "s_waitcnt vmcnt(6)\n\t"
    "v_mfma_f32_16x16x32_bf16 a[20:23], a[212:215], v[60:63], a[20:23]\n\t"
    "buffer_load_dwordx4 a[240:243], v8, %0, s40 offen offset:1024\n\t"
    "s_waitcnt vmcnt(6)\n\t"
    "v_mfma_f32_16x16x32_bf16 a[24:27], a[216:219], v[48:51], a[24:27]\n\t"
    "buffer_load_dwordx4 a[244:247], v8, %0, s40 offen offset:2048\n\t"
    "s_waitcnt vmcnt(6)\n\t"
    "v_mfma_f32_16x16x32_bf16 a[24:27], a[220:223], v[52:55], a[24:27]\n\t"
    "buffer_load_dwordx4 a[248:251], v8, %0, s40 offen offset:3072\n\t"
    "s_waitcnt vmcnt(6)\n\t"
    "v_mfma_f32_16x16x32_bf16 a[24:27], a[224:227], v[56:59], a[24:27]\n\t"
    "s_add_u32 s40, s40, 4096\n\t"
    "buffer_load_dwordx4 a[252:255], v8, %0, s40 offen offset:0\n\t"
    "ds_write_b128 v25, a[20:23] offset:0\n\t"
    "s_waitcnt vmcnt(6)\n\t"
    "v_mfma_f32_16x16x32_bf16 a[24:27], a[228:231], v[60:63], a[24:27]\n\t"
    "buffer_load_dwordx4 v[228:231], v8, %0, s40 offen offset:1024\n\t"
    "s_waitcnt vmcnt(6)\n\t"
    "v_mfma_f32_16x16x32_bf16 a[28:31], a[232:235], v[48:51], a[28:31]\n\t"
    "buffer_load_dwordx4 v[232:235], v8, %0, s40 offen offset:2048\n\t"
    "s_waitcnt vmcnt(6)\n\t"
    "v_mfma_f32_16x16x32_bf16 a[28:31], a[236:239], v[52:55], a[28:31]\n\t"
    "s_add_u32 s41, %10, 0xad5800\n\t"
    "buffer_load_dwordx4 v[68:71], v8, %0, s41 offen offset:0 sc1\n\t"
    "ds_write_b128 v26, a[24:27] offset:0\n\t"
    "s_waitcnt vmcnt(6)\n\t"
    "v_mfma_f32_16x16x32_bf16 a[28:31], a[240:243], v[56:59], a[28:31]\n\t"
    "buffer_load_dwordx4 v[72:75], v8, %0, s41 offen offset:1024 sc1\n\t"
    "s_waitcnt vmcnt(6)\n\t"
    "v_mfma_f32_16x16x32_bf16 a[28:31], a[244:247], v[60:63], a[28:31]\n\t"
    "buffer_load_dwordx4 v[76:79], v8, %0, s41 offen offset:2048 sc1\n\t"
    "s_waitcnt vmcnt(6)\n\t"
    "v_mfma_f32_16x16x32_bf16 v[132:135], a[248:251], v[48:51], v[132:135]\n\t"
    "buffer_load_dwordx4 v[80:83], v8, %0, s41 offen offset:3072 sc1\n\t"
    "s_waitcnt vmcnt(6)\n\t"
    "v_mfma_f32_16x16x32_bf16 v[132:135], a[252:255], v[52:55], v[132:135]\n\t"
    "s_add_u32 s42, %10, 0xab9800\n\t"
    "buffer_load_dwordx4 v[88:91], v8, %0, s42 offen offset:0 sc1\n\t"
    "ds_write_b128 v28, a[28:31] offset:0\n\t"
    "s_waitcnt vmcnt(6)\n\t"
    "v_mfma_f32_16x16x32_bf16 v[132:135], v[228:231], v[56:59], v[132:135]\n\t"
    "buffer_load_dwordx4 v[92:95], v8, %0, s42 offen offset:1024 sc1\n\t"
    "s_waitcnt vmcnt(6)\n\t"
    "v_mfma_f32_16x16x32_bf16 v[132:135], v[232:235], v[60:63], v[132:135]\n\t"
    "buffer_load_dwordx4 v[96:99], v8, %0, s42 offen offset:2048 sc1\n\t"
    "s_waitcnt lgkmcnt(0)\n\t"
    "s_barrier\n\t"
    "buffer_load_dwordx4 v[100:103], v8, %0, s42 offen offset:3072 sc1\n\t"
    "ds_read_b128 v[176:179], v27 offset:0\n\t"
    "ds_read_b128 v[180:183], v27 offset:1024\n\t"
    "ds_read_b128 v[184:187], v27 offset:2048\n\t"
    "ds_read_b32 v188, v29 offset:0\n\t"
    "ds_read_b32 v189, v29 offset:1024\n\t"
    "ds_read_b32 v190, v29 offset:2048\n\t"
    "ds_read_b32 v191, v29 offset:3072\n\t"
    "s_waitcnt vmcnt(7)\n\t"
    "v_mfma_f32_16x16x32_bf16 a[32:35], a[184:187], v[68:71], a[32:35]\n\t"
    "s_add_u32 s41, %10, 0xae3800\n\t"
    "buffer_load_dwordx4 v[108:111], v8, %0, s41 offen offset:0 sc1\n\t"
    "s_waitcnt lgkmcnt(6)\n\t"
    "s_waitcnt lgkmcnt(5)\n\t"
    "v_add_f32 v176, v176, v180\n\t"
    "v_add_f32 v177, v177, v181\n\t"
    "v_add_f32 v178, v178, v182\n\t"
    "s_waitcnt vmcnt(7)\n\t"
    "v_mfma_f32_16x16x32_bf16 a[32:35], a[188:191], v[72:75], a[32:35]\n\t"
    "buffer_load_dwordx4 v[112:115], v8, %0, s41 offen offset:1024 sc1\n\t"
    "v_add_f32 v179, v179, v183\n\t"
    "s_waitcnt lgkmcnt(4)\n\t"
    "v_add_f32 v132, v132, v184\n\t"
    "v_add_f32 v133, v133, v185\n\t"
    "s_waitcnt vmcnt(7)\n\t"
    "v_mfma_f32_16x16x32_bf16 a[32:35], a[192:195], v[76:79], a[32:35]\n\t"
    "buffer_load_dwordx4 v[116:119], v8, %0, s41 offen offset:2048 sc1\n\t"
    "v_add_f32 v134, v134, v186\n\t"
    "v_add_f32 v135, v135, v187\n\t"
    "s_waitcnt lgkmcnt(3)\n\t"
    "s_waitcnt lgkmcnt(2)\n\t"
    "v_add_f32 v188, v188, v189\n\t"
    "s_waitcnt vmcnt(7)\n\t"
    "v_mfma_f32_16x16x32_bf16 a[32:35], a[196:199], v[80:83], a[32:35]\n\t"
    "buffer_load_dwordx4 v[120:123], v8, %0, s41 offen offset:3072 sc1\n\t"
    "s_waitcnt lgkmcnt(1)\n\t"
    "s_waitcnt lgkmcnt(0)\n\t"
    "v_add_f32 v190, v190, v191\n\t"
    "v_add_f32 v132, v132, v176\n\t"
    "v_add_f32 v133, v133, v177\n\t"
    "v_mfma_f32_16x16x32_bf16 a[36:39], a[200:203], v[68:71], a[36:39]\n\t"
    "v_add_f32 v134, v134, v178\n\t"
    "ds_write_b128 v24, a[32:35] offset:16384\n\t"
    "v_add_f32 v135, v135, v179\n\t"
    "v_mfma_f32_16x16x32_bf16 a[36:39], a[204:207], v[72:75], a[36:39]\n\t"
    "v_add_f32 v188, v188, v190\n\t"
    "s_mov_b32 s43, 20480\n\t"
    "v_cvt_pk_bf16_f32 v212, v132, v133\n\t"
    "v_mfma_f32_16x16x32_bf16 a[36:39], a[208:211], v[76:79], a[36:39]\n\t"
    "v_cvt_pk_bf16_f32 v213, v134, v135\n\t"
    "v_add_f32 v188, v188, v145\n\t"
    "buffer_store_dwordx2 v[212:213], v22, %2, s43 offen\n\t"
    "v_mfma_f32_16x16x32_bf16 a[36:39], a[212:215], v[80:83], a[36:39]\n\t"
    "v_cvt_pk_bf16_f32 v214, v188, v188\n\t"
    "buffer_store_short v214, v23, %2, s43 offen\n\t"
    "v_mfma_f32_16x16x32_bf16 a[40:43], a[216:219], v[68:71], a[40:43]\n\t"
    "v_mfma_f32_16x16x32_bf16 a[40:43], a[220:223], v[72:75], a[40:43]\n\t"
    "v_mfma_f32_16x16x32_bf16 a[40:43], a[224:227], v[76:79], a[40:43]\n\t"
    "v_mfma_f32_16x16x32_bf16 a[40:43], a[228:231], v[80:83], a[40:43]\n\t"
    "v_mfma_f32_16x16x32_bf16 a[44:47], a[232:235], v[68:71], a[44:47]\n\t"
    "v_mfma_f32_16x16x32_bf16 a[44:47], a[236:239], v[72:75], a[44:47]\n\t"
    "ds_write_b128 v25, a[36:39] offset:16384\n\t"
    "v_mfma_f32_16x16x32_bf16 a[44:47], a[240:243], v[76:79], a[44:47]\n\t"
    "v_mfma_f32_16x16x32_bf16 a[44:47], a[244:247], v[80:83], a[44:47]\n\t"
    "v_mfma_f32_16x16x32_bf16 v[136:139], a[248:251], v[68:71], v[136:139]\n\t"
    "v_mfma_f32_16x16x32_bf16 v[136:139], a[252:255], v[72:75], v[136:139]\n\t"
    "v_mfma_f32_16x16x32_bf16 v[136:139], v[228:231], v[76:79], v[136:139]\n\t"
    "ds_write_b128 v26, a[40:43] offset:16384\n\t"
    "v_mfma_f32_16x16x32_bf16 v[136:139], v[232:235], v[80:83], v[136:139]\n\t"
    "s_nop 2\n\t"
    "ds_write_b128 v28, a[44:47] offset:16384\n\t"
    "s_waitcnt lgkmcnt(0)\n\t"
    "s_barrier\n\t"
    "s_waitcnt vmcnt(9)\n\t"
    "v_mfma_f32_16x16x32_bf16 a[0:3], a[184:187], v[88:91], a[0:3]\n\t"
    "ds_read_b128 v[176:179], v27 offset:16384\n\t"
    "ds_read_b128 v[180:183], v27 offset:17408\n\t"
    "ds_read_b128 v[184:187], v27 offset:18432\n\t"
    "s_waitcnt vmcnt(8)\n\t"
    "v_mfma_f32_16x16x32_bf16 a[0:3], a[188:191], v[92:95], a[0:3]\n\t"
    "ds_read_b32 v188, v29 offset:16384\n\t"
    "ds_read_b32 v189, v29 offset:17408\n\t"
    "ds_read_b32 v190, v29 offset:18432\n\t"
    "s_waitcnt vmcnt(7)\n\t"
    "v_mfma_f32_16x16x32_bf16 a[0:3], a[192:195], v[96:99], a[0:3]\n\t"
    "ds_read_b32 v191, v29 offset:19456\n\t"
    "s_waitcnt vmcnt(6)\n\t"
    "v_mfma_f32_16x16x32_bf16 a[0:3], a[196:199], v[100:103], a[0:3]\n\t"
    "v_mfma_f32_16x16x32_bf16 a[4:7], a[200:203], v[88:91], a[4:7]\n\t"
    "v_mfma_f32_16x16x32_bf16 a[4:7], a[204:207], v[92:95], a[4:7]\n\t"
    "v_mfma_f32_16x16x32_bf16 a[4:7], a[208:211], v[96:99], a[4:7]\n\t"
    "v_mfma_f32_16x16x32_bf16 a[4:7], a[212:215], v[100:103], a[4:7]\n\t"
    "v_mfma_f32_16x16x32_bf16 a[8:11], a[216:219], v[88:91], a[8:11]\n\t"
    "v_mfma_f32_16x16x32_bf16 a[8:11], a[220:223], v[92:95], a[8:11]\n\t"
    "v_mfma_f32_16x16x32_bf16 a[8:11], a[224:227], v[96:99], a[8:11]\n\t"
    "s_waitcnt lgkmcnt(6)\n\t"
    "s_waitcnt lgkmcnt(5)\n\t"
    "v_add_f32 v176, v176, v180\n\t"
    "ds_write_b128 v24, a[0:3] offset:0\n\t"
    "v_add_f32 v177, v177, v181\n\t"
    "v_mfma_f32_16x16x32_bf16 a[8:11], a[228:231], v[100:103], a[8:11]\n\t"
    "ds_write_b128 v25, a[4:7] offset:0\n\t"
    "v_add_f32 v178, v178, v182\n\t"
    "v_add_f32 v179, v179, v183\n\t"
    "v_mfma_f32_16x16x32_bf16 a[12:15], a[232:235], v[88:91], a[12:15]\n\t"
    "s_waitcnt lgkmcnt(6)\n\t"
    "v_add_f32 v136, v136, v184\n\t"
    "v_add_f32 v137, v137, v185\n\t"
    "v_add_f32 v138, v138, v186\n\t"
    "v_mfma_f32_16x16x32_bf16 a[12:15], a[236:239], v[92:95], a[12:15]\n\t"
    "ds_write_b128 v26, a[8:11] offset:0\n\t"
    "v_add_f32 v139, v139, v187\n\t"
    "s_waitcnt lgkmcnt(6)\n\t"
    "s_waitcnt lgkmcnt(5)\n\t"
    "v_add_f32 v188, v188, v189\n\t"
    "v_mfma_f32_16x16x32_bf16 a[12:15], a[240:243], v[96:99], a[12:15]\n\t"
    "s_waitcnt lgkmcnt(4)\n\t"
    "s_waitcnt lgkmcnt(3)\n\t"
    "v_add_f32 v190, v190, v191\n\t"
    "v_add_f32 v136, v136, v176\n\t"
    "v_add_f32 v137, v137, v177\n\t"
    "v_mfma_f32_16x16x32_bf16 a[12:15], a[244:247], v[100:103], a[12:15]\n\t"
    "v_add_f32 v138, v138, v178\n\t"
    "v_add_f32 v139, v139, v179\n\t"
    "v_add_f32 v188, v188, v190\n\t"
    "v_mfma_f32_16x16x32_bf16 v[128:131], a[248:251], v[88:91], v[128:131]\n\t"
    "s_mov_b32 s43, 40960\n\t"
    "v_cvt_pk_bf16_f32 v212, v136, v137\n\t"
    "v_cvt_pk_bf16_f32 v213, v138, v139\n\t"
    "v_mfma_f32_16x16x32_bf16 v[128:131], a[252:255], v[92:95], v[128:131]\n\t"
    "ds_write_b128 v28, a[12:15] offset:0\n\t"
    "v_add_f32 v188, v188, v146\n\t"
    "buffer_store_dwordx2 v[212:213], v22, %2, s43 offen\n\t"
    "v_mfma_f32_16x16x32_bf16 v[128:131], v[228:231], v[96:99], v[128:131]\n\t"
    "v_cvt_pk_bf16_f32 v214, v188, v188\n\t"
    "buffer_store_short v214, v23, %2, s43 offen\n\t"
    "v_mfma_f32_16x16x32_bf16 v[128:131], v[232:235], v[100:103], v[128:131]\n\t"
    "s_waitcnt lgkmcnt(0)\n\t"
    "s_barrier\n\t"
    "s_waitcnt vmcnt(7)\n\t"
    "v_mfma_f32_16x16x32_bf16 a[48:51], a[184:187], v[108:111], a[48:51]\n\t"
    "ds_read_b128 v[176:179], v27 offset:0\n\t"
    "ds_read_b128 v[180:183], v27 offset:1024\n\t"
    "ds_read_b128 v[184:187], v27 offset:2048\n\t"
    "s_waitcnt vmcnt(6)\n\t"
    "v_mfma_f32_16x16x32_bf16 a[48:51], a[188:191], v[112:115], a[48:51]\n\t"
    "ds_read_b32 v188, v29 offset:0\n\t"
    "ds_read_b32 v189, v29 offset:1024\n\t"
    "ds_read_b32 v190, v29 offset:2048\n\t"
    "s_waitcnt vmcnt(5)\n\t"
    "v_mfma_f32_16x16x32_bf16 a[48:51], a[192:195], v[116:119], a[48:51]\n\t"
    "ds_read_b32 v191, v29 offset:3072\n\t"
    "s_waitcnt vmcnt(4)\n\t"
    "v_mfma_f32_16x16x32_bf16 a[48:51], a[196:199], v[120:123], a[48:51]\n\t"
    "v_mfma_f32_16x16x32_bf16 a[52:55], a[200:203], v[108:111], a[52:55]\n\t"
    "v_mfma_f32_16x16x32_bf16 a[52:55], a[204:207], v[112:115], a[52:55]\n\t"
    "v_mfma_f32_16x16x32_bf16 a[52:55], a[208:211], v[116:119], a[52:55]\n\t"
    "v_mfma_f32_16x16x32_bf16 a[52:55], a[212:215], v[120:123], a[52:55]\n\t"
    "v_mfma_f32_16x16x32_bf16 a[56:59], a[216:219], v[108:111], a[56:59]\n\t"
    "v_mfma_f32_16x16x32_bf16 a[56:59], a[220:223], v[112:115], a[56:59]\n\t"
    "v_mfma_f32_16x16x32_bf16 a[56:59], a[224:227], v[116:119], a[56:59]\n\t"
    "s_waitcnt lgkmcnt(6)\n\t"
    "s_waitcnt lgkmcnt(5)\n\t"
    "v_add_f32 v176, v176, v180\n\t"
    "ds_write_b128 v24, a[48:51] offset:16384\n\t"
    "v_add_f32 v177, v177, v181\n\t"
    "v_mfma_f32_16x16x32_bf16 a[56:59], a[228:231], v[120:123], a[56:59]\n\t"
    "ds_write_b128 v25, a[52:55] offset:16384\n\t"
    "v_add_f32 v178, v178, v182\n\t"
    "v_add_f32 v179, v179, v183\n\t"
    "v_mfma_f32_16x16x32_bf16 a[60:63], a[232:235], v[108:111], a[60:63]\n\t"
    "s_waitcnt lgkmcnt(6)\n\t"
    "v_add_f32 v128, v128, v184\n\t"
    "v_add_f32 v129, v129, v185\n\t"
    "v_add_f32 v130, v130, v186\n\t"
    "v_mfma_f32_16x16x32_bf16 a[60:63], a[236:239], v[112:115], a[60:63]\n\t"
    "ds_write_b128 v26, a[56:59] offset:16384\n\t"
    "v_add_f32 v131, v131, v187\n\t"
    "s_waitcnt lgkmcnt(6)\n\t"
    "s_waitcnt lgkmcnt(5)\n\t"
    "v_add_f32 v188, v188, v189\n\t"
    "v_mfma_f32_16x16x32_bf16 a[60:63], a[240:243], v[116:119], a[60:63]\n\t"
    "s_waitcnt lgkmcnt(4)\n\t"
    "s_waitcnt lgkmcnt(3)\n\t"
    "v_add_f32 v190, v190, v191\n\t"
    "v_add_f32 v128, v128, v176\n\t"
    "v_add_f32 v129, v129, v177\n\t"
    "v_mfma_f32_16x16x32_bf16 a[60:63], a[244:247], v[120:123], a[60:63]\n\t"
    "v_add_f32 v130, v130, v178\n\t"
    "v_add_f32 v131, v131, v179\n\t"
    "v_add_f32 v188, v188, v190\n\t"
    "v_mfma_f32_16x16x32_bf16 v[140:143], a[248:251], v[108:111], v[140:143]\n\t"
    "s_mov_b32 s43, 0\n\t"
    "v_cvt_pk_bf16_f32 v212, v128, v129\n\t"
    "v_cvt_pk_bf16_f32 v213, v130, v131\n\t"
    "v_mfma_f32_16x16x32_bf16 v[140:143], a[252:255], v[112:115], v[140:143]\n\t"
    "ds_write_b128 v28, a[60:63] offset:16384\n\t"
    "v_add_f32 v188, v188, v144\n\t"
    "buffer_store_dwordx2 v[212:213], v22, %2, s43 offen\n\t"
    "v_mfma_f32_16x16x32_bf16 v[140:143], v[228:231], v[116:119], v[140:143]\n\t"
    "v_cvt_pk_bf16_f32 v214, v188, v188\n\t"
    "buffer_store_short v214, v23, %2, s43 offen\n\t"
    "v_mfma_f32_16x16x32_bf16 v[140:143], v[232:235], v[120:123], v[140:143]\n\t"
    "s_waitcnt lgkmcnt(0)\n\t"
    "s_barrier\n\t"
    "ds_read_b128 v[176:179], v27 offset:16384\n\t"
    "ds_read_b128 v[180:183], v27 offset:17408\n\t"
    "ds_read_b128 v[184:187], v27 offset:18432\n\t"
    "ds_read_b32 v188, v29 offset:16384\n\t"
    "ds_read_b32 v189, v29 offset:17408\n\t"
    "ds_read_b32 v190, v29 offset:18432\n\t"
    "ds_read_b32 v191, v29 offset:19456\n\t"
    "s_waitcnt lgkmcnt(6)\n\t"
    "s_waitcnt lgkmcnt(5)\n\t"
    "v_add_f32 v176, v176, v180\n\t"
    "v_add_f32 v177, v177, v181\n\t"
    "v_add_f32 v178, v178, v182\n\t"
    "v_add_f32 v179, v179, v183\n\t"
    "s_waitcnt lgkmcnt(4)\n\t"
    "v_add_f32 v140, v140, v184\n\t"
    "v_add_f32 v141, v141, v185\n\t"
    "v_add_f32 v142, v142, v186\n\t"
    "v_add_f32 v143, v143, v187\n\t"
    "s_waitcnt lgkmcnt(3)\n\t"
    "s_waitcnt lgkmcnt(2)\n\t"
    "v_add_f32 v188, v188, v189\n\t"
    "s_waitcnt lgkmcnt(1)\n\t"
    "s_waitcnt lgkmcnt(0)\n\t"
    "v_add_f32 v190, v190, v191\n\t"
    "v_add_f32 v140, v140, v176\n\t"
    "v_add_f32 v141, v141, v177\n\t"
    "v_add_f32 v142, v142, v178\n\t"
    "v_add_f32 v143, v143, v179\n\t"
    "v_add_f32 v188, v188, v190\n\t"
    "s_mov_b32 s43, 61440\n\t"
    "v_cvt_pk_bf16_f32 v212, v140, v141\n\t"
    "v_cvt_pk_bf16_f32 v213, v142, v143\n\t"
    "v_add_f32 v188, v188, v147\n\t"
    "buffer_store_dwordx2 v[212:213], v22, %2, s43 offen\n\t"
    "v_cvt_pk_bf16_f32 v214, v188, v188\n\t"
    "buffer_store_short v214, v23, %2, s43 offen\n\t"
    "s_branch KEND_%=\n\t"
    "H1_%=:\n\t"
    "s_sleep 2\n\t"
    "s_add_u32 s46, %8, 0x1443800\n\t"
    "buffer_load_dwordx4 v[48:51], v8, %0, s46 offen offset:0 sc1\n\t"
    "buffer_load_dwordx4 v[52:55], v8, %0, s46 offen offset:1024 sc1\n\t"
    "buffer_load_dwordx4 v[56:59], v8, %0, s46 offen offset:2048 sc1\n\t"
    "buffer_load_dwordx4 v[60:63], v8, %0, s46 offen offset:3072 sc1\n\t"
    "buffer_load_dwordx4 v[64:67], v9, %0, s46 offen offset:0 sc1\n\t"
    "s_waitcnt vmcnt(0)\n\t"
    "v_max3_u32 v227, v48, v52, v56\n\t"
    "v_max3_u32 v227, v227, v60, v64\n\t"
    "v_cmp_eq_u32 vcc, -1, v227\n\t"
    "s_nop 1\n\t"
    "s_cbranch_vccz R2_%=\n\t"
    "s_cmp_eq_u32 s45, 0\n\t"
    "s_cbranch_scc1 R2_%=\n\t"
    "s_sub_u32 s45, s45, 1\n\t"
    "s_branch H1_%=\n\t"
    "H3_%=:\n\t"
    "s_sleep 2\n\t"
    "s_add_u32 s46, %8, 0x1448800\n\t"
    "buffer_load_dwordx4 v[68:71], v8, %0, s46 offen offset:0 sc1\n\t"
    "buffer_load_dwordx4 v[72:75], v8, %0, s46 offen offset:1024 sc1\n\t"
    "buffer_load_dwordx4 v[76:79], v8, %0, s46 offen offset:2048 sc1\n\t"
    "buffer_load_dwordx4 v[80:83], v8, %0, s46 offen offset:3072 sc1\n\t"
    "buffer_load_dwordx4 v[84:87], v9, %0, s46 offen offset:0 sc1\n\t"
    "s_waitcnt vmcnt(0)\n\t"
    "v_max3_u32 v227, v68, v72, v76\n\t"
    "v_max3_u32 v227, v227, v80, v84\n\t"
    "v_cmp_eq_u32 vcc, -1, v227\n\t"
    "s_nop 1\n\t"
    "s_cbranch_vccz R4_%=\n\t"
    "s_cmp_eq_u32 s45, 0\n\t"
    "s_cbranch_scc1 R4_%=\n\t"
    "s_sub_u32 s45, s45, 1\n\t"
    "s_branch H3_%=\n\t"
    "H5_%=:\n\t"
    "s_sleep 2\n\t"
    "s_add_u32 s46, %8, 0x144d800\n\t"
    "buffer_load_dwordx4 v[88:91], v8, %0, s46 offen offset:0 sc1\n\t"
    "buffer_load_dwordx4 v[92:95], v8, %0, s46 offen offset:1024 sc1\n\t"
    "buffer_load_dwordx4 v[96:99], v8, %0, s46 offen offset:2048 sc1\n\t"
    "buffer_load_dwordx4 v[100:103], v8, %0, s46 offen offset:3072 sc1\n\t"
    "buffer_load_dwordx4 v[104:107], v9, %0, s46 offen offset:0 sc1\n\t"
    "s_waitcnt vmcnt(0)\n\t"
    "v_max3_u32 v227, v88, v92, v96\n\t"
    "v_max3_u32 v227, v227, v100, v104\n\t"
    "v_cmp_eq_u32 vcc, -1, v227\n\t"
    "s_nop 1\n\t"
    "s_cbranch_vccz R6_%=\n\t"
    "s_cmp_eq_u32 s45, 0\n\t"
    "s_cbranch_scc1 R6_%=\n\t"
    "s_sub_u32 s45, s45, 1\n\t"
    "s_branch H5_%=\n\t"
    "H7_%=:\n\t"
    "s_sleep 2\n\t"
    "s_add_u32 s46, %8, 0x1452800\n\t"
    "buffer_load_dwordx4 v[108:111], v8, %0, s46 offen offset:0 sc1\n\t"
    "buffer_load_dwordx4 v[112:115], v8, %0, s46 offen offset:1024 sc1\n\t"
    "buffer_load_dwordx4 v[116:119], v8, %0, s46 offen offset:2048 sc1\n\t"
    "buffer_load_dwordx4 v[120:123], v8, %0, s46 offen offset:3072 sc1\n\t"
    "buffer_load_dwordx4 v[124:127], v9, %0, s46 offen offset:0 sc1\n\t"
    "s_waitcnt vmcnt(0)\n\t"
    "v_max3_u32 v227, v108, v112, v116\n\t"
    "v_max3_u32 v227, v227, v120, v124\n\t"
    "v_cmp_eq_u32 vcc, -1, v227\n\t"
    "s_nop 1\n\t"
    "s_cbranch_vccz R8_%=\n\t"
    "s_cmp_eq_u32 s45, 0\n\t"
    "s_cbranch_scc1 R8_%=\n\t"
    "s_sub_u32 s45, s45, 1\n\t"
    "s_branch H7_%=\n\t"
    "H9_%=:\n\t"
    "s_sleep 2\n\t"
    "s_add_u32 s46, %8, 0x16c3800\n\t"
    "buffer_load_dwordx4 v[48:51], v8, %0, s46 offen offset:0 sc1\n\t"
    "buffer_load_dwordx4 v[52:55], v8, %0, s46 offen offset:1024 sc1\n\t"
    "buffer_load_dwordx4 v[56:59], v8, %0, s46 offen offset:2048 sc1\n\t"
    "buffer_load_dwordx4 v[60:63], v8, %0, s46 offen offset:3072 sc1\n\t"
    "buffer_load_dwordx4 v[64:67], v9, %0, s46 offen offset:0 sc1\n\t"
    "s_waitcnt vmcnt(0)\n\t"
    "v_max3_u32 v227, v48, v52, v56\n\t"
    "v_max3_u32 v227, v227, v60, v64\n\t"
    "v_max3_u32 v227, v227, v50, v54\n\t"
    "v_max3_u32 v227, v227, v58, v62\n\t"
    "v_max_u32 v227, v227, v66\n\t"
    "v_cmp_eq_u32 vcc, -1, v227\n\t"
    "s_nop 1\n\t"
    "s_cbranch_vccz R10_%=\n\t"
    "s_cmp_eq_u32 s45, 0\n\t"
    "s_cbranch_scc1 R10_%=\n\t"
    "s_sub_u32 s45, s45, 1\n\t"
    "s_branch H9_%=\n\t"
    "H11_%=:\n\t"
    "s_sleep 2\n\t"
    "s_add_u32 s46, %8, 0x16c8800\n\t"
    "buffer_load_dwordx4 v[68:71], v8, %0, s46 offen offset:0 sc1\n\t"
    "buffer_load_dwordx4 v[72:75], v8, %0, s46 offen offset:1024 sc1\n\t"
    "buffer_load_dwordx4 v[76:79], v8, %0, s46 offen offset:2048 sc1\n\t"
    "buffer_load_dwordx4 v[80:83], v8, %0, s46 offen offset:3072 sc1\n\t"
    "buffer_load_dwordx4 v[84:87], v9, %0, s46 offen offset:0 sc1\n\t"
    "s_waitcnt vmcnt(0)\n\t"
    "v_max3_u32 v227, v68, v72, v76\n\t"
    "v_max3_u32 v227, v227, v80, v84\n\t"
    "v_max3_u32 v227, v227, v70, v74\n\t"
    "v_max3_u32 v227, v227, v78, v82\n\t"
    "v_max_u32 v227, v227, v86\n\t"
    "v_cmp_eq_u32 vcc, -1, v227\n\t"
    "s_nop 1\n\t"
    "s_cbranch_vccz R12_%=\n\t"
    "s_cmp_eq_u32 s45, 0\n\t"
    "s_cbranch_scc1 R12_%=\n\t"
    "s_sub_u32 s45, s45, 1\n\t"
    "s_branch H11_%=\n\t"
    "H13_%=:\n\t"
    "s_sleep 2\n\t"
    "s_add_u32 s46, %8, 0x16cd800\n\t"
    "buffer_load_dwordx4 v[88:91], v8, %0, s46 offen offset:0 sc1\n\t"
    "buffer_load_dwordx4 v[92:95], v8, %0, s46 offen offset:1024 sc1\n\t"
    "buffer_load_dwordx4 v[96:99], v8, %0, s46 offen offset:2048 sc1\n\t"
    "buffer_load_dwordx4 v[100:103], v8, %0, s46 offen offset:3072 sc1\n\t"
    "buffer_load_dwordx4 v[104:107], v9, %0, s46 offen offset:0 sc1\n\t"
    "s_waitcnt vmcnt(0)\n\t"
    "v_max3_u32 v227, v88, v92, v96\n\t"
    "v_max3_u32 v227, v227, v100, v104\n\t"
    "v_max3_u32 v227, v227, v90, v94\n\t"
    "v_max3_u32 v227, v227, v98, v102\n\t"
    "v_max_u32 v227, v227, v106\n\t"
    "v_cmp_eq_u32 vcc, -1, v227\n\t"
    "s_nop 1\n\t"
    "s_cbranch_vccz R14_%=\n\t"
    "s_cmp_eq_u32 s45, 0\n\t"
    "s_cbranch_scc1 R14_%=\n\t"
    "s_sub_u32 s45, s45, 1\n\t"
    "s_branch H13_%=\n\t"
    "H15_%=:\n\t"
    "s_sleep 2\n\t"
    "s_add_u32 s46, %8, 0x16d2800\n\t"
    "buffer_load_dwordx4 v[108:111], v8, %0, s46 offen offset:0 sc1\n\t"
    "buffer_load_dwordx4 v[112:115], v8, %0, s46 offen offset:1024 sc1\n\t"
    "buffer_load_dwordx4 v[116:119], v8, %0, s46 offen offset:2048 sc1\n\t"
    "buffer_load_dwordx4 v[120:123], v8, %0, s46 offen offset:3072 sc1\n\t"
    "buffer_load_dwordx4 v[124:127], v9, %0, s46 offen offset:0 sc1\n\t"
    "s_waitcnt vmcnt(0)\n\t"
    "v_max3_u32 v227, v108, v112, v116\n\t"
    "v_max3_u32 v227, v227, v120, v124\n\t"
    "v_max3_u32 v227, v227, v110, v114\n\t"
    "v_max3_u32 v227, v227, v118, v122\n\t"
    "v_max_u32 v227, v227, v126\n\t"
    "v_cmp_eq_u32 vcc, -1, v227\n\t"
    "s_nop 1\n\t"
    "s_cbranch_vccz R16_%=\n\t"
    "s_cmp_eq_u32 s45, 0\n\t"
    "s_cbranch_scc1 R16_%=\n\t"
    "s_sub_u32 s45, s45, 1\n\t"
    "s_branch H15_%=\n\t"
    "H17_%=:\n\t"
    "s_sleep 2\n\t"
    "s_add_u32 s46, %8, 0x11c3800\n\t"
    "buffer_load_dwordx4 v[48:51], v8, %0, s46 offen offset:0 sc1\n\t"
    "buffer_load_dwordx4 v[52:55], v8, %0, s46 offen offset:1024 sc1\n\t"
    "buffer_load_dwordx4 v[56:59], v8, %0, s46 offen offset:2048 sc1\n\t"
    "buffer_load_dwordx4 v[60:63], v8, %0, s46 offen offset:3072 sc1\n\t"
    "buffer_load_dwordx4 v[64:67], v9, %0, s46 offen offset:0 sc1\n\t"
    "s_waitcnt vmcnt(0)\n\t"
    "v_max3_u32 v227, v48, v52, v56\n\t"
    "v_max3_u32 v227, v227, v60, v64\n\t"
    "v_max3_u32 v227, v227, v50, v54\n\t"
    "v_max3_u32 v227, v227, v58, v62\n\t"
    "v_max_u32 v227, v227, v66\n\t"
    "v_cmp_eq_u32 vcc, -1, v227\n\t"
    "s_nop 1\n\t"
    "s_cbranch_vccz R18_%=\n\t"
    "s_cmp_eq_u32 s45, 0\n\t"
    "s_cbranch_scc1 R18_%=\n\t"
    "s_sub_u32 s45, s45, 1\n\t"
    "s_branch H17_%=\n\t"
    "H19_%=:\n\t"
    "s_sleep 2\n\t"
    "s_add_u32 s46, %8, 0x11c8800\n\t"
    "buffer_load_dwordx4 v[68:71], v8, %0, s46 offen offset:0 sc1\n\t"
    "buffer_load_dwordx4 v[72:75], v8, %0, s46 offen offset:1024 sc1\n\t"
    "buffer_load_dwordx4 v[76:79], v8, %0, s46 offen offset:2048 sc1\n\t"
    "buffer_load_dwordx4 v[80:83], v8, %0, s46 offen offset:3072 sc1\n\t"
    "buffer_load_dwordx4 v[84:87], v9, %0, s46 offen offset:0 sc1\n\t"
    "s_waitcnt vmcnt(0)\n\t"
    "v_max3_u32 v227, v68, v72, v76\n\t"
    "v_max3_u32 v227, v227, v80, v84\n\t"
    "v_max3_u32 v227, v227, v70, v74\n\t"
    "v_max3_u32 v227, v227, v78, v82\n\t"
    "v_max_u32 v227, v227, v86\n\t"
    "v_cmp_eq_u32 vcc, -1, v227\n\t"
    "s_nop 1\n\t"
    "s_cbranch_vccz R20_%=\n\t"
    "s_cmp_eq_u32 s45, 0\n\t"
    "s_cbranch_scc1 R20_%=\n\t"
    "s_sub_u32 s45, s45, 1\n\t"
    "s_branch H19_%=\n\t"
    "H21_%=:\n\t"
    "s_sleep 2\n\t"
    "s_add_u32 s46, %8, 0x11be800\n\t"
    "buffer_load_dwordx4 v[88:91], v8, %0, s46 offen offset:0 sc1\n\t"
    "buffer_load_dwordx4 v[92:95], v8, %0, s46 offen offset:1024 sc1\n\t"
    "buffer_load_dwordx4 v[96:99], v8, %0, s46 offen offset:2048 sc1\n\t"
    "buffer_load_dwordx4 v[100:103], v8, %0, s46 offen offset:3072 sc1\n\t"
    "buffer_load_dwordx4 v[104:107], v9, %0, s46 offen offset:0 sc1\n\t"
    "s_waitcnt vmcnt(0)\n\t"
    "v_max3_u32 v227, v88, v92, v96\n\t"
    "v_max3_u32 v227, v227, v100, v104\n\t"
    "v_max3_u32 v227, v227, v90, v94\n\t"
    "v_max3_u32 v227, v227, v98, v102\n\t"
    "v_max_u32 v227, v227, v106\n\t"
    "v_cmp_eq_u32 vcc, -1, v227\n\t"
    "s_nop 1\n\t"
    "s_cbranch_vccz R22_%=\n\t"
    "s_cmp_eq_u32 s45, 0\n\t"
    "s_cbranch_scc1 R22_%=\n\t"
    "s_sub_u32 s45, s45, 1\n\t"
    "s_branch H21_%=\n\t"
    "H23_%=:\n\t"
    "s_sleep 2\n\t"
    "s_add_u32 s46, %8, 0x11cd800\n\t"
    "buffer_load_dwordx4 v[108:111], v8, %0, s46 offen offset:0 sc1\n\t"
    "buffer_load_dwordx4 v[112:115], v8, %0, s46 offen offset:1024 sc1\n\t"
    "buffer_load_dwordx4 v[116:119], v8, %0, s46 offen offset:2048 sc1\n\t"
    "buffer_load_dwordx4 v[120:123], v8, %0, s46 offen offset:3072 sc1\n\t"
    "buffer_load_dwordx4 v[124:127], v9, %0, s46 offen offset:0 sc1\n\t"
    "s_waitcnt vmcnt(0)\n\t"
    "v_max3_u32 v227, v108, v112, v116\n\t"
    "v_max3_u32 v227, v227, v120, v124\n\t"
    "v_max3_u32 v227, v227, v110, v114\n\t"
    "v_max3_u32 v227, v227, v118, v122\n\t"
    "v_max_u32 v227, v227, v126\n\t"
    "v_cmp_eq_u32 vcc, -1, v227\n\t"
    "s_nop 1\n\t"
    "s_cbranch_vccz R24_%=\n\t"
    "s_cmp_eq_u32 s45, 0\n\t"
    "s_cbranch_scc1 R24_%=\n\t"
    "s_sub_u32 s45, s45, 1\n\t"
    "s_branch H23_%=\n\t"
    "H25_%=:\n\t"
    "s_sleep 2\n\t"
    "s_add_u32 s46, %8, 0x1948800\n\t"
    "buffer_load_dwordx4 v[48:51], v8, %0, s46 offen offset:0 sc1\n\t"
    "buffer_load_dwordx4 v[52:55], v8, %0, s46 offen offset:1024 sc1\n\t"
    "buffer_load_dwordx4 v[56:59], v8, %0, s46 offen offset:2048 sc1\n\t"
    "buffer_load_dwordx4 v[60:63], v8, %0, s46 offen offset:3072 sc1\n\t"
    "buffer_load_dwordx4 v[64:67], v9, %0, s46 offen offset:0 sc1\n\t"
    "s_waitcnt vmcnt(0)\n\t"
    "v_max3_u32 v227, v48, v52, v56\n\t"
    "v_max3_u32 v227, v227, v60, v64\n\t"
    "v_max3_u32 v227, v227, v50, v54\n\t"
    "v_max3_u32 v227, v227, v58, v62\n\t"
    "v_max_u32 v227, v227, v66\n\t"
    "v_cmp_eq_u32 vcc, -1, v227\n\t"
    "s_nop 1\n\t"
    "s_cbranch_vccz R26_%=\n\t"
    "s_cmp_eq_u32 s45, 0\n\t"
    "s_cbranch_scc1 R26_%=\n\t"
    "s_sub_u32 s45, s45, 1\n\t"
    "s_branch H25_%=\n\t"
    "H27_%=:\n\t"
    "s_sleep 2\n\t"
    "s_add_u32 s46, %8, 0x194d800\n\t"
    "buffer_load_dwordx4 v[68:71], v8, %0, s46 offen offset:0 sc1\n\t"
    "buffer_load_dwordx4 v[72:75], v8, %0, s46 offen offset:1024 sc1\n\t"
    "buffer_load_dwordx4 v[76:79], v8, %0, s46 offen offset:2048 sc1\n\t"
    "buffer_load_dwordx4 v[80:83], v8, %0, s46 offen offset:3072 sc1\n\t"
    "buffer_load_dwordx4 v[84:87], v9, %0, s46 offen offset:0 sc1\n\t"
    "s_waitcnt vmcnt(0)\n\t"
    "v_max3_u32 v227, v68, v72, v76\n\t"
    "v_max3_u32 v227, v227, v80, v84\n\t"
    "v_max3_u32 v227, v227, v70, v74\n\t"
    "v_max3_u32 v227, v227, v78, v82\n\t"
    "v_max_u32 v227, v227, v86\n\t"
    "v_cmp_eq_u32 vcc, -1, v227\n\t"
    "s_nop 1\n\t"
    "s_cbranch_vccz R28_%=\n\t"
    "s_cmp_eq_u32 s45, 0\n\t"
    "s_cbranch_scc1 R28_%=\n\t"
    "s_sub_u32 s45, s45, 1\n\t"
    "s_branch H27_%=\n\t"
    "H29_%=:\n\t"
    "s_sleep 2\n\t"
    "s_add_u32 s46, %8, 0x1943800\n\t"
    "buffer_load_dwordx4 v[88:91], v8, %0, s46 offen offset:0 sc1\n\t"
    "buffer_load_dwordx4 v[92:95], v8, %0, s46 offen offset:1024 sc1\n\t"
    "buffer_load_dwordx4 v[96:99], v8, %0, s46 offen offset:2048 sc1\n\t"
    "buffer_load_dwordx4 v[100:103], v8, %0, s46 offen offset:3072 sc1\n\t"
    "buffer_load_dwordx4 v[104:107], v9, %0, s46 offen offset:0 sc1\n\t"
    "s_waitcnt vmcnt(0)\n\t"
    "v_max3_u32 v227, v88, v92, v96\n\t"
    "v_max3_u32 v227, v227, v100, v104\n\t"
    "v_max3_u32 v227, v227, v90, v94\n\t"
    "v_max3_u32 v227, v227, v98, v102\n\t"
    "v_max_u32 v227, v227, v106\n\t"
    "v_cmp_eq_u32 vcc, -1, v227\n\t"
    "s_nop 1\n\t"
    "s_cbranch_vccz R30_%=\n\t"
    "s_cmp_eq_u32 s45, 0\n\t"
    "s_cbranch_scc1 R30_%=\n\t"
    "s_sub_u32 s45, s45, 1\n\t"
    "s_branch H29_%=\n\t"
    "H31_%=:\n\t"
    "s_sleep 2\n\t"
    "s_add_u32 s46, %8, 0x1952800\n\t"
    "buffer_load_dwordx4 v[108:111], v8, %0, s46 offen offset:0 sc1\n\t"
    "buffer_load_dwordx4 v[112:115], v8, %0, s46 offen offset:1024 sc1\n\t"
    "buffer_load_dwordx4 v[116:119], v8, %0, s46 offen offset:2048 sc1\n\t"
    "buffer_load_dwordx4 v[120:123], v8, %0, s46 offen offset:3072 sc1\n\t"
    "buffer_load_dwordx4 v[124:127], v9, %0, s46 offen offset:0 sc1\n\t"
    "s_waitcnt vmcnt(0)\n\t"
    "v_max3_u32 v227, v108, v112, v116\n\t"
    "v_max3_u32 v227, v227, v120, v124\n\t"
    "v_max3_u32 v227, v227, v110, v114\n\t"
    "v_max3_u32 v227, v227, v118, v122\n\t"
    "v_max_u32 v227, v227, v126\n\t"
    "v_cmp_eq_u32 vcc, -1, v227\n\t"
    "s_nop 1\n\t"
    "s_cbranch_vccz R32_%=\n\t"
    "s_cmp_eq_u32 s45, 0\n\t"
    "s_cbranch_scc1 R32_%=\n\t"
    "s_sub_u32 s45, s45, 1\n\t"
    "s_branch H31_%=\n\t"
    "H33_%=:\n\t"
    "s_sleep 2\n\t"
    "buffer_load_dword v13, v35, %0, s47 offen sc1\n\t"
    "s_waitcnt vmcnt(0)\n\t"
    "v_cmp_eq_u32 vcc, -1, v13\n\t"
    "s_cbranch_vccz R34_%=\n\t"
    "s_cmp_eq_u32 s45, 0\n\t"
    "s_cbranch_scc1 R34_%=\n\t"
    "s_sub_u32 s45, s45, 1\n\t"
    "s_branch H33_%=\n\t"
    "H35_%=:\n\t"
    "s_sleep 2\n\t"
    "buffer_load_dword v13, v35, %0, s47 offen sc1\n\t"
    "s_waitcnt vmcnt(0)\n\t"
    "v_cmp_eq_u32 vcc, -1, v13\n\t"
    "s_cbranch_vccz R36_%=\n\t"
    "s_cmp_eq_u32 s45, 0\n\t"
    "s_cbranch_scc1 R36_%=\n\t"
    "s_sub_u32 s45, s45, 1\n\t"
    "s_branch H35_%=\n\t"
    "H37_%=:\n\t"
    "s_sleep 2\n\t"
    "buffer_load_dword v13, v35, %0, s47 offen sc1\n\t"
    "s_waitcnt vmcnt(0)\n\t"
    "v_cmp_eq_u32 vcc, -1, v13\n\t"
    "s_cbranch_vccz R38_%=\n\t"
    "s_cmp_eq_u32 s45, 0\n\t"
    "s_cbranch_scc1 R38_%=\n\t"
    "s_sub_u32 s45, s45, 1\n\t"
    "s_branch H37_%=\n\t"
    "KEND_%=:\n\t"
    ".Lk2_code_end:\n\t"
    :: "s"(rsws), "s"(rsx), "s"(rso), "v"(opq), "v"(vtab), "v"(lane16), "v"(vocs), "s"(sW), "s"(sB20), "s"(sB56), "s"(sB56q), "s"(sP20), "s"(sP56), "s"(sFL),
       "s"(sOBx), "s"(sOBy)
    : "v8", "v9", "v10", "v11", "v12", "v13", "v14", "v15", "v16", "v17", "v18", "v19", "v20", "v21", "v22", "v23", "v24", "v25", "v26", "v27", "v28", "v29", "v30", "v31", "v32", "v33", "v34", "v35", "v36", "v37", "v38", "v39", "v40", "v41", "v42", "v43", "v44", "v45", "v46", "v47", "v48", "v49", "v50", "v51", "v52", "v53", "v54", "v55", "v56", "v57", "v58", "v59", "v60", "v61", "v62", "v63", "v64", "v65", "v66", "v67", "v68", "v69", "v70", "v71", "v72", "v73", "v74", "v75", "v76", "v77", "v78", "v79", "v80", "v81", "v82", "v83", "v84", "v85", "v86", "v87", "v88", "v89", "v90", "v91", "v92", "v93", "v94", "v95", "v96", "v97", "v98", "v99", "v100", "v101", "v102", "v103", "v104", "v105", "v106", "v107", "v108", "v109", "v110", "v111", "v112", "v113", "v114", "v115", "v116", "v117", "v118", "v119", "v120", "v121", "v122", "v123", "v124", "v125", "v126", "v127", "v128", "v129", "v130", "v131", "v132", "v133", "v134", "v135", "v136", "v137", "v138", "v139", "v140", "v141", "v142", "v143", "v144", "v145", "v146", "v147", "v148", "v149", "v150", "v151", "v152", "v153", "v154", "v155", "v156", "v157", "v158", "v159", "v160", "v161", "v162", "v163", "v164", "v165", "v166", "v167", "v168", "v169", "v170", "v171", "v172", "v173", "v174", "v175", "v176", "v177", "v178", "v179", "v180", "v181", "v182", "v183", "v184", "v185", "v186", "v187", "v188", "v189", "v190", "v191", "v192", "v193", "v194", "v195", "v196", "v197", "v198", "v199", "v200", "v201", "v202", "v203", "v204", "v205", "v206", "v207", "v208", "v209", "v210", "v211", "v212", "v213", "v214", "v215", "v216", "v217", "v218", "v219", "v220", "v221", "v222", "v223", "v224", "v225", "v226", "v227", "v228", "v229", "v230", "v231", "v232", "v233", "v234", "v235", "v236", "v237", "v238", "v239", "v240", "v241", "v242", "v243", "v244", "v245", "v246", "v247", "v248", "v249", "v250", "v251", "v252", "v253", "v254", "v255", "a0", "a1", "a2", "a3", "a4", "a5", "a6", "a7", "a8", "a9", "a10", "a11", "a12", "a13", "a14", "a15", "a16", "a17", "a18", "a19", "a20", "a21", "a22", "a23", "a24", "a25", "a26", "a27", "a28", "a29", "a30", "a31", "a32", "a33", "a34", "a35", "a36", "a37", "a38", "a39", "a40", "a41", "a42", "a43", "a44", "a45", "a46", "a47", "a48", "a49", "a50", "a51", "a52", "a53", "a54", "a55", "a56", "a57", "a58", "a59", "a60", "a61", "a62", "a63", "a64", "a65", "a66", "a67", "a68", "a69", "a70", "a71", "a72", "a73", "a74", "a75", "a76", "a77", "a78", "a79", "a80", "a81", "a82", "a83", "a84", "a85", "a86", "a87", "a88", "a89", "a90", "a91", "a92", "a93", "a94", "a95", "a96", "a97", "a98", "a99", "a100", "a101", "a102", "a103", "a104", "a105", "a106", "a107", "a108", "a109", "a110", "a111", "a112", "a113", "a114", "a115", "a116", "a117", "a118", "a119", "a120", "a121", "a122", "a123", "a124", "a125", "a126", "a127", "a128", "a129", "a130", "a131", "a132", "a133", "a134", "a135", "a136", "a137", "a138", "a139", "a140", "a141", "a142", "a143", "a144", "a145", "a146", "a147", "a148", "a149", "a150", "a151", "a152", "a153", "a154", "a155", "a156", "a157", "a158", "a159", "a160", "a161", "a162", "a163", "a164", "a165", "a166", "a167", "a168", "a169", "a170", "a171", "a172", "a173", "a174", "a175", "a176", "a177", "a178", "a179", "a180", "a181", "a182", "a183", "a184", "a185", "a186", "a187", "a188", "a189", "a190", "a191", "a192", "a193", "a194", "a195", "a196", "a197", "a198", "a199", "a200", "a201", "a202", "a203", "a204", "a205", "a206", "a207", "a208", "a209", "a210", "a211", "a212", "a213", "a214", "a215", "a216", "a217", "a218", "a219", "a220", "a221", "a222", "a223", "a224", "a225", "a226", "a227", "a228", "a229", "a230", "a231", "a232", "a233", "a234", "a235", "a236", "a237", "a238", "a239", "a240", "a241", "a242", "a243", "a244", "a245", "a246", "a247", "a248", "a249", "a250", "a251", "a252", "a253", "a254", "a255", "s40", "s41", "s42", "s43", "s44", "s45", "s46", "s47", "s48", "s49", "s50", "s51", "s52", "s53", "s54", "s55", "s56", "s57", "s58", "s59", "s60", "s61", "s62", "s63", "s64", "s65", "s66", "s67", "s68", "s69", "s70", "s71", "s72", "s73", "s74", "s75", "s76", "s77", "s78", "s79", "s80", "s81", "s82", "s83", "s84", "s85", "s86", "s87", "s88", "s89", "s90", "s91", "s92", "s93", "s94", "s95", "s96", "s97", "s98", "s99", "s100", "s101", "vcc", "scc", "memory"
  );
}

extern "C" void kernel_launch(void* const* d_in, const int* in_sizes, int n_in, void* d_out, int out_size, void* d_ws, size_t ws_size, hipStream_t stream) {
  (void)hipFuncSetAttribute((const void*)k1_qkv, hipFuncAttributeMaxDynamicSharedMemorySize, K1_LDS);
  (void)hipFuncSetAttribute((const void*)k2_main, hipFuncAttributeMaxDynamicSharedMemorySize, K2_LDS);
  k1_qkv<<<256, K1_THREADS, K1_LDS, stream>>>((const u16*)d_in[0], (const u16*)d_in[1], (const u16*)d_in[2], (const u16*)d_in[4], (const u16*)d_in[5],
                                       (const u16*)d_in[6], (const u16*)d_in[7], (char*)d_ws);
  k2_main<<<256, K2_THREADS, K2_LDS, stream>>>((const u16*)d_in[0], (u16*)d_out, (char*)d_ws);
}
```
